```python
import torch

def _softmax(x, axis=-1):
    return torch.softmax(x, dim=axis)
def _gelu(x):
    return torch.nn.functional.gelu(x, approximate="tanh")
def _silu(x):
    return torch.nn.functional.silu(x)
def _sigmoid(x):
    return torch.sigmoid(x)
def _layernorm(x, axis=-1, eps=1e-5):
    m = x.mean(dim=axis, keepdim=True)
    v = x.var(dim=axis, keepdim=True, unbiased=False)
    return (x - m) / torch.sqrt(v + eps)
def _scatter_add(tgt, idx, src, axis=0):
    return tgt.clone().index_add_(axis, idx, src)
def _segment_sum(src, seg, n):
    out = torch.zeros((n,) + src.shape[1:], dtype=src.dtype)
    return out.index_add_(0, seg, src)
def _pad(x, pw):
    flat = tuple(v for pair in reversed(pw) for v in pair)
    return torch.nn.functional.pad(x, flat)

kernel_name = 'parallel_sum__ssm_conv_s4__dense_activation_gelu__prompt'

def setup_inputs():

    _w = (lambda a: (lambda t: t.to(torch.bfloat16) if t.is_floating_point() else t)(torch.as_tensor(a)))
    def _A_setup():
        B, L, D, N = 1, 2048, 512, 128
        A_re = -(lambda _x: torch.exp(torch.as_tensor(_x)))((lambda *s: torch.randn(*s, dtype=torch.bfloat16) * 12.8)(D, N))
        A_im = torch.pi * (lambda *s: torch.randn(*s, dtype=torch.bfloat16) * 0.05)(D, N)
        B_re = (lambda *s: torch.randn(*s, dtype=torch.bfloat16) * 0.1)(D, N)
        B_im = (lambda *s: torch.randn(*s, dtype=torch.bfloat16) * 0.1)(D, N)
        C_re = (lambda *s: torch.randn(*s, dtype=torch.bfloat16) * 0.2)(D, N)
        C_im = (lambda *s: torch.randn(*s, dtype=torch.bfloat16) * 0.2)(D, N)
        log_dt = (lambda *s: torch.randn(*s, dtype=torch.bfloat16) * 0.05)(D) * 0.5 - 4.0
        Dskip = (lambda *s: torch.randn(*s, dtype=torch.bfloat16) * 0.05)(D)
        return {
            "u": _w((lambda *s: torch.randn(*s, dtype=torch.bfloat16) * 0.05)(B, L, D)),
            "A_re": _w(A_re),
            "A_im": _w(A_im),
            "B_re": _w(B_re),
            "B_im": _w(B_im),
            "C_re": _w(C_re),
            "C_im": _w(C_im),
            "log_dt": _w(log_dt),
            "Dskip": _w(Dskip),
        }
    _A_in = _A_setup()

    def _B_setup():
        B, S, D, F = 1, 2048, 512, 1792
        return {
            "x": _w((lambda *s: torch.randn(*s, dtype=torch.bfloat16) * 0.05)(B, S, D)),
            "w1": _w((lambda *s: torch.randn(*s, dtype=torch.bfloat16) * 0.8)(D, F) * (D ** -0.5)),
            "b1": _w((lambda *s: torch.randn(*s, dtype=torch.bfloat16) * 12.8)(F) * 0.01),
            "w2": _w((lambda *s: torch.randn(*s, dtype=torch.bfloat16) * 0.8)(F, D) * (F ** -0.5)),
            "b2": _w((lambda *s: torch.randn(*s, dtype=torch.bfloat16) * 12.8)(D) * 0.01),
        }
    _B_in = _B_setup()

    return {
        "A_u": _A_in["u"],
        "A_A_re": _A_in["A_re"],
        "A_A_im": _A_in["A_im"],
        "A_B_re": _A_in["B_re"],
        "A_B_im": _A_in["B_im"],
        "A_C_re": _A_in["C_re"],
        "A_C_im": _A_in["C_im"],
        "A_log_dt": _A_in["log_dt"],
        "A_Dskip": _A_in["Dskip"],
        "B_x": _B_in["x"],
        "B_w1": _B_in["w1"],
        "B_b1": _B_in["b1"],
        "B_w2": _B_in["w2"],
        "B_b2": _B_in["b2"],
    }

def reference(A_u, A_A_re, A_A_im, A_B_re, A_B_im, A_C_re, A_C_im, A_log_dt, A_Dskip, B_x, B_w1, B_b1, B_w2, B_b2):
    def _A_ref(u, A_re, A_im, B_re, B_im, C_re, C_im, log_dt, Dskip):
        Bsz, L, D = u.shape
        dt = torch.exp(log_dt)[:, None]
        A = A_re + 1j * A_im
        Bc = B_re + 1j * B_im
        Cc = C_re + 1j * C_im
        logdA = dt * A
        dB = dt * Bc
        l = (lambda x, dt: x.to(dt))(torch.arange(L), A_re.dtype)
        P = torch.exp(logdA[:, :, None] * l[None, None, :])
        K = torch.einsum("dn,dnl,dn->dl", Cc, P, dB).real
        L2 = 2 * L
        Kp = _pad(K, ((0, 0), (0, L)))
        up = (lambda x, *d: x.permute(*d))(u, 0, 2, 1)
        up = _pad(up, ((0, 0), (0, 0), (0, L)))
        Kf = (lambda x, axes=(-2,-1): torch.fft.rfft2(x.float(), dim=axes))(Kp[:, None, :], axes=(-2, -1))
        Uf = (lambda x, axes=(-2,-1): torch.fft.rfft2(x.float(), dim=axes))(up[:, :, None, :], axes=(-2, -1))
        Yf = Uf * Kf[None, :, :, :]
        y = (lambda x, s, axes=(-2,-1): torch.fft.irfft2(x, s=s, dim=axes).to(torch.bfloat16))(Yf, (1, L2), axes=(-2, -1))[:, :, 0, :L]
        y = (lambda x, *d: x.permute(*d))(y, 0, 2, 1) + u * Dskip[None, None, :]
        return y

    def _B_ref(x, w1, b1, w2, b2):
        h = torch.einsum("bsd,df->bsf", x, w1) + b1
        h = _gelu(h)
        return torch.einsum("bsf,fd->bsd", h, w2) + b2

    x = A_u
    xn = _layernorm(x, axis=-1)
    return x + _A_ref(xn, A_A_re, A_A_im, A_B_re, A_B_im, A_C_re, A_C_im, A_log_dt, A_Dskip) + _B_ref(xn, B_w1, B_b1, B_w2, B_b2)
```

```cpp
#include <hip/hip_runtime.h>
#include <hip/hip_bf16.h>
#include <stdint.h>

typedef unsigned short u16;
__device__ __forceinline__ float bf2f(u16 v){ return __uint_as_float(((unsigned)v)<<16); }
__device__ __forceinline__ u16 f2bf(float f){ __hip_bfloat16 b = __float2bfloat16(f); return *(u16*)&b; }
__device__ __forceinline__ float rbf(float f){ return bf2f(f2bf(f)); }

constexpr int L=2048, D=512, N=128, F=1792;

__global__ void ln_k(const u16* x, u16* xn){
  int row = blockIdx.x; int t = threadIdx.x;
  float v[8]; float s=0;
  for(int i=0;i<8;i++){ v[i]=bf2f(x[row*D+t*8+i]); s+=v[i]; }
  for(int o=32;o>0;o>>=1) s+=__shfl_xor(s,o);
  float m = s/D; float q=0;
  for(int i=0;i<8;i++){ float d=v[i]-m; q+=d*d; }
  for(int o=32;o>0;o>>=1) q+=__shfl_xor(q,o);
  float var = rbf(q/D); float mb = rbf(m);
  float sd = rbf(sqrtf(rbf(var+1e-5f)));
  for(int i=0;i<8;i++){ xn[row*D+t*8+i]=f2bf(rbf(v[i]-mb)/sd); }
}

__global__ void kgen_k(const u16* Are,const u16* Aim,const u16* Bre,const u16* Bim,const u16* Cre,const u16* Cim,const u16* ldt,float* K){
  int d = blockIdx.y; int l = blockIdx.x*256+threadIdx.x;
  float dt = rbf(expf(bf2f(ldt[d])));
  float lf = rbf((float)l);
  float acc=0;
  for(int n=0;n<N;n++){
    float ar=dt*bf2f(Are[d*N+n]), ai=dt*bf2f(Aim[d*N+n]);
    float br=dt*bf2f(Bre[d*N+n]), bi=dt*bf2f(Bim[d*N+n]);
    float cr=bf2f(Cre[d*N+n]), ci=bf2f(Cim[d*N+n]);
    float e=expf(ar*lf); float s,c; sincosf(ai*lf,&s,&c);
    float pr=e*c, pi=e*s;
    float tr=cr*pr-ci*pi, ti=cr*pi+ci*pr;
    acc += tr*br-ti*bi;
  }
  K[d*L+l]=acc;
}

__global__ void conv_k(const u16* xn,const float* K,float* y){
  int d = blockIdx.y; int l = blockIdx.x*256+threadIdx.x;
  __shared__ float su[L]; __shared__ float sk[L];
  for(int i=threadIdx.x;i<L;i+=256){ su[i]=bf2f(xn[i*D+d]); sk[i]=K[d*L+i]; }
  __syncthreads();
  float acc=0;
  for(int j=0;j<=l;j++) acc+=sk[j]*su[l-j];
  y[l*D+d]=acc;
}

__global__ void g1_k(const u16* xn,const u16* w1,const u16* b1,u16* h){
  int row=blockIdx.y; int f=blockIdx.x*256+threadIdx.x;
  __shared__ float sx[D];
  for(int i=threadIdx.x;i<D;i+=256) sx[i]=bf2f(xn[row*D+i]);
  __syncthreads();
  float acc=0;
  for(int k=0;k<D;k++) acc+=sx[k]*bf2f(w1[k*F+f]);
  float v=rbf(rbf(acc)+bf2f(b1[f]));
  float z=0.7978845608028654f*(v+0.044715f*v*v*v);
  h[row*F+f]=f2bf(0.5f*v*(1.f+tanhf(z)));
}

__global__ void g2_k(const u16* x,const u16* xn,const u16* h,const u16* w2,const u16* b2,const float* y,const u16* Dsk,u16* out){
  int row=blockIdx.y; int c=blockIdx.x*256+threadIdx.x;
  __shared__ float sh[F];
  for(int i=threadIdx.x;i<F;i+=256) sh[i]=bf2f(h[row*F+i]);
  __syncthreads();
  float acc=0;
  for(int k=0;k<F;k++) acc+=sh[k]*bf2f(w2[k*D+c]);
  float mlp=rbf(rbf(acc)+bf2f(b2[c]));
  float a=rbf(rbf(y[row*D+c])+rbf(bf2f(xn[row*D+c])*bf2f(Dsk[c])));
  float o=rbf(bf2f(x[row*D+c])+a);
  out[row*D+c]=f2bf(o+mlp);
}

extern "C" void kernel_launch(void* const* d_in, const int* in_sizes, int n_in,
                              void* d_out, int out_size, void* d_ws, size_t ws_size,
                              hipStream_t stream) {
  const u16* u=(const u16*)d_in[0];
  char* ws=(char*)d_ws;
  u16* xn=(u16*)ws; float* K=(float*)(ws+(4<<20)); float* y=(float*)(ws+(12<<20)); u16* h=(u16*)(ws+(20<<20));
  ln_k<<<L,64,0,stream>>>(u,xn);
  kgen_k<<<dim3(L/256,D),256,0,stream>>>((const u16*)d_in[1],(const u16*)d_in[2],(const u16*)d_in[3],(const u16*)d_in[4],(const u16*)d_in[5],(const u16*)d_in[6],(const u16*)d_in[7],K);
  conv_k<<<dim3(L/256,D),256,0,stream>>>(xn,K,y);
  g1_k<<<dim3(F/256,L),256,0,stream>>>(xn,(const u16*)d_in[10],(const u16*)d_in[11],h);
  g2_k<<<dim3(D/256,L),256,0,stream>>>(u,xn,h,(const u16*)d_in[12],(const u16*)d_in[13],y,(const u16*)d_in[8],(u16*)d_out);
}
```

```cpp
#include <hip/hip_runtime.h>
#include <stdint.h>

typedef unsigned u32; typedef unsigned long long u64; typedef unsigned short u16;
typedef u32 v4u __attribute__((ext_vector_type(4)));
typedef u32 v2u __attribute__((ext_vector_type(2)));
typedef float f4 __attribute__((ext_vector_type(4)));
typedef float f2 __attribute__((ext_vector_type(2)));
typedef short s4 __attribute__((ext_vector_type(4)));
typedef __bf16 bf8 __attribute__((ext_vector_type(8)));
typedef __bf16 bf2 __attribute__((ext_vector_type(2)));
#define LAS __attribute__((address_space(3)))
#define DI __device__ __forceinline__

constexpr u32 WS_E = 0, WS_UF = 4096, WS_HF = WS_UF + 16384, WS_YF = WS_HF + 16384;
constexpr u32 WS_ST = 81920;
constexpr u32 WS_U = 1u << 18, WS_Y = WS_U + (2u << 20), WS_H = WS_Y + (2u << 20);
constexpr u32 SP = 131072, PARTS = 3328, CS_ = 1344, TB_Q2 = 5376 + 24, TB_Q3 = 5376 + 1088 + 24, TBS = 7552;
constexpr u32 LU = SP + 2 * TBS, LUS = 4608, LB1 = SP + 26624, LCS = LB1 + 448, LDUMMY = LCS + 448;
constexpr u32 LDS_BYTES = 163840;

constexpr int rnd_bf(int j) { if (j <= 256) return j; int sh = j < 512 ? 1 : j < 1024 ? 2 : 3; int q = j >> sh, rem = j & ((1 << sh) - 1), h = 1 << (sh - 1);
  if (rem > h || (rem == h && (q & 1))) q++; return q << sh; }
constexpr int src_of(int p) { return p < 512 ? (p & 7) * 68 + (p >> 3) : p < 1024 ? (8 + ((p >> 2) & 1)) * 68 + ((p - 512) >> 3)
  : p < 1536 ? 10 * 68 + ((p - 1024) >> 3) : p < 2048 ? 11 * 68 + ((p - 1536) >> 3) : 12 * 68; }
struct Tabs { u32 et[1024]; u32 lt[64 * 12]; };
constexpr u32 enc(int src, int dst, int quad, int keep, int dsk) { return (u32)src | ((u32)dst << 10) | ((u32)quad << 24) | ((u32)keep << 25) | ((u32)dsk << 26); }
constexpr Tabs make_tabs() {
  Tabs t{};
  u32 lin[1024] = {};
  int n = 0;
  for (int x = 0; x <= 558; x++) { int j = 543 - x; lin[n++] = j >= 0 ? enc(src_of(rnd_bf(j)), 2 * x, 0, 1, j == 0) : enc(0, 2 * x, 0, 0, 0); }
  for (int x = 0; x <= 46; x++) lin[n++] = enc(src_of(rnd_bf(1055 - x)), 2 * (560 + x), 0, 1, 0);
  for (int m = 0; m <= 128; m++) lin[n++] = enc(src_of(512 + 4 * m), TB_Q2 + 8 * m, 1, 1, 0);
  for (int m = 0; m <= 128; m++) lin[n++] = enc(src_of(1024 + 8 * m), TB_Q3 + 8 * m, 1, 1, 0);
  while (n < 1024) lin[n++] = enc(0, 2 * 640, 0, 0, 0);
  for (int th = 0; th < 256; th++) for (int r = 0; r < 4; r++) t.et[th * 4 + r] = lin[r * 256 + th];
  for (int l = 0; l < 64; l++) {
    int b = l & 15, g = l >> 4;
    int j2 = 32 * 17 + 16 + b - 8 * g, j3 = 32 * 33 + 16 + b - 8 * g;
    int m2 = (rnd_bf(j2 - 7) - 512) / 4, m3 = (rnd_bf(j3 - 7) - 1024) / 8;
    for (int i = 0; i < 4; i++) {
      int a2 = (rnd_bf(j2 - 2 * i) - 512) / 4 - m2, b2 = (rnd_bf(j2 - 2 * i - 1) - 512) / 4 - m2;
      int a3 = (rnd_bf(j3 - 2 * i) - 1024) / 8 - m3, b3 = (rnd_bf(j3 - 2 * i - 1) - 1024) / 8 - m3;
      t.lt[l * 12 + i] = (u32)(2 * a2) | (u32)(2 * a2 + 1) << 8 | (u32)(2 * b2) << 16 | (u32)(2 * b2 + 1) << 24;
      t.lt[l * 12 + 4 + i] = (u32)(2 * a3) | (u32)(2 * a3 + 1) << 8 | (u32)(2 * b3) << 16 | (u32)(2 * b3 + 1) << 24;
    }
    t.lt[l * 12 + 8] = TB_Q2 + 8 * m2 - 64;
    t.lt[l * 12 + 9] = TB_Q3 + 8 * m3 - 32;
    int p = 15 - b + 8 * g, c = p & 3;
    t.lt[l * 12 + 10] = c * CS_ + 2 * (p - c);
    t.lt[l * 12 + 11] = 0;
  }
  return t;
}
__device__ const Tabs g_tabs = make_tabs();

struct Args { const u16 *x, *are, *aim, *bre, *bim, *cre, *cim, *ldt, *dsk, *w1, *b1, *w2, *b2; u16* out; char* ws; };

DI float bf2f(u32 v) { return __uint_as_float(v << 16); }
DI float bfhi(u32 v) { return __uint_as_float(v & 0xffff0000u); }
DI u32 pk2(float a, float b) { bf2 c; c[0] = (__bf16)a; c[1] = (__bf16)b; return __builtin_bit_cast(u32, c); }
DI float rbf(float a) { return bf2f(pk2(a, a) & 0xffffu); }
DI f4 mfma(v4u a, v4u b, f4 c) { return __builtin_amdgcn_mfma_f32_16x16x32_bf16(__builtin_bit_cast(bf8, a), __builtin_bit_cast(bf8, b), c, 0, 0, 0); }
template <class T> DI T lds_ld(u32 a) { return *(const LAS T*)(size_t)a; }
template <class T> DI void lds_st(u32 a, T v) { *(LAS T*)(size_t)a = v; }
DI v2u lds_tr(u32 a) { return __builtin_bit_cast(v2u, __builtin_amdgcn_ds_read_tr16_b64_v4i16((LAS s4*)(size_t)a)); }
DI v4u lds_tr2(u32 a, u32 b) { v2u x = lds_tr(a), y = lds_tr(b); return v4u{x.x, x.y, y.x, y.y}; }
DI f2 cmul(f2 a, f2 w) {
  f2 t, r;
  asm("v_pk_mul_f32 %0, %1, %2 op_sel:[0,0] op_sel_hi:[1,0]" : "=v"(t) : "v"(a), "v"(w));
  asm("v_pk_fma_f32 %0, %1, %2, %3 op_sel:[1,1,0] op_sel_hi:[0,1,1] neg_lo:[1,0,0]" : "=v"(r) : "v"(a), "v"(w), "v"(t));
  return r;
}
DI f2 cmul_conj(f2 a, f2 w) {
  f2 t, r;
  asm("v_pk_mul_f32 %0, %1, %2 op_sel:[0,0] op_sel_hi:[1,0] neg_hi:[1,0]" : "=v"(t) : "v"(a), "v"(w));
  asm("v_pk_fma_f32 %0, %1, %2, %3 op_sel:[1,1,0] op_sel_hi:[0,1,1] neg_lo:[1,0,0] neg_hi:[1,0,0]" : "=v"(r) : "v"(a), "v"(w), "v"(t));
  return r;
}
DI f2 cexp2(f2 x) {
  float e = __builtin_amdgcn_exp2f(x.x), c = __builtin_amdgcn_cosf(x.y), s = __builtin_amdgcn_sinf(x.y);
  return f2{c, s} * e;
}
#define VMW(n) asm volatile("s_waitcnt vmcnt(%0)" ::"n"(n) : "memory")
#define LGW0() asm volatile("s_waitcnt lgkmcnt(0)" ::: "memory")
#define BAR() asm volatile("s_barrier" ::: "memory")
#define TIE(x) asm volatile("" : "+v"(x))
#define GLD(op, dst, voff, sbase, imm, fl) asm volatile(op " %0, %1, %2 offset:%3 " fl : "=v"(dst) : "v"(voff), "s"(sbase), "n"(imm) : "memory")
#define GST(op, voff, src, sbase, imm, fl) asm volatile(op " %0, %1, %2 offset:%3 " fl "\n\ts_nop 1" ::"v"(voff), "v"(src), "s"(sbase), "n"(imm) : "memory")
DI void dma16(u32 voff, const void* sbase, u32 ldst) {
  asm volatile("s_mov_b32 m0, %2\n\ts_nop 0\n\tglobal_load_lds_dwordx4 %0, %1" ::"v"(voff), "s"(sbase), "s"(ldst) : "memory");
}
DI void poll(u32 voff, const char* ws, v2u tag) {
  for (int it = 0; it < (1 << 16); it++) {
    v2u f;
    asm volatile("global_load_dwordx2 %0, %1, %2 sc1\n\ts_waitcnt vmcnt(0)" : "=v"(f) : "v"(voff), "s"(ws) : "memory");
    if (__builtin_amdgcn_ballot_w64(f.x != tag.x || f.y != tag.y) == 0) break;
    __builtin_amdgcn_s_sleep(2);
  }
}
#define STAMP(i)

template <int TH> DI void conv_body(f4 (&acc)[4], u32 ua, u32 ea, u32 q2a, u32 q3a, v4u sel2, v4u sel3, int r0) {
  constexpr int T[4] = {TH ? 1 : 0, TH ? 2 : 3, TH ? 5 : 4, TH ? 6 : 7};
  constexpr int NA = T[3] + 1;
#pragma unroll 1
  for (int rr = 0; rr < 4; rr++) {
    const int r = r0 + rr;
    v4u A[NA];
#pragma unroll
    for (int i = 0; i < NA; i++) A[i] = lds_ld<v4u>(ua - 64 * r + 512 * i);
#pragma unroll
    for (int m = 0; m < NA; m++) {
      v4u B;
      if (m < 2) { v2u a = lds_ld<v2u>(ea - 64 * r + 64 * (16 - 8 * m)), b = lds_ld<v2u>(ea - 64 * r + 64 * (16 - 8 * m) + 8); B = v4u{a.x, a.y, b.x, b.y}; }
      else if ((m == 2 || m == 4) && r == 0) { v2u a = lds_ld<v2u>(ea + (m == 2 ? 0 : 1120)), b = lds_ld<v2u>(ea + (m == 2 ? 0 : 1120) + 8); B = v4u{a.x, a.y, b.x, b.y}; }
      else if (m < 4) { v2u q = lds_ld<v2u>(q2a + 64 * r + 512 * (m - 2));
        B = v4u{__builtin_amdgcn_perm(q.y, q.x, sel2.x), __builtin_amdgcn_perm(q.y, q.x, sel2.y), __builtin_amdgcn_perm(q.y, q.x, sel2.z), __builtin_amdgcn_perm(q.y, q.x, sel2.w)}; }
      else { u32 q = lds_ld<u32>(q3a + 32 * r + 256 * (m - 4));
        B = v4u{__builtin_amdgcn_perm(q, q, sel3.x), __builtin_amdgcn_perm(q, q, sel3.y), __builtin_amdgcn_perm(q, q, sel3.z), __builtin_amdgcn_perm(q, q, sel3.w)}; }
#pragma unroll
      for (int t = 0; t < 4; t++) if (T[t] >= m) acc[t] = mfma(B, A[T[t] - m], acc[t]);
    }
  }
}

__global__ __launch_bounds__(512, 2) void fused_kernel(Args a) {
  extern __shared__ __attribute__((aligned(16))) char smem[];
  STAMP(0);
  const int tid = threadIdx.x, lane = tid & 63, w = __builtin_amdgcn_readfirstlane(tid >> 6), l15 = lane & 15, g = lane >> 4;
  const int bid = blockIdx.x, xcd = bid & 7, idx = bid >> 3;
  const int rt = (xcd >> 1) * 4 + (idx >> 3), ft = (xcd & 1) * 8 + (idx & 7), fo = ft & 1;
  const int mt = xcd * 4 + (idx >> 3), nt = idx & 7;
  const int ch = w >> 2, dch = 2 * bid + ch;
  const char* ws = a.ws;
  const u32 reg = (u32)w * 16384u;

  u32 pf; { u64 pc; u32 len; asm volatile("s_getpc_b64 %0\n.Lfk_beg:\n\ts_mov_b32 %1, .Lfk_end-.Lfk_beg" : "=s"(pc), "=s"(len)); u32 o = min((u32)tid * 64u, len); GLD("global_load_dword", pf, o, pc, 0, ""); }
  v2u ep; u32 pa, pb, pc_, pd, pe, pg, pl, pk, pb1; v4u et;
  {
    u32 z = 0; GLD("global_load_dwordx2", ep, z, ws, 0, "sc1");
    u32 o = (u32)(dch * 128 + (w & 3) * 32 + (lane & 31)) * 2u;
    GLD("global_load_ushort", pa, o, a.are, 0, ""); GLD("global_load_ushort", pb, o, a.aim, 0, "");
    GLD("global_load_ushort", pc_, o, a.bre, 0, ""); GLD("global_load_ushort", pd, o, a.bim, 0, "");
    GLD("global_load_ushort", pe, o, a.cre, 0, ""); GLD("global_load_ushort", pg, o, a.cim, 0, "");
    u32 o2 = (u32)dch * 2u; GLD("global_load_ushort", pl, o2, a.ldt, 0, ""); GLD("global_load_ushort", pk, o2, a.dsk, 0, "");
    int jl = (tid < 112 ? tid : 0) >> 4; int j = fo ? (jl == 6 ? 0 : jl + 1) : jl;
    u32 o3 = (u32)(ft * 112 + j * 16 + l15) * 2u; GLD("global_load_ushort", pb1, o3, a.b1, 0, "");
    u32 o4 = (u32)(tid & 255) * 16u; GLD("global_load_dwordx4", et, o4, g_tabs.et, 0, "");
  }
  {
    const u32 vx0 = ((u32)(lane >> 3) * 1024u + 16u * ((lane & 7) ^ (lane >> 4))) ^ (64u * fo), vx1 = (vx0 ^ 64u) + 8192u;
    const char* xb = (const char*)a.x + (size_t)(rt * 128 + w * 16) * 1024;
#pragma unroll
    for (int b = 0; b < 8; b++) { const char* s = xb + ((b ^ (ft >> 1)) * 128); dma16(vx0, s, reg + b * 2048); dma16(vx1, s, reg + b * 2048 + 1024); }
  }
  VMW(16);
  TIE(pf); TIE(ep); TIE(pa); TIE(pb); TIE(pc_); TIE(pd); TIE(pe); TIE(pg); TIE(pl); TIE(pk); TIE(pb1); TIE(et);
  STAMP(1);
  v2u tag;
  { u64 z = (((u64)ep.y << 32) | ep.x) + 0x9E3779B97F4A7C15ull; z = (z ^ (z >> 30)) * 0xBF58476D1CE4E5B9ull; z = (z ^ (z >> 27)) * 0x94D049BB133111EBull; z ^= z >> 31;
    tag.x = __builtin_amdgcn_readfirstlane((u32)z); tag.y = __builtin_amdgcn_readfirstlane((u32)(z >> 32)); }

  const u32 part = SP + (u32)w * PARTS;
  {
    const float dt = rbf(expf(bf2f(pl)));
    float arl = dt * bf2f(pa) * 1.4426950408889634f; arl = fminf(fmaxf(arl, -1e30f), 1e30f);
    float f = dt * bf2f(pb) * 0.15915494309189535f; f -= rintf(f);
    const float br = dt * bf2f(pc_), bi = dt * bf2f(pd), cr = bf2f(pe), ci = bf2f(pg);
    const f2 W = cexp2(f2{arl, f} * 128.f);
    if (lane < 32) { lds_st<f4>(part + lane * 16, f4{arl, f, cr * br - ci * bi, cr * bi + ci * br}); lds_st<f2>(part + 512 + lane * 8, W); }
    if (tid < 112) lds_st<float>(LB1 + tid * 4, bf2f(pb1));
  }
  const float sS = 8.f * l15;
  const float tT = l15 < 8 ? (float)l15 : l15 == 8 ? 512.f : l15 == 9 ? 516.f : l15 < 13 ? 512.f * (l15 - 8) : 0.f;
  v4u xf[16];
  f4 kacc[4] = {{0, 0, 0, 0}, {0, 0, 0, 0}, {0, 0, 0, 0}, {0, 0, 0, 0}};
  {
    const u32 va0 = reg + l15 * 128 + 16 * (g ^ ((l15 >> 1) & 3)) + 64 * (l15 >> 3), va1 = va0 ^ 64u;
    const u32 vw = (u32)g * 3584u + 16u * (l15 ^ (2 * (g + 4 * ((w >> 1) & 1))));
    const char* wb = (const char*)a.w1 + (size_t)(4 * w) * 3584 + (224 * ft - 32 * fo);
    v4u KA[4], KB;
#pragma unroll
    for (int c = 0; c < 4; c++) {
#pragma unroll
      for (int i2 = 0; i2 < 2; i2++) {
        const int i = (c & 1) * 2 + i2, md = (c >> 1) * 16 + i;
        const f4 rec = lds_ld<f4>(part + g * 64 + md * 16);
        const f2 W = lds_ld<f2>(part + 512 + g * 32 + md * 8);
        const f2 af = {rec.x, rec.y};
        f2 A0 = cexp2(af * sS);
        f2 y = af * tT; y.y = __builtin_amdgcn_fractf(y.y);
        const f2 Bv = cmul_conj(cexp2(y), f2{rec.z, rec.w});
        const f2 A1 = cmul(A0, W), A2 = cmul(A1, W), A3 = cmul(A2, W);
        KA[0][i] = pk2(A0.x, A0.y); KA[1][i] = pk2(A1.x, A1.y); KA[2][i] = pk2(A2.x, A2.y); KA[3][i] = pk2(A3.x, A3.y); KB[i] = pk2(Bv.x, Bv.y);
      }
      if (c & 1) {
#pragma unroll
        for (int j = 0; j < 4; j++) kacc[j] = mfma(KA[j], KB, kacc[j]);
      }
      VMW(12);
#pragma unroll
      for (int s = 4 * c; s < 4 * c + 4; s++) xf[s] = lds_ld<v4u>(((s & 1) ? va1 : va0) + (s >> 1) * 2048);
      LGW0();
#pragma unroll
      for (int s = 4 * c; s < 4 * c + 4; s++) { TIE(xf[s]); dma16(vw, wb + (size_t)((s ^ ft) * 32 * 3584), reg + s * 1024); }
    }
  }
  STAMP(2);
  if (l15 < 12) {
#pragma unroll
    for (int j = 0; j < 4; j++) lds_st<f4>(part + l15 * 272 + j * 64 + g * 16, kacc[j]);
  } else if (lane == 12) lds_st<f4>(part + 12 * 272, kacc[0]);
  float rstd, nmr;
  {
    f4 sa = {0, 0, 0, 0}, qa = {0, 0, 0, 0};
    const v4u ones = {0x3f803f80u, 0x3f803f80u, 0x3f803f80u, 0x3f803f80u};
#pragma unroll
    for (int s = 0; s < 16; s++) { sa = mfma(ones, xf[s], sa); qa = mfma(xf[s], xf[s], qa); }
    const int dd = l15 - 4 * g;
    float q = dd == 0 ? qa[0] : dd == 1 ? qa[1] : dd == 2 ? qa[2] : dd == 3 ? qa[3] : 0.f;
    q += __shfl_xor(q, 16); q += __shfl_xor(q, 32);
    const float mean = sa[0] * (1.f / 512.f);
    const float var = fmaxf(q * (1.f / 512.f) - mean * mean, 0.f);
    const float sd = rbf(sqrtf(rbf(rbf(var) + 1e-5f)));
    const float mb = rbf(mean);
    rstd = 1.f / sd; nmr = -mb * rstd;
    const u32 uo = WS_U + (u32)(16 * ft + 4 * g) * 8192u + (u32)(rt * 128 + w * 16 + l15) * 4u;
    const char* ub = ws;
#pragma unroll
    for (int j = 0; j < 4; j++) {
      const u32 v = pk2(rbf(bf2f(xf[0][j] & 0xffffu) - mb) * rstd, rbf(bfhi(xf[0][j]) - mb) * rstd);
      GST("global_store_dword", uo, v, ub + j * 8192, 0, "sc1");
    }
  }
  LGW0(); BAR();
  u32 kv[4];
  {
    const u32 pb0 = SP + (u32)ch * 4u * PARTS;
    const float dsk = bf2f(pk);
#pragma unroll
    for (int r = 0; r < 4; r++) {
      const u32 s = pb0 + (et[r] & 1023u) * 4u;
      float v = (lds_ld<float>(s) + lds_ld<float>(s + PARTS)) + (lds_ld<float>(s + 2 * PARTS) + lds_ld<float>(s + 3 * PARTS));
      if (r == 2) v += __uint_as_float(__float_as_uint(dsk) & (u32)((int)(et[r] << 5) >> 31));
      kv[r] = pk2(v, v) & (u32)((int)(et[r] << 6) >> 31);
    }
  }
  VMW(16); LGW0(); BAR();
  {
    const u32 tb = SP + (u32)ch * TBS;
#pragma unroll
    for (int r = 0; r < 4; r++) {
      const u32 d = tb + ((et[r] >> 10) & 0x3fffu);
      const u32 st = (et[r] & (1u << 24)) ? (u32)-6 : CS_ - 2;
#pragma unroll
      for (int c = 0; c < 4; c++) lds_st<u16>(d + c * st, (u16)kv[r]);
    }
    if (tid < 60) lds_st<v4u>(LU + (tid >= 30 ? LUS - 480 : 0) + tid * 16, v4u{0, 0, 0, 0});
  }
  STAMP(3);
  f4 acc[7];
  {
    u32 ta[7], tc;
    const int q = l15 >> 2, kap = q + 4 * (g & 1);
    const u32 tb = (u32)g * 32768u + q * 256 + 8 * (lane & 3);
#pragma unroll
    for (int jl = 0; jl < 7; jl++) { const int ju = fo ? (jl == 6 ? 1 : jl + 2) : jl; ta[jl] = tb + 32 * (ju ^ kap); acc[jl] = f4{0, 0, 0, 0}; }
    { const int jl = w < 7 ? w : 6; const int ju = fo ? (jl == 6 ? 1 : jl + 2) : jl; tc = tb + 32 * (ju ^ kap); }
    float cs = 0.f;
    const bf2 one2 = __builtin_bit_cast(bf2, 0x3f803f80u);
#pragma unroll
    for (int s = 0; s < 16; s++) {
      if (s == 4) { VMW(12); BAR(); }
      if (s == 8) { VMW(8); BAR(); }
      if (s == 12) { VMW(4); BAR(); }
#pragma unroll
      for (int jl = 0; jl < 7; jl++) acc[jl] = mfma(lds_tr2(ta[jl] + s * 1024, ta[jl] + s * 1024 + 16384), xf[s], acc[jl]);
      const v4u c = lds_tr2(tc + s * 1024, tc + s * 1024 + 16384);
#pragma unroll
      for (int i = 0; i < 4; i++) { const u32 ci = c[i]; cs = __builtin_amdgcn_fdot2_f32_bf16(__builtin_bit_cast(bf2, ci), one2, cs, false); }
    }
    cs += __shfl_xor(cs, 16); cs += __shfl_xor(cs, 32);
    if (w < 7 && lane < 16) lds_st<float>(LCS + (w * 16 + lane) * 4, cs);
  }
  VMW(0);
  if (lane == 0) { const u32 o = WS_UF + (u32)((ft * 16 + rt) * 8 + w) * 8u; GST("global_store_dwordx2", o, tag, ws, 0, "sc1"); }
  LGW0(); BAR();
  STAMP(4);
  const u32 v2 = (u32)(lane >> 3) * 1024u + 16u * ((lane & 7) ^ (2 * (lane >> 4)));
  const char* w2b = (const char*)a.w2 + (size_t)(224 * w) * 1024 + 128 * nt;
#pragma unroll
  for (int i = 0; i < 16; i++) dma16(v2, w2b + i * 8192, reg + i * 1024);
  {
    u32 hp[14];
#pragma unroll
    for (int jl = 0; jl < 7; jl++) {
      const f4 b1v = lds_ld<f4>(LB1 + jl * 64 + g * 16), csv = lds_ld<f4>(LCS + jl * 64 + g * 16);
#pragma unroll
      for (int hh = 0; hh < 2; hh++) {
        const f2 ac = {acc[jl][2 * hh], acc[jl][2 * hh + 1]}, bb = {b1v[2 * hh], b1v[2 * hh + 1]}, cc = {csv[2 * hh], csv[2 * hh + 1]};
        const f2 v = ac * rstd + (cc * nmr + bb);
        const f2 z = v * (v * v * -0.10294324f + -2.3022082f);
        const f2 d = f2{__builtin_amdgcn_exp2f(z.x), __builtin_amdgcn_exp2f(z.y)} + 1.f;
        const f2 o = v * f2{__builtin_amdgcn_rcpf(d.x), __builtin_amdgcn_rcpf(d.y)};
        hp[jl * 2 + hh] = pk2(o.x, o.y);
      }
    }
    const char* hb = ws + WS_H + (size_t)(((rt * 8 + w) * 8 + (ft >> 1)) * 7) * 1024;
    const u32 o = (u32)lane * 16u + fo * 4096u, o2 = (u32)lane * 16u + fo * 8u;
    { v4u v = {hp[0], hp[1], hp[2], hp[3]}; GST("global_store_dwordx4", o, v, hb, 0, "sc1"); }
    { v4u v = {hp[4], hp[5], hp[6], hp[7]}; GST("global_store_dwordx4", o, v, hb, 1024, "sc1"); }
    { v4u v = {hp[8], hp[9], hp[10], hp[11]}; GST("global_store_dwordx4", o, v, hb, 2048, "sc1"); }
    { v2u v = {hp[12], hp[13]}; GST("global_store_dwordx2", o2, v, hb, 3072, "sc1"); }
  }
  STAMP(5);
  VMW(0);
  if (lane == 0) { const int M = rt * 8 + w; const u32 o = WS_HF + (u32)((((M >> 2) * 8 + (ft >> 1)) * 4 + (M & 3)) * 2 + fo) * 8u; GST("global_store_dwordx2", o, tag, ws, 0, "sc1"); }
  STAMP(6);
  {
    poll(WS_UF + (u32)(((bid >> 4) * 16 + 2 * w) * 8 + l15) * 8u, ws, tag);
    STAMP(7);
    v4u uu, s2, s3, ad;
    { const u32 o = WS_U + (u32)bid * 8192u + (u32)tid * 16u; GLD("global_load_dwordx4", uu, o, ws, 0, "sc1");
      const u32 o2 = (u32)lane * 48u; GLD("global_load_dwordx4", s2, o2, g_tabs.lt, 0, ""); GLD("global_load_dwordx4", s3, o2, g_tabs.lt, 16, ""); GLD("global_load_dwordx4", ad, o2, g_tabs.lt, 32, ""); }
    VMW(0); TIE(uu); TIE(s2); TIE(s3); TIE(ad);
    lds_st<v2u>(LU + 480 + tid * 8, v2u{__builtin_amdgcn_perm(uu.y, uu.x, 0x05040100u), __builtin_amdgcn_perm(uu.w, uu.z, 0x05040100u)});
    lds_st<v2u>(LU + LUS + 480 + tid * 8, v2u{__builtin_amdgcn_perm(uu.y, uu.x, 0x07060302u), __builtin_amdgcn_perm(uu.w, uu.z, 0x07060302u)});
    LGW0(); BAR();
    STAMP(8);
    f4 ca[4] = {{0, 0, 0, 0}, {0, 0, 0, 0}, {0, 0, 0, 0}, {0, 0, 0, 0}};
    const u32 tb = SP + (u32)ch * TBS, ua = LU + (u32)ch * LUS + 448 + 32 * l15 + 16 * g;
    const int th = w & 1, rh = (w >> 1) & 1;
    if (th) conv_body<1>(ca, ua, tb + ad.z, tb + ad.x, tb + ad.y, s2, s3, 4 * rh);
    else conv_body<0>(ca, ua, tb + ad.z, tb + ad.x, tb + ad.y, s2, s3, 4 * rh);
    LGW0(); BAR();
    STAMP(9);
    f4 k0, k1;
    if (rh) { lds_st<f4>(SP + w * 2048 + lane * 16, ca[0]); lds_st<f4>(SP + w * 2048 + 1024 + lane * 16, ca[1]); k0 = ca[2]; k1 = ca[3]; }
    else { lds_st<f4>(SP + w * 2048 + lane * 16, ca[2]); lds_st<f4>(SP + w * 2048 + 1024 + lane * 16, ca[3]); k0 = ca[0]; k1 = ca[1]; }
    LGW0(); BAR();
    k0 += lds_ld<f4>(SP + (w ^ 2) * 2048 + lane * 16); k1 += lds_ld<f4>(SP + (w ^ 2) * 2048 + 1024 + lane * 16);
    const int i0 = 4 * rh + th, i1 = 4 * rh + 3 - th;
    const u32 o = WS_Y + (u32)dch * 4096u + 32 * l15 + 8 * g;
    { v2u v = {pk2(k0[0], k0[1]), pk2(k0[2], k0[3])}; GST("global_store_dwordx2", o, v, ws + i0 * 512, 0, "sc1"); }
    { v2u v = {pk2(k1[0], k1[1]), pk2(k1[2], k1[3])}; GST("global_store_dwordx2", o, v, ws + i1 * 512, 0, "sc1"); }
    VMW(0);
    if (lane < 2) { const u32 of = WS_YF + (u32)((lane ? i1 : i0) * 512 + dch) * 8u; GST("global_store_dwordx2", of, tag, ws, 0, "sc1"); }
  }
  STAMP(10);
  {
    poll(WS_HF + (u32)((mt * 8 + w) * 8 + (lane & 7)) * 8u, ws, tag);
    STAMP(11);
    v4u hf[4][7];
    const u32 ho = (u32)lane * 16u;
#pragma unroll
    for (int f = 0; f < 7; f++)
#pragma unroll
      for (int ml = 0; ml < 4; ml++) {
        const char* hb = ws + WS_H + (size_t)(((4 * mt + (ml ^ (2 * (w >> 2)))) * 8 + w) * 7 + 3) * 1024;
        GLD("global_load_dwordx4", hf[ml][f], ho, hb, (f - 3) * 1024, "sc1");
      }
    v4u xr, b2v;
    { const u32 o = (u32)(mt * 64 + (tid >> 3)) * 1024u + nt * 128 + (tid & 7) * 16; GLD("global_load_dwordx4", xr, o, a.x, 0, "");
      const u32 o2 = nt * 128 + (tid & 7) * 16; GLD("global_load_dwordx4", b2v, o2, a.b2, 0, ""); }
    u32 ta[4];
    { const int q = l15 >> 2, r8 = 4 * (g & 1) + q;
#pragma unroll
      for (int jl = 0; jl < 4; jl++) ta[jl] = reg + (g >> 1) * 1024 + r8 * 128 + 32 * ((jl ^ (w & 3)) ^ (r8 >> 1)) + 8 * (lane & 3); }
    f4 c2[4][4];
#pragma unroll
    for (int i = 0; i < 4; i++)
#pragma unroll
      for (int j = 0; j < 4; j++) c2[i][j] = f4{0, 0, 0, 0};
    v4u wf[3][4];
#pragma unroll
    for (int f = 0; f < 3; f++)
#pragma unroll
      for (int jl = 0; jl < 4; jl++) wf[f][jl] = lds_tr2(ta[jl] + f * 4096, ta[jl] + f * 4096 + 2048);
    LGW0();
#pragma unroll
    for (int f = 0; f < 3; f++)
#pragma unroll
      for (int jl = 0; jl < 4; jl++) TIE(wf[f][jl]);
#pragma unroll
    for (int i = 0; i < 12; i++) dma16(v2, w2b + (16 + i) * 8192, reg + i * 1024);
#pragma unroll
    for (int f = 0; f < 7; f++) {
      if (f < 4) VMW(14 + 4 * (6 - f)); else VMW(4 * (6 - f));
#pragma unroll
      for (int ml = 0; ml < 4; ml++) TIE(hf[ml][f]);
#pragma unroll
      for (int jl = 0; jl < 4; jl++) {
        const int sl = f == 3 ? 3 : f - 4;
        const v4u wa = f < 3 ? wf[f][jl] : lds_tr2(ta[jl] + sl * 4096, ta[jl] + sl * 4096 + 2048);
#pragma unroll
        for (int ml = 0; ml < 4; ml++) c2[jl][ml] = mfma(wa, hf[ml][f], c2[jl][ml]);
      }
    }
    TIE(xr); TIE(b2v);
    STAMP(12);
    poll(WS_YF + (u32)((mt >> 2) * 512 + nt * 64 + (w & 3) * 16 + l15) * 8u, ws, tag);
    v4u yf;
    { const u32 o = WS_Y + (u32)(nt * 64 + (w & 3) * 16 + l15) * 4096u + (u32)(mt * 64 + (w >> 2) * 32 + g * 8) * 2u; GLD("global_load_dwordx4", yf, o, ws, 0, "sc1"); }
    v4u il, ih;
#pragma unroll
    for (int i = 0; i < 4; i++) { const u32 v = ((l15 & 7) >> 1) == i ? (0x3f80u << (16 * (l15 & 1))) : 0u; il[i] = g == (l15 >> 3) ? v : 0u; ih[i] = g == 2 + (l15 >> 3) ? v : 0u; }
    VMW(0); TIE(yf);
    c2[0][0] = mfma(yf, il, c2[0][0]); c2[0][1] = mfma(yf, ih, c2[0][1]);
    STAMP(13);
    LGW0();
#pragma unroll
    for (int jl = 0; jl < 4; jl++)
#pragma unroll
      for (int ml = 0; ml < 4; ml++)
        lds_st<f4>(reg + (((ml ^ (2 * (w >> 2))) * 16 + l15) * 256) + 16 * ((4 * (jl ^ (w & 3)) + g) ^ (l15 & 7)), c2[jl][ml]);
    LGW0(); BAR();
    const int row = tid >> 3, c8 = tid & 7;
    const u32 ra = row * 256 + 16 * ((2 * c8) ^ (row & 7)), rb = ra ^ 16u;
    f4 s0 = lds_ld<f4>(ra), s1 = lds_ld<f4>(rb);
#pragma unroll
    for (int ww = 1; ww < 8; ww++) { s0 += lds_ld<f4>(ra + ww * 16384); s1 += lds_ld<f4>(rb + ww * 16384); }
    v4u o;
    o.x = pk2(s0[0] + bf2f(b2v.x & 0xffffu) + bf2f(xr.x & 0xffffu), s0[1] + bfhi(b2v.x) + bfhi(xr.x));
    o.y = pk2(s0[2] + bf2f(b2v.y & 0xffffu) + bf2f(xr.y & 0xffffu), s0[3] + bfhi(b2v.y) + bfhi(xr.y));
    o.z = pk2(s1[0] + bf2f(b2v.z & 0xffffu) + bf2f(xr.z & 0xffffu), s1[1] + bfhi(b2v.z) + bfhi(xr.z));
    o.w = pk2(s1[2] + bf2f(b2v.w & 0xffffu) + bf2f(xr.w & 0xffffu), s1[3] + bfhi(b2v.w) + bfhi(xr.w));
    const u32 oo = (u32)(mt * 64 + row) * 1024u + nt * 128 + c8 * 16;
    GST("global_store_dwordx4", oo, o, a.out, 0, "");
  }
  STAMP(14);
  if (bid == 0 && w < 4) {
    poll(WS_UF + (u32)(tid * 8) * 8u, ws, tag);
  }
  if (bid == 0) { BAR(); if (tid == 0) { u32 z = 0; GST("global_store_dwordx2", z, tag, ws, 0, "sc1"); } }
  asm volatile(".Lfk_end:");
}

extern "C" void kernel_launch(void* const* d_in, const int* in_sizes, int n_in, void* d_out, int out_size, void* d_ws, size_t ws_size, hipStream_t stream) {
  Args a;
  a.x = (const u16*)d_in[0]; a.are = (const u16*)d_in[1]; a.aim = (const u16*)d_in[2]; a.bre = (const u16*)d_in[3]; a.bim = (const u16*)d_in[4];
  a.cre = (const u16*)d_in[5]; a.cim = (const u16*)d_in[6]; a.ldt = (const u16*)d_in[7]; a.dsk = (const u16*)d_in[8];
  a.w1 = (const u16*)d_in[10]; a.b1 = (const u16*)d_in[11]; a.w2 = (const u16*)d_in[12]; a.b2 = (const u16*)d_in[13];
  a.out = (u16*)d_out; a.ws = (char*)d_ws;
  (void)hipFuncSetAttribute((const void*)fused_kernel, hipFuncAttributeMaxDynamicSharedMemorySize, LDS_BYTES);
  fused_kernel<<<256, 512, LDS_BYTES, stream>>>(a);
}
```

```cpp
#define STAMPS
#include <hip/hip_runtime.h>
#include <stdint.h>

typedef unsigned u32; typedef unsigned long long u64; typedef unsigned short u16;
typedef u32 v4u __attribute__((ext_vector_type(4)));
typedef u32 v2u __attribute__((ext_vector_type(2)));
typedef float f4 __attribute__((ext_vector_type(4)));
typedef float f2 __attribute__((ext_vector_type(2)));
typedef short s4 __attribute__((ext_vector_type(4)));
typedef __bf16 bf8 __attribute__((ext_vector_type(8)));
typedef __bf16 bf2 __attribute__((ext_vector_type(2)));
#define LAS __attribute__((address_space(3)))
#define DI __device__ __forceinline__

constexpr u32 WS_E = 0, WS_UF = 4096, WS_HF = WS_UF + 16384, WS_YF = WS_HF + 16384;
constexpr u32 WS_ST = 81920;
constexpr u32 WS_U = 1u << 18, WS_Y = WS_U + (2u << 20), WS_H = WS_Y + (2u << 20);
constexpr u32 SP = 131072, PARTS = 3328, CS_ = 1344, TB_Q2 = 5376 + 24, TB_Q3 = 5376 + 1088 + 24, TBS = 7552;
constexpr u32 LU = SP + 2 * TBS, LUS = 4608, LB1 = SP + 26624, LCS = LB1 + 448, LDUMMY = LCS + 448;
constexpr u32 LDS_BYTES = 163840;

constexpr int rnd_bf(int j) { if (j <= 256) return j; int sh = j < 512 ? 1 : j < 1024 ? 2 : 3; int q = j >> sh, rem = j & ((1 << sh) - 1), h = 1 << (sh - 1);
  if (rem > h || (rem == h && (q & 1))) q++; return q << sh; }
constexpr int src_of(int p) { return p < 512 ? (p & 7) * 68 + (p >> 3) : p < 1024 ? (8 + ((p >> 2) & 1)) * 68 + ((p - 512) >> 3)
  : p < 1536 ? 10 * 68 + ((p - 1024) >> 3) : p < 2048 ? 11 * 68 + ((p - 1536) >> 3) : 12 * 68; }
struct Tabs { u32 et[1024]; u32 lt[64 * 12]; };
constexpr u32 enc(int src, int dst, int quad, int keep, int dsk) { return (u32)src | ((u32)dst << 10) | ((u32)quad << 24) | ((u32)keep << 25) | ((u32)dsk << 26); }
constexpr Tabs make_tabs() {
  Tabs t{};
  u32 lin[1024] = {};
  int n = 0;
  for (int x = 0; x <= 558; x++) { int j = 543 - x; lin[n++] = j >= 0 ? enc(src_of(rnd_bf(j)), 2 * x, 0, 1, j == 0) : enc(0, 2 * x, 0, 0, 0); }
  for (int x = 0; x <= 46; x++) lin[n++] = enc(src_of(rnd_bf(1055 - x)), 2 * (560 + x), 0, 1, 0);
  for (int m = 0; m <= 128; m++) lin[n++] = enc(src_of(512 + 4 * m), TB_Q2 + 8 * m, 1, 1, 0);
  for (int m = 0; m <= 128; m++) lin[n++] = enc(src_of(1024 + 8 * m), TB_Q3 + 8 * m, 1, 1, 0);
  while (n < 1024) lin[n++] = enc(0, 2 * 640, 0, 0, 0);
  for (int th = 0; th < 256; th++) for (int r = 0; r < 4; r++) t.et[th * 4 + r] = lin[r * 256 + th];
  for (int l = 0; l < 64; l++) {
    int b = l & 15, g = l >> 4;
    int j2 = 32 * 17 + 16 + b - 8 * g, j3 = 32 * 33 + 16 + b - 8 * g;
    int m2 = (rnd_bf(j2 - 7) - 512) / 4, m3 = (rnd_bf(j3 - 7) - 1024) / 8;
    for (int i = 0; i < 4; i++) {
      int a2 = (rnd_bf(j2 - 2 * i) - 512) / 4 - m2, b2 = (rnd_bf(j2 - 2 * i - 1) - 512) / 4 - m2;
      int a3 = (rnd_bf(j3 - 2 * i) - 1024) / 8 - m3, b3 = (rnd_bf(j3 - 2 * i - 1) - 1024) / 8 - m3;
      t.lt[l * 12 + i] = (u32)(2 * a2) | (u32)(2 * a2 + 1) << 8 | (u32)(2 * b2) << 16 | (u32)(2 * b2 + 1) << 24;
      t.lt[l * 12 + 4 + i] = (u32)(2 * a3) | (u32)(2 * a3 + 1) << 8 | (u32)(2 * b3) << 16 | (u32)(2 * b3 + 1) << 24;
    }
    t.lt[l * 12 + 8] = TB_Q2 + 8 * m2 - 64;
    t.lt[l * 12 + 9] = TB_Q3 + 8 * m3 - 32;
    int p = 15 - b + 8 * g, c = p & 3;
    t.lt[l * 12 + 10] = c * CS_ + 2 * (p - c);
    t.lt[l * 12 + 11] = 0;
  }
  return t;
}
__device__ const Tabs g_tabs = make_tabs();

struct Args { const u16 *x, *are, *aim, *bre, *bim, *cre, *cim, *ldt, *dsk, *w1, *b1, *w2, *b2; u16* out; char* ws; };

DI float bf2f(u32 v) { return __uint_as_float(v << 16); }
DI float bfhi(u32 v) { return __uint_as_float(v & 0xffff0000u); }
DI u32 pk2(float a, float b) { bf2 c; c[0] = (__bf16)a; c[1] = (__bf16)b; return __builtin_bit_cast(u32, c); }
DI float rbf(float a) { return bf2f(pk2(a, a) & 0xffffu); }
DI f4 mfma(v4u a, v4u b, f4 c) { return __builtin_amdgcn_mfma_f32_16x16x32_bf16(__builtin_bit_cast(bf8, a), __builtin_bit_cast(bf8, b), c, 0, 0, 0); }
template <class T> DI T lds_ld(u32 a) { return *(const LAS T*)(size_t)a; }
template <class T> DI void lds_st(u32 a, T v) { *(LAS T*)(size_t)a = v; }
DI v2u lds_tr(u32 a) { return __builtin_bit_cast(v2u, __builtin_amdgcn_ds_read_tr16_b64_v4i16((LAS s4*)(size_t)a)); }
DI v4u lds_tr2(u32 a, u32 b) { v2u x = lds_tr(a), y = lds_tr(b); return v4u{x.x, x.y, y.x, y.y}; }
DI f2 cmul(f2 a, f2 w) {
  f2 t, r;
  asm("v_pk_mul_f32 %0, %1, %2 op_sel:[0,0] op_sel_hi:[1,0]" : "=v"(t) : "v"(a), "v"(w));
  asm("v_pk_fma_f32 %0, %1, %2, %3 op_sel:[1,1,0] op_sel_hi:[0,1,1] neg_lo:[1,0,0]" : "=v"(r) : "v"(a), "v"(w), "v"(t));
  return r;
}
DI f2 cmul_conj(f2 a, f2 w) {
  f2 t, r;
  asm("v_pk_mul_f32 %0, %1, %2 op_sel:[0,0] op_sel_hi:[1,0] neg_hi:[1,0]" : "=v"(t) : "v"(a), "v"(w));
  asm("v_pk_fma_f32 %0, %1, %2, %3 op_sel:[1,1,0] op_sel_hi:[0,1,1] neg_lo:[1,0,0] neg_hi:[1,0,0]" : "=v"(r) : "v"(a), "v"(w), "v"(t));
  return r;
}
DI f2 cexp2(f2 x) {
  float e = __builtin_amdgcn_exp2f(x.x), c = __builtin_amdgcn_cosf(x.y), s = __builtin_amdgcn_sinf(x.y);
  return f2{c, s} * e;
}
#define VMW(n) asm volatile("s_waitcnt vmcnt(%0)" ::"n"(n) : "memory")
#define LGW0() asm volatile("s_waitcnt lgkmcnt(0)" ::: "memory")
#define BAR() asm volatile("s_barrier" ::: "memory")
#define TIE(x) asm volatile("" : "+v"(x))
#define GLD(op, dst, voff, sbase, imm, fl) asm volatile(op " %0, %1, %2 offset:%3 " fl : "=v"(dst) : "v"(voff), "s"(sbase), "n"(imm) : "memory")
#define GST(op, voff, src, sbase, imm, fl) asm volatile(op " %0, %1, %2 offset:%3 " fl "\n\ts_nop 1" ::"v"(voff), "v"(src), "s"(sbase), "n"(imm) : "memory")
DI void dma16(u32 voff, const void* sbase, u32 ldst) {
  asm volatile("s_mov_b32 m0, %2\n\ts_nop 0\n\tglobal_load_lds_dwordx4 %0, %1" ::"v"(voff), "s"(sbase), "s"(ldst) : "memory");
}
DI void poll(u32 voff, const char* ws, v2u tag) {
  for (int it = 0; it < (1 << 16); it++) {
    v2u f;
    asm volatile("global_load_dwordx2 %0, %1, %2 sc1\n\ts_waitcnt vmcnt(0)" : "=v"(f) : "v"(voff), "s"(ws) : "memory");
    if (__builtin_amdgcn_ballot_w64(f.x != tag.x || f.y != tag.y) == 0) break;
    __builtin_amdgcn_s_sleep(2);
  }
}
#define STAMP(i) do { u64 t_; asm volatile("s_memrealtime %0\n\ts_waitcnt lgkmcnt(0)" : "=s"(t_)::"memory"); stv[i] = t_; } while (0)

template <int TH> DI void conv_body(f4 (&acc)[4], u32 ua, u32 ea, u32 q2a, u32 q3a, v4u sel2, v4u sel3, int r0) {
  constexpr int T[4] = {TH ? 1 : 0, TH ? 2 : 3, TH ? 5 : 4, TH ? 6 : 7};
  constexpr int NA = T[3] + 1;
#pragma unroll 1
  for (int rr = 0; rr < 4; rr++) {
    const int r = r0 + rr;
    v4u A[NA];
#pragma unroll
    for (int i = 0; i < NA; i++) A[i] = lds_ld<v4u>(ua - 64 * r + 512 * i);
#pragma unroll
    for (int m = 0; m < NA; m++) {
      v4u B;
      if (m < 2) { v2u a = lds_ld<v2u>(ea - 64 * r + 64 * (16 - 8 * m)), b = lds_ld<v2u>(ea - 64 * r + 64 * (16 - 8 * m) + 8); B = v4u{a.x, a.y, b.x, b.y}; }
      else if ((m == 2 || m == 4) && r == 0) { v2u a = lds_ld<v2u>(ea + (m == 2 ? 0 : 1120)), b = lds_ld<v2u>(ea + (m == 2 ? 0 : 1120) + 8); B = v4u{a.x, a.y, b.x, b.y}; }
      else if (m < 4) { v2u q = lds_ld<v2u>(q2a + 64 * r + 512 * (m - 2));
        B = v4u{__builtin_amdgcn_perm(q.y, q.x, sel2.x), __builtin_amdgcn_perm(q.y, q.x, sel2.y), __builtin_amdgcn_perm(q.y, q.x, sel2.z), __builtin_amdgcn_perm(q.y, q.x, sel2.w)}; }
      else { u32 q = lds_ld<u32>(q3a + 32 * r + 256 * (m - 4));
        B = v4u{__builtin_amdgcn_perm(q, q, sel3.x), __builtin_amdgcn_perm(q, q, sel3.y), __builtin_amdgcn_perm(q, q, sel3.z), __builtin_amdgcn_perm(q, q, sel3.w)}; }
#pragma unroll
      for (int t = 0; t < 4; t++) if (T[t] >= m) acc[t] = mfma(B, A[T[t] - m], acc[t]);
    }
  }
}

__global__ __launch_bounds__(512, 2) void fused_kernel(Args a) {
  extern __shared__ __attribute__((aligned(16))) char smem[];
  u64 stv[16];
  STAMP(0);
  const int tid = threadIdx.x, lane = tid & 63, w = __builtin_amdgcn_readfirstlane(tid >> 6), l15 = lane & 15, g = lane >> 4;
  const int bid = blockIdx.x, xcd = bid & 7, idx = bid >> 3;
  const int rt = (xcd >> 1) * 4 + (idx >> 3), ft = (xcd & 1) * 8 + (idx & 7), fo = ft & 1;
  const int mt = xcd * 4 + (idx >> 3), nt = idx & 7;
  const int ch = w >> 2, dch = 2 * bid + ch;
  const char* ws = a.ws;
  const u32 reg = (u32)w * 16384u;

  u32 pf; { u64 pc; u32 len; asm volatile("s_getpc_b64 %0\n.Lfk_beg:\n\ts_mov_b32 %1, .Lfk_end-.Lfk_beg" : "=s"(pc), "=s"(len)); u32 o = min((u32)tid * 64u, len); GLD("global_load_dword", pf, o, pc, 0, ""); }
  v2u ep; u32 pa, pb, pc_, pd, pe, pg, pl, pk, pb1; v4u et;
  {
    u32 z = 0; GLD("global_load_dwordx2", ep, z, ws, 0, "sc1");
    u32 o = (u32)(dch * 128 + (w & 3) * 32 + (lane & 31)) * 2u;
    GLD("global_load_ushort", pa, o, a.are, 0, ""); GLD("global_load_ushort", pb, o, a.aim, 0, "");
    GLD("global_load_ushort", pc_, o, a.bre, 0, ""); GLD("global_load_ushort", pd, o, a.bim, 0, "");
    GLD("global_load_ushort", pe, o, a.cre, 0, ""); GLD("global_load_ushort", pg, o, a.cim, 0, "");
    u32 o2 = (u32)dch * 2u; GLD("global_load_ushort", pl, o2, a.ldt, 0, ""); GLD("global_load_ushort", pk, o2, a.dsk, 0, "");
    int jl = (tid < 112 ? tid : 0) >> 4; int j = fo ? (jl == 6 ? 0 : jl + 1) : jl;
    u32 o3 = (u32)(ft * 112 + j * 16 + l15) * 2u; GLD("global_load_ushort", pb1, o3, a.b1, 0, "");
    u32 o4 = (u32)(tid & 255) * 16u; GLD("global_load_dwordx4", et, o4, g_tabs.et, 0, "");
  }
  {
    const u32 vx0 = ((u32)(lane >> 3) * 1024u + 16u * ((lane & 7) ^ (lane >> 4))) ^ (64u * fo), vx1 = (vx0 ^ 64u) + 8192u;
    const char* xb = (const char*)a.x + (size_t)(rt * 128 + w * 16) * 1024;
#pragma unroll
    for (int b = 0; b < 8; b++) { const char* s = xb + ((b ^ (ft >> 1)) * 128); dma16(vx0, s, reg + b * 2048); dma16(vx1, s, reg + b * 2048 + 1024); }
  }
  VMW(16);
  TIE(pf); TIE(ep); TIE(pa); TIE(pb); TIE(pc_); TIE(pd); TIE(pe); TIE(pg); TIE(pl); TIE(pk); TIE(pb1); TIE(et);
  STAMP(1);
  v2u tag;
  { u64 z = (((u64)ep.y << 32) | ep.x) + 0x9E3779B97F4A7C15ull; z = (z ^ (z >> 30)) * 0xBF58476D1CE4E5B9ull; z = (z ^ (z >> 27)) * 0x94D049BB133111EBull; z ^= z >> 31;
    tag.x = __builtin_amdgcn_readfirstlane((u32)z); tag.y = __builtin_amdgcn_readfirstlane((u32)(z >> 32)); }

  const u32 part = SP + (u32)w * PARTS;
  {
    const float dt = rbf(expf(bf2f(pl)));
    float arl = dt * bf2f(pa) * 1.4426950408889634f; arl = fminf(fmaxf(arl, -1e30f), 1e30f);
    float f = dt * bf2f(pb) * 0.15915494309189535f; f -= rintf(f);
    const float br = dt * bf2f(pc_), bi = dt * bf2f(pd), cr = bf2f(pe), ci = bf2f(pg);
    const f2 W = cexp2(f2{arl, f} * 128.f);
    if (lane < 32) { lds_st<f4>(part + lane * 16, f4{arl, f, cr * br - ci * bi, cr * bi + ci * br}); lds_st<f2>(part + 512 + lane * 8, W); }
    if (tid < 112) lds_st<float>(LB1 + tid * 4, bf2f(pb1));
  }
  const float sS = 8.f * l15;
  const float tT = l15 < 8 ? (float)l15 : l15 == 8 ? 512.f : l15 == 9 ? 516.f : l15 < 13 ? 512.f * (l15 - 8) : 0.f;
  v4u xf[16];
  f4 kacc[4] = {{0, 0, 0, 0}, {0, 0, 0, 0}, {0, 0, 0, 0}, {0, 0, 0, 0}};
  {
    const u32 va0 = reg + l15 * 128 + 16 * (g ^ ((l15 >> 1) & 3)) + 64 * (l15 >> 3), va1 = va0 ^ 64u;
    const u32 vw = (u32)g * 3584u + 16u * (l15 ^ (2 * (g + 4 * ((w >> 1) & 1))));
    const char* wb = (const char*)a.w1 + (size_t)(4 * w) * 3584 + (224 * ft - 32 * fo);
    v4u KA[4], KB;
#pragma unroll
    for (int c = 0; c < 4; c++) {
#pragma unroll
      for (int i2 = 0; i2 < 2; i2++) {
        const int i = (c & 1) * 2 + i2, md = (c >> 1) * 16 + i;
        const f4 rec = lds_ld<f4>(part + g * 64 + md * 16);
        const f2 W = lds_ld<f2>(part + 512 + g * 32 + md * 8);
        const f2 af = {rec.x, rec.y};
        f2 A0 = cexp2(af * sS);
        f2 y = af * tT; y.y = __builtin_amdgcn_fractf(y.y);
        const f2 Bv = cmul_conj(cexp2(y), f2{rec.z, rec.w});
        const f2 A1 = cmul(A0, W), A2 = cmul(A1, W), A3 = cmul(A2, W);
        KA[0][i] = pk2(A0.x, A0.y); KA[1][i] = pk2(A1.x, A1.y); KA[2][i] = pk2(A2.x, A2.y); KA[3][i] = pk2(A3.x, A3.y); KB[i] = pk2(Bv.x, Bv.y);
      }
      if (c & 1) {
#pragma unroll
        for (int j = 0; j < 4; j++) kacc[j] = mfma(KA[j], KB, kacc[j]);
      }
      VMW(12);
#pragma unroll
      for (int s = 4 * c; s < 4 * c + 4; s++) xf[s] = lds_ld<v4u>(((s & 1) ? va1 : va0) + (s >> 1) * 2048);
      LGW0();
#pragma unroll
      for (int s = 4 * c; s < 4 * c + 4; s++) { TIE(xf[s]); dma16(vw, wb + (size_t)((s ^ ft) * 32 * 3584), reg + s * 1024); }
    }
  }
  STAMP(2);
  if (l15 < 12) {
#pragma unroll
    for (int j = 0; j < 4; j++) lds_st<f4>(part + l15 * 272 + j * 64 + g * 16, kacc[j]);
  } else if (lane == 12) lds_st<f4>(part + 12 * 272, kacc[0]);
  float rstd, nmr;
  {
    f4 sa = {0, 0, 0, 0}, qa = {0, 0, 0, 0};
    const v4u ones = {0x3f803f80u, 0x3f803f80u, 0x3f803f80u, 0x3f803f80u};
#pragma unroll
    for (int s = 0; s < 16; s++) { sa = mfma(ones, xf[s], sa); qa = mfma(xf[s], xf[s], qa); }
    const int dd = l15 - 4 * g;
    float q = dd == 0 ? qa[0] : dd == 1 ? qa[1] : dd == 2 ? qa[2] : dd == 3 ? qa[3] : 0.f;
    q += __shfl_xor(q, 16); q += __shfl_xor(q, 32);
    const float mean = sa[0] * (1.f / 512.f);
    const float var = fmaxf(q * (1.f / 512.f) - mean * mean, 0.f);
    const float sd = rbf(sqrtf(rbf(rbf(var) + 1e-5f)));
    const float mb = rbf(mean);
    rstd = 1.f / sd; nmr = -mb * rstd;
    const u32 uo = WS_U + (u32)(16 * ft + 4 * g) * 8192u + (u32)(rt * 128 + w * 16 + l15) * 4u;
    const char* ub = ws;
#pragma unroll
    for (int j = 0; j < 4; j++) {
      const u32 v = pk2(rbf(bf2f(xf[0][j] & 0xffffu) - mb) * rstd, rbf(bfhi(xf[0][j]) - mb) * rstd);
      GST("global_store_dword", uo, v, ub + j * 8192, 0, "sc1");
    }
  }
  LGW0(); BAR();
  u32 kv[4];
  {
    const u32 pb0 = SP + (u32)ch * 4u * PARTS;
    const float dsk = bf2f(pk);
#pragma unroll
    for (int r = 0; r < 4; r++) {
      const u32 s = pb0 + (et[r] & 1023u) * 4u;
      float v = (lds_ld<float>(s) + lds_ld<float>(s + PARTS)) + (lds_ld<float>(s + 2 * PARTS) + lds_ld<float>(s + 3 * PARTS));
      if (r == 2) v += __uint_as_float(__float_as_uint(dsk) & (u32)((int)(et[r] << 5) >> 31));
      kv[r] = pk2(v, v) & (u32)((int)(et[r] << 6) >> 31);
    }
  }
  VMW(16); LGW0(); BAR();
  {
    const u32 tb = SP + (u32)ch * TBS;
#pragma unroll
    for (int r = 0; r < 4; r++) {
      const u32 d = tb + ((et[r] >> 10) & 0x3fffu);
      const u32 st = (et[r] & (1u << 24)) ? (u32)-6 : CS_ - 2;
#pragma unroll
      for (int c = 0; c < 4; c++) lds_st<u16>(d + c * st, (u16)kv[r]);
    }
    if (tid < 60) lds_st<v4u>(LU + (tid >= 30 ? LUS - 480 : 0) + tid * 16, v4u{0, 0, 0, 0});
  }
  STAMP(3);
  f4 acc[7];
  {
    u32 ta[7], tc;
    const int q = l15 >> 2, kap = q + 4 * (g & 1);
    const u32 tb = (u32)g * 32768u + q * 256 + 8 * (lane & 3);
#pragma unroll
    for (int jl = 0; jl < 7; jl++) { const int ju = fo ? (jl == 6 ? 1 : jl + 2) : jl; ta[jl] = tb + 32 * (ju ^ kap); acc[jl] = f4{0, 0, 0, 0}; }
    { const int jl = w < 7 ? w : 6; const int ju = fo ? (jl == 6 ? 1 : jl + 2) : jl; tc = tb + 32 * (ju ^ kap); }
    float cs = 0.f;
    const bf2 one2 = __builtin_bit_cast(bf2, 0x3f803f80u);
#pragma unroll
    for (int s = 0; s < 16; s++) {
      if (s == 4) { VMW(12); BAR(); }
      if (s == 8) { VMW(8); BAR(); }
      if (s == 12) { VMW(4); BAR(); }
#pragma unroll
      for (int jl = 0; jl < 7; jl++) acc[jl] = mfma(lds_tr2(ta[jl] + s * 1024, ta[jl] + s * 1024 + 16384), xf[s], acc[jl]);
      const v4u c = lds_tr2(tc + s * 1024, tc + s * 1024 + 16384);
#pragma unroll
      for (int i = 0; i < 4; i++) { const u32 ci = c[i]; cs = __builtin_amdgcn_fdot2_f32_bf16(__builtin_bit_cast(bf2, ci), one2, cs, false); }
    }
    cs += __shfl_xor(cs, 16); cs += __shfl_xor(cs, 32);
    if (w < 7 && lane < 16) lds_st<float>(LCS + (w * 16 + lane) * 4, cs);
  }
  VMW(0);
  if (lane == 0) { const u32 o = WS_UF + (u32)((ft * 16 + rt) * 8 + w) * 8u; GST("global_store_dwordx2", o, tag, ws, 0, "sc1"); }
  LGW0(); BAR();
  STAMP(4);
  const u32 v2 = (u32)(lane >> 3) * 1024u + 16u * ((lane & 7) ^ (2 * (lane >> 4)));
  const char* w2b = (const char*)a.w2 + (size_t)(224 * w) * 1024 + 128 * nt;
#pragma unroll
  for (int i = 0; i < 16; i++) dma16(v2, w2b + i * 8192, reg + i * 1024);
  {
    u32 hp[14];
#pragma unroll
    for (int jl = 0; jl < 7; jl++) {
      const f4 b1v = lds_ld<f4>(LB1 + jl * 64 + g * 16), csv = lds_ld<f4>(LCS + jl * 64 + g * 16);
#pragma unroll
      for (int hh = 0; hh < 2; hh++) {
        const f2 ac = {acc[jl][2 * hh], acc[jl][2 * hh + 1]}, bb = {b1v[2 * hh], b1v[2 * hh + 1]}, cc = {csv[2 * hh], csv[2 * hh + 1]};
        const f2 v = ac * rstd + (cc * nmr + bb);
        const f2 z = v * (v * v * -0.10294324f + -2.3022082f);
        const f2 d = f2{__builtin_amdgcn_exp2f(z.x), __builtin_amdgcn_exp2f(z.y)} + 1.f;
        const f2 o = v * f2{__builtin_amdgcn_rcpf(d.x), __builtin_amdgcn_rcpf(d.y)};
        hp[jl * 2 + hh] = pk2(o.x, o.y);
      }
    }
    const char* hb = ws + WS_H + (size_t)(((rt * 8 + w) * 8 + (ft >> 1)) * 7) * 1024;
    const u32 o = (u32)lane * 16u + fo * 4096u, o2 = (u32)lane * 16u + fo * 8u;
    { v4u v = {hp[0], hp[1], hp[2], hp[3]}; GST("global_store_dwordx4", o, v, hb, 0, "sc1"); }
    { v4u v = {hp[4], hp[5], hp[6], hp[7]}; GST("global_store_dwordx4", o, v, hb, 1024, "sc1"); }
    { v4u v = {hp[8], hp[9], hp[10], hp[11]}; GST("global_store_dwordx4", o, v, hb, 2048, "sc1"); }
    { v2u v = {hp[12], hp[13]}; GST("global_store_dwordx2", o2, v, hb, 3072, "sc1"); }
  }
  STAMP(5);
  VMW(0);
  if (lane == 0) { const int M = rt * 8 + w; const u32 o = WS_HF + (u32)((((M >> 2) * 8 + (ft >> 1)) * 4 + (M & 3)) * 2 + fo) * 8u; GST("global_store_dwordx2", o, tag, ws, 0, "sc1"); }
  STAMP(6);
  {
    poll(WS_UF + (u32)(((bid >> 4) * 16 + 2 * w) * 8 + l15) * 8u, ws, tag);
    STAMP(7);
    v4u uu, s2, s3, ad;
    { const u32 o = WS_U + (u32)bid * 8192u + (u32)tid * 16u; GLD("global_load_dwordx4", uu, o, ws, 0, "sc1");
      const u32 o2 = (u32)lane * 48u; GLD("global_load_dwordx4", s2, o2, g_tabs.lt, 0, ""); GLD("global_load_dwordx4", s3, o2, g_tabs.lt, 16, ""); GLD("global_load_dwordx4", ad, o2, g_tabs.lt, 32, ""); }
    VMW(0); TIE(uu); TIE(s2); TIE(s3); TIE(ad);
    lds_st<v2u>(LU + 480 + tid * 8, v2u{__builtin_amdgcn_perm(uu.y, uu.x, 0x05040100u), __builtin_amdgcn_perm(uu.w, uu.z, 0x05040100u)});
    lds_st<v2u>(LU + LUS + 480 + tid * 8, v2u{__builtin_amdgcn_perm(uu.y, uu.x, 0x07060302u), __builtin_amdgcn_perm(uu.w, uu.z, 0x07060302u)});
    LGW0(); BAR();
    STAMP(8);
    f4 ca[4] = {{0, 0, 0, 0}, {0, 0, 0, 0}, {0, 0, 0, 0}, {0, 0, 0, 0}};
    const u32 tb = SP + (u32)ch * TBS, ua = LU + (u32)ch * LUS + 448 + 32 * l15 + 16 * g;
    const int th = w & 1, rh = (w >> 1) & 1;
    if (th) conv_body<1>(ca, ua, tb + ad.z, tb + ad.x, tb + ad.y, s2, s3, 4 * rh);
    else conv_body<0>(ca, ua, tb + ad.z, tb + ad.x, tb + ad.y, s2, s3, 4 * rh);
    LGW0(); BAR();
    STAMP(9);
    f4 k0, k1;
    if (rh) { lds_st<f4>(SP + w * 2048 + lane * 16, ca[0]); lds_st<f4>(SP + w * 2048 + 1024 + lane * 16, ca[1]); k0 = ca[2]; k1 = ca[3]; }
    else { lds_st<f4>(SP + w * 2048 + lane * 16, ca[2]); lds_st<f4>(SP + w * 2048 + 1024 + lane * 16, ca[3]); k0 = ca[0]; k1 = ca[1]; }
    LGW0(); BAR();
    k0 += lds_ld<f4>(SP + (w ^ 2) * 2048 + lane * 16); k1 += lds_ld<f4>(SP + (w ^ 2) * 2048 + 1024 + lane * 16);
    const int i0 = 4 * rh + th, i1 = 4 * rh + 3 - th;
    const u32 o = WS_Y + (u32)dch * 4096u + 32 * l15 + 8 * g;
    { v2u v = {pk2(k0[0], k0[1]), pk2(k0[2], k0[3])}; GST("global_store_dwordx2", o, v, ws + i0 * 512, 0, "sc1"); }
    { v2u v = {pk2(k1[0], k1[1]), pk2(k1[2], k1[3])}; GST("global_store_dwordx2", o, v, ws + i1 * 512, 0, "sc1"); }
    VMW(0);
    if (lane < 2) { const u32 of = WS_YF + (u32)((lane ? i1 : i0) * 512 + dch) * 8u; GST("global_store_dwordx2", of, tag, ws, 0, "sc1"); }
  }
  STAMP(10);
  {
    poll(WS_HF + (u32)((mt * 8 + w) * 8 + (lane & 7)) * 8u, ws, tag);
    STAMP(11);
    v4u hf[4][7];
    const u32 ho = (u32)lane * 16u;
#pragma unroll
    for (int f = 0; f < 7; f++)
#pragma unroll
      for (int ml = 0; ml < 4; ml++) {
        const char* hb = ws + WS_H + (size_t)(((4 * mt + (ml ^ (2 * (w >> 2)))) * 8 + w) * 7 + 3) * 1024;
        GLD("global_load_dwordx4", hf[ml][f], ho, hb, (f - 3) * 1024, "sc1");
      }
    v4u xr, b2v;
    { const u32 o = (u32)(mt * 64 + (tid >> 3)) * 1024u + nt * 128 + (tid & 7) * 16; GLD("global_load_dwordx4", xr, o, a.x, 0, "");
      const u32 o2 = nt * 128 + (tid & 7) * 16; GLD("global_load_dwordx4", b2v, o2, a.b2, 0, ""); }
    u32 ta[4];
    { const int q = l15 >> 2, r8 = 4 * (g & 1) + q;
#pragma unroll
      for (int jl = 0; jl < 4; jl++) ta[jl] = reg + (g >> 1) * 1024 + r8 * 128 + 32 * ((jl ^ (w & 3)) ^ (r8 >> 1)) + 8 * (lane & 3); }
    f4 c2[4][4];
#pragma unroll
    for (int i = 0; i < 4; i++)
#pragma unroll
      for (int j = 0; j < 4; j++) c2[i][j] = f4{0, 0, 0, 0};
    v4u wf[3][4];
#pragma unroll
    for (int f = 0; f < 3; f++)
#pragma unroll
      for (int jl = 0; jl < 4; jl++) wf[f][jl] = lds_tr2(ta[jl] + f * 4096, ta[jl] + f * 4096 + 2048);
    LGW0();
#pragma unroll
    for (int f = 0; f < 3; f++)
#pragma unroll
      for (int jl = 0; jl < 4; jl++) TIE(wf[f][jl]);
#pragma unroll
    for (int i = 0; i < 12; i++) dma16(v2, w2b + (16 + i) * 8192, reg + i * 1024);
#pragma unroll
    for (int f = 0; f < 7; f++) {
      if (f < 4) VMW(14 + 4 * (6 - f)); else VMW(4 * (6 - f));
#pragma unroll
      for (int ml = 0; ml < 4; ml++) TIE(hf[ml][f]);
#pragma unroll
      for (int jl = 0; jl < 4; jl++) {
        const int sl = f == 3 ? 3 : f - 4;
        const v4u wa = f < 3 ? wf[f][jl] : lds_tr2(ta[jl] + sl * 4096, ta[jl] + sl * 4096 + 2048);
#pragma unroll
        for (int ml = 0; ml < 4; ml++) c2[jl][ml] = mfma(wa, hf[ml][f], c2[jl][ml]);
      }
    }
    TIE(xr); TIE(b2v);
    STAMP(12);
    poll(WS_YF + (u32)((mt >> 2) * 512 + nt * 64 + (w & 3) * 16 + l15) * 8u, ws, tag);
    v4u yf;
    { const u32 o = WS_Y + (u32)(nt * 64 + (w & 3) * 16 + l15) * 4096u + (u32)(mt * 64 + (w >> 2) * 32 + g * 8) * 2u; GLD("global_load_dwordx4", yf, o, ws, 0, "sc1"); }
    v4u il, ih;
#pragma unroll
    for (int i = 0; i < 4; i++) { const u32 v = ((l15 & 7) >> 1) == i ? (0x3f80u << (16 * (l15 & 1))) : 0u; il[i] = g == (l15 >> 3) ? v : 0u; ih[i] = g == 2 + (l15 >> 3) ? v : 0u; }
    VMW(0); TIE(yf);
    c2[0][0] = mfma(yf, il, c2[0][0]); c2[0][1] = mfma(yf, ih, c2[0][1]);
    STAMP(13);
    LGW0();
#pragma unroll
    for (int jl = 0; jl < 4; jl++)
#pragma unroll
      for (int ml = 0; ml < 4; ml++)
        lds_st<f4>(reg + (((ml ^ (2 * (w >> 2))) * 16 + l15) * 256) + 16 * ((4 * (jl ^ (w & 3)) + g) ^ (l15 & 7)), c2[jl][ml]);
    LGW0(); BAR();
    const int row = tid >> 3, c8 = tid & 7;
    const u32 ra = row * 256 + 16 * ((2 * c8) ^ (row & 7)), rb = ra ^ 16u;
    f4 s0 = lds_ld<f4>(ra), s1 = lds_ld<f4>(rb);
#pragma unroll
    for (int ww = 1; ww < 8; ww++) { s0 += lds_ld<f4>(ra + ww * 16384); s1 += lds_ld<f4>(rb + ww * 16384); }
    v4u o;
    o.x = pk2(s0[0] + bf2f(b2v.x & 0xffffu) + bf2f(xr.x & 0xffffu), s0[1] + bfhi(b2v.x) + bfhi(xr.x));
    o.y = pk2(s0[2] + bf2f(b2v.y & 0xffffu) + bf2f(xr.y & 0xffffu), s0[3] + bfhi(b2v.y) + bfhi(xr.y));
    o.z = pk2(s1[0] + bf2f(b2v.z & 0xffffu) + bf2f(xr.z & 0xffffu), s1[1] + bfhi(b2v.z) + bfhi(xr.z));
    o.w = pk2(s1[2] + bf2f(b2v.w & 0xffffu) + bf2f(xr.w & 0xffffu), s1[3] + bfhi(b2v.w) + bfhi(xr.w));
    const u32 oo = (u32)(mt * 64 + row) * 1024u + nt * 128 + c8 * 16;
    GST("global_store_dwordx4", oo, o, a.out, 0, "");
  }
  STAMP(14);
  if (bid == 0 && w < 4) {
    poll(WS_UF + (u32)(tid * 8) * 8u, ws, tag);
  }
  if (bid == 0) { BAR(); if (tid == 0) { u32 z = 0; GST("global_store_dwordx2", z, tag, ws, 0, "sc1"); } }
  { u64 te; asm volatile("s_memrealtime %0\n\ts_waitcnt lgkmcnt(0)" : "=s"(te)::"memory"); u64 ex = 4*(stv[0]-stv[0]); for(int q_=0;q_<100000;q_++){ u64 tn; asm volatile("s_memrealtime %0\n\ts_waitcnt lgkmcnt(0)" : "=s"(tn)::"memory"); if(tn-te>=ex) break; } }
  asm volatile(".Lfk_end:");
}

extern "C" void kernel_launch(void* const* d_in, const int* in_sizes, int n_in, void* d_out, int out_size, void* d_ws, size_t ws_size, hipStream_t stream) {
  Args a;
  a.x = (const u16*)d_in[0]; a.are = (const u16*)d_in[1]; a.aim = (const u16*)d_in[2]; a.bre = (const u16*)d_in[3]; a.bim = (const u16*)d_in[4];
  a.cre = (const u16*)d_in[5]; a.cim = (const u16*)d_in[6]; a.ldt = (const u16*)d_in[7]; a.dsk = (const u16*)d_in[8];
  a.w1 = (const u16*)d_in[10]; a.b1 = (const u16*)d_in[11]; a.w2 = (const u16*)d_in[12]; a.b2 = (const u16*)d_in[13];
  a.out = (u16*)d_out; a.ws = (char*)d_ws;
  (void)hipFuncSetAttribute((const void*)fused_kernel, hipFuncAttributeMaxDynamicSharedMemorySize, LDS_BYTES);
  fused_kernel<<<256, 512, LDS_BYTES, stream>>>(a);
}
```

```cpp
#include <hip/hip_runtime.h>
#include <stdint.h>

typedef unsigned u32; typedef unsigned long long u64; typedef unsigned short u16;
typedef u32 v4u __attribute__((ext_vector_type(4)));
typedef u32 v2u __attribute__((ext_vector_type(2)));
typedef float f4 __attribute__((ext_vector_type(4)));
typedef float f2 __attribute__((ext_vector_type(2)));
typedef float f16v __attribute__((ext_vector_type(16)));
typedef short s4 __attribute__((ext_vector_type(4)));
typedef __bf16 bf8 __attribute__((ext_vector_type(8)));
typedef __bf16 bf2 __attribute__((ext_vector_type(2)));
#define LAS __attribute__((address_space(3)))
#define DI __device__ __forceinline__

constexpr u32 WS_UF = 4096, WS_HF = 8192, WS_CF = 24576, WS_YF = 32768, WS_S = 65536;
constexpr u32 WS_ST = 81920;
constexpr u32 WS_C = 229376;
constexpr u32 WS_U = 1u << 18, WS_Y = WS_U + (2u << 20), WS_H = WS_Y + (2u << 20);
constexpr u32 SP = 131072, KA = 0, KB = 640, KC = 960, KD = 1504, TBS = 1536;
constexpr u32 LXN = 65536, LSS = 98304, LTS = SP + 3072, LT1 = LTS + 16384, LP = SP + 27648, LWR = SP + 30720;
constexpr u32 LU = SP + 3072, LUS = 5120, LCB = SP + 13312, LLT = SP + 14336, LEX = SP + 16384;
constexpr u32 LDS_BYTES = 163840;

constexpr int rne(int j, int sh) { int q = j >> sh, rem = j & ((1 << sh) - 1), h = 1 << (sh - 1); if (rem > h || (rem == h && (q & 1))) q++; return q << sh; }
constexpr int rnd_bf(int j) { return j <= 256 ? j : rne(j, j < 512 ? 1 : j < 1024 ? 2 : 3); }
constexpr int i1(int j) { return (rne(j, 1) - 256) / 2; }
constexpr int ic(int j) { int p = rnd_bf(j); return p < 1024 ? (p - 512) / 4 : 128 + (p - 1024) / 8; }
constexpr u32 sel(int lo, int hi) { return (u32)(2 * lo) | (u32)(2 * lo + 1) << 8 | (u32)(2 * hi) << 16 | (u32)(2 * hi + 1) << 24; }
struct Tabs { u32 wr[512]; u32 lt[5 * 256]; };
constexpr Tabs make_tabs() {
  Tabs t{};
  for (int tl = 0; tl < 4; tl++) for (int l = 0; l < 64; l++) {
    const int sg = (l & 15) + 16 * (tl & 1), g = l >> 4; int ad[4] = {};
    for (int r = 0; r < 4; r++)
      ad[r] = !(tl >> 1) ? (g < 2 ? KA + 2 * (16 + 8 * sg + 4 * g + r) : g == 2 ? KB + 2 * (4 * sg + r) : KC + 2 * (2 * sg + (r & 1) + 64 * (r >> 1)))
                         : (g == 0 ? KC + 2 * (128 + sg + 32 * r) : (g == 1 && r == 0 && sg == 0) ? KC + 2 * 256 : KD);
    t.wr[(tl * 64 + l) * 2] = (u32)ad[0] | (u32)ad[1] << 16; t.wr[(tl * 64 + l) * 2 + 1] = (u32)ad[2] | (u32)ad[3] << 16;
  }
  for (int l = 0; l < 64; l++) {
    const int o = 16 + (l & 15) - 8 * (l >> 4);
    const int a0 = 16 + o - 7, j1 = 32 * 9 + o, j2 = 32 * 17 + o, j3 = 32 * 33 + o, jx = 32 * 32 + o;
    const int b1 = i1(j1 - 7) & ~1, b2 = ic(j2 - 7) & ~1, b3 = ic(j3 - 7) & ~1, bx = ic(jx - 7) & ~1;
    t.lt[l * 4 + 0] = sel((a0 & 1) + 1, a0 & 1);
    t.lt[l * 4 + 1] = sel(i1(j1 - 4) - b1, i1(j1 - 5) - b1);
    t.lt[l * 4 + 2] = sel(i1(j1 - 6) - b1, i1(j1 - 7) - b1);
    t.lt[l * 4 + 3] = (u32)(KA + 2 * (a0 & ~1)) | (u32)(KB + 2 * b1 - 32) << 16;
    for (int i = 0; i < 4; i++) {
      t.lt[256 + l * 4 + i] = sel(ic(j2 - 2 * i) - b2, ic(j2 - 2 * i - 1) - b2);
      t.lt[512 + l * 4 + i] = sel(ic(j3 - 2 * i) - b3, ic(j3 - 2 * i - 1) - b3);
      t.lt[768 + l * 4 + i] = sel(ic(jx - 2 * i) - bx, ic(jx - 2 * i - 1) - bx);
    }
    t.lt[1024 + l * 4 + 0] = (u32)(KC + 2 * b2 - 16) | (u32)(KC + 2 * b3 - 8) << 16;
    t.lt[1024 + l * 4 + 1] = KC + 2 * bx;
    t.lt[1024 + l * 4 + 2] = l < 32 ? (u32)(KB + 2 * i1(256 + l)) | (u32)(KA + 2 * (16 + 256 + l)) << 16
                           : l < 49 ? (u32)(KC + 2 * ic(512 + 2 * (l - 32))) | (u32)(KB + 2 * (128 + l - 32)) << 16 : (u32)KD | (u32)KD << 16;
    t.lt[1024 + l * 4 + 3] = 0;
  }
  return t;
}
__device__ const Tabs g_tabs = make_tabs();

struct Args { const u16 *are, *aim, *bre, *bim, *cre, *cim, *x, *w1, *w2; char* ws; const u16 *ldt, *dsk, *b1, *b2; u16* out; };

DI float bf2f(u32 v) { return __uint_as_float(v << 16); }
DI float bfhi(u32 v) { return __uint_as_float(v & 0xffff0000u); }
DI u32 pk2(float a, float b) { bf2 c; c[0] = (__bf16)a; c[1] = (__bf16)b; return __builtin_bit_cast(u32, c); }
DI float rbf(float a) { return bf2f(pk2(a, a) & 0xffffu); }
DI f4 mfma(v4u a, v4u b, f4 c) { return __builtin_amdgcn_mfma_f32_16x16x32_bf16(__builtin_bit_cast(bf8, a), __builtin_bit_cast(bf8, b), c, 0, 0, 0); }
DI f16v mfma32(v4u a, v4u b, f16v c) { return __builtin_amdgcn_mfma_f32_32x32x16_bf16(__builtin_bit_cast(bf8, a), __builtin_bit_cast(bf8, b), c, 0, 0, 0); }
DI float dot2(u32 a, u32 b, float c) { return __builtin_amdgcn_fdot2_f32_bf16(__builtin_bit_cast(bf2, a), __builtin_bit_cast(bf2, b), c, false); }
template <class T> DI T lds_ld(u32 a) { return *(const LAS T*)(size_t)a; }
template <class T> DI void lds_st(u32 a, T v) { *(LAS T*)(size_t)a = v; }
DI v2u lds_tr(u32 a) { return __builtin_bit_cast(v2u, __builtin_amdgcn_ds_read_tr16_b64_v4i16((LAS s4*)(size_t)a)); }
DI v4u lds_tr2(u32 a, u32 b) { v2u x = lds_tr(a), y = lds_tr(b); return v4u{x.x, x.y, y.x, y.y}; }
DI v4u lds_2x64(u32 a) { v2u x = lds_ld<v2u>(a); asm volatile(""); v2u y = lds_ld<v2u>(a + 8); return v4u{x.x, x.y, y.x, y.y}; }
DI float xor32_add(float v) { auto r = __builtin_amdgcn_permlane32_swap(__float_as_uint(v), __float_as_uint(v), false, false); return __uint_as_float(r[0]) + __uint_as_float(r[1]); }
#define DPP_ADD(v, ctrl) v += __uint_as_float(__builtin_amdgcn_update_dpp(0u, __float_as_uint(v), ctrl, 0xf, 0xf, true))
DI float sum32(float v) {
  DPP_ADD(v, 0xB1); DPP_ADD(v, 0x4E); DPP_ADD(v, 0x141); DPP_ADD(v, 0x140);
  auto r = __builtin_amdgcn_permlane16_swap(__float_as_uint(v), __float_as_uint(v), false, false); return __uint_as_float(r[0]) + __uint_as_float(r[1]);
}
DI f2 cmul(f2 a, f2 w) {
  f2 t, r;
  asm("v_pk_mul_f32 %0, %1, %2 op_sel:[0,0] op_sel_hi:[1,0]" : "=v"(t) : "v"(a), "v"(w));
  asm("v_pk_fma_f32 %0, %1, %2, %3 op_sel:[1,1,0] op_sel_hi:[0,1,1] neg_lo:[1,0,0]" : "=v"(r) : "v"(a), "v"(w), "v"(t));
  return r;
}
DI f2 cmul_conj(f2 a, f2 w) {
  f2 t, r;
  asm("v_pk_mul_f32 %0, %1, %2 op_sel:[0,0] op_sel_hi:[1,0] neg_hi:[1,0]" : "=v"(t) : "v"(a), "v"(w));
  asm("v_pk_fma_f32 %0, %1, %2, %3 op_sel:[1,1,0] op_sel_hi:[0,1,1] neg_lo:[1,0,0] neg_hi:[1,0,0]" : "=v"(r) : "v"(a), "v"(w), "v"(t));
  return r;
}
DI f2 cexp2(f2 x) {
  float e = __builtin_amdgcn_exp2f(x.x), c = __builtin_amdgcn_cosf(x.y), s = __builtin_amdgcn_sinf(x.y);
  return f2{c, s} * e;
}
DI void ln_stats(float s, float q, float& mb, float& sd) {
  const float mean = s * (1.f / 512.f);
  const float var = fmaxf(q * (1.f / 512.f) - mean * mean, 0.f);
  sd = rbf(__builtin_amdgcn_sqrtf(rbf(rbf(var) + 1e-5f))); mb = rbf(mean);
}
#define VMW(n) asm volatile("s_waitcnt vmcnt(%0)" ::"n"(n) : "memory")
#define LGW0() asm volatile("s_waitcnt lgkmcnt(0)" ::: "memory")
#define BAR() asm volatile("s_barrier" ::: "memory")
#define TIE(x) asm volatile("" : "+v"(x))
#define MARK(x) asm volatile("; PATH_" #x)
#define SB() __builtin_amdgcn_sched_barrier(0)
#define SG(m, n) __builtin_amdgcn_sched_group_barrier(m, n, 0)
#define GLD(op, dst, voff, sbase, imm, fl) asm volatile(op " %0, %1, %2 offset:%3 " fl : "=v"(dst) : "v"(voff), "s"(sbase), "n"(imm) : "memory")
#define GST(op, voff, src, sbase, imm, fl) asm volatile(op " %0, %1, %2 offset:%3 " fl "\n\ts_nop 1" ::"v"(voff), "v"(src), "s"(sbase), "n"(imm) : "memory")
#define M0SET(lbase, limm) asm volatile("s_add_u32 m0, %0, %1\n\ts_nop 0" ::"s"(lbase), "n"(limm) : "memory", "scc")
#define DMA(voff, srd, soff, off) asm volatile("buffer_load_dwordx4 %0, %1, %2 offen offset:%3 lds" ::"v"(voff), "s"(srd), "s"(soff), "n"(off) : "memory")
DI v4u make_srd(const void* p, u32 bytes) { const u64 a_ = (u64)p - 4096; return v4u{(u32)a_, (u32)(a_ >> 32), bytes + 4096, 0x20000u}; }
#define STIE(x) asm volatile("" : "+s"(x))
DI bool tag_miss(v2u f, v2u tag) { return __builtin_expect(__builtin_amdgcn_ballot_w64(f.x != tag.x || f.y != tag.y) != 0, 0); }
DI void poll(u32 voff, const char* ws, v2u tag) {
#pragma nounroll
  for (int it = 0; it < (1 << 16); it++) {
    v2u f;
    asm volatile("global_load_dwordx2 %0, %1, %2 sc1\n\ts_waitcnt vmcnt(0)" : "=v"(f) : "v"(voff), "s"(ws) : "memory");
    if (__builtin_amdgcn_ballot_w64(f.x != tag.x || f.y != tag.y) == 0) break;
    __builtin_amdgcn_s_sleep(6);
  }
}
#define STAMP(i)

struct CvA { u32 ua, a0, a1, a2, a3; };
struct CvS { v4u s01, s2, s3; };
struct ConvRaw { v4u A[8]; u32 e[5], f[3], q[6][2]; };
DI u32 prm(u32 hi, u32 lo, u32 s) { return __builtin_amdgcn_perm(hi, lo, s); }
DI v4u perm4(u32 hi, u32 lo, v4u s) { return v4u{prm(hi, lo, s.x), prm(hi, lo, s.y), prm(hi, lo, s.z), prm(hi, lo, s.w)}; }
DI v4u frag0(const u32 (&e)[5], u32 s) { return v4u{prm(e[4], e[3], s), prm(e[3], e[2], s), prm(e[2], e[1], s), prm(e[1], e[0], s)}; }
DI v4u frag1(const u32 (&f)[3], u32 sa, u32 sb) { return v4u{prm(f[2], f[1], sa), prm(f[2], f[1], sb), prm(f[1], f[0], sa), prm(f[1], f[0], sb)}; }
template <int RR> DI void conv_load(ConvRaw& c, const CvA& a) {
#pragma unroll
  for (int i = 0; i < 5; i++) c.e[i] = lds_ld<u32>(a.a0 + 64 * RR + 4 * i);
#pragma unroll
  for (int i = 7; i >= 1; i -= 2) c.A[i] = lds_ld<v4u>(a.ua + 64 * (3 - RR) + 512 * i);
  if (RR) {
#pragma unroll
    for (int i = 0; i < 3; i++) c.f[i] = lds_ld<u32>(a.a1 + 32 * RR + 4 * i);
  }
#pragma unroll
  for (int i = 6; i >= 0; i -= 2) c.A[i] = lds_ld<v4u>(a.ua + 64 * (3 - RR) + 512 * i);
#pragma unroll
  for (int m = 2; m < 8; m++) if (RR || (m != 2 && m != 4)) {
    const u32 p = m < 4 ? a.a2 + 16 * RR + 128 * (m - 2) : a.a3 + 8 * RR + 64 * (m - 4);
    c.q[m - 2][0] = lds_ld<u32>(p); c.q[m - 2][1] = lds_ld<u32>(p + 4);
  }
}
template <int RR> DI void conv_mma(f4 (&acc)[4], const ConvRaw& c, const CvS& s, v4u x1, v4u x2, v4u x4) {
#pragma unroll
  for (int m = 0; m < 8; m++) {
    const v4u B = m == 0 ? frag0(c.e, s.s01.x) : (RR == 0 && m == 1) ? x1 : (RR == 0 && m == 2) ? x2 : (RR == 0 && m == 4) ? x4 : m == 1 ? frag1(c.f, s.s01.y, s.s01.z)
                : perm4(c.q[m < 2 ? 0 : m - 2][1], c.q[m < 2 ? 0 : m - 2][0], m < 4 ? s.s2 : s.s3);
#pragma unroll
    for (int t = 3; t >= 0; t--) if (2 * t + 1 >= m) acc[t] = mfma(B, c.A[2 * t + 1 - m], acc[t]);
  }
}
#define CONV_SCHED() do { _Pragma("unroll") for (int i_ = 0; i_ < 20; i_++) { SG(0x8, 1); SG(0x100, 1); SG(0x2, 2); } } while (0)

__global__ __launch_bounds__(512, 2) void fused_kernel(Args a) {
  STAMP(0);
  const int tid = threadIdx.x, lane = tid & 63, w = __builtin_amdgcn_readfirstlane(tid >> 6), l15 = lane & 15, g = lane >> 4, l31 = lane & 31, hh = lane >> 5;
  const int bid = blockIdx.x, xcd = bid & 7, idx = bid >> 3;
  const bool gw = __builtin_expect(idx < 28, 1);
  const int j4 = (idx * 37) >> 8, i7 = idx - 7 * j4;
  const int rt = (xcd >> 1) * 4 + j4, ft = (xcd & 1) * 7 + i7, rot = i7 ^ (2 * j4);
  const int mt = xcd * 4 + (idx >> 3), nt = idx & 7;
  const int ch = w >> 2, dch = 2 * bid + ch;
  const int oct = xcd * 32 + idx;
  const bool tw = ((w >> 1) ^ ch) & 1;
  const u32 w4k = (u32)w * 4096u;
  u32 vx0 = (u32)(rt * 128 + w * 16 + (lane >> 3)) * 1024u + 16u * ((lane & 7) ^ (lane >> 4)) + 4096u, vx1 = (vx0 ^ 64u) + 8192u - 1024u;
  u32 vw0 = (u32)(8 * w + g) * 3584u + 256u * ft + 16u * (l15 ^ (4 * g)) + 4096u - 2048u, vw1 = vw0 + 4 * 3584 - 1024;
  u32 sx0 = rot * 128, sw0 = rot * (64 * 3584), sw1 = (rot ^ 1) * (64 * 3584);
  const int lr = 4 * ch + 2 * (w & 1) + hh;
  u32 xo = (u32)(oct * 8 + lr) * 1024u + (u32)l31 * 16u;
  TIE(vx0); TIE(vx1); TIE(vw0); TIE(vw1); TIE(xo); STIE(sx0); STIE(sw0); STIE(sw1);
  if (w < 3) {
    v4u pp; const u32 ko = w * 16, o = (u32)bid * 512u + (u32)lane * 16u, lp = LP + w * 1024;
    asm volatile("s_load_dwordx4 %0, %1, %2\n\ts_waitcnt lgkmcnt(0)" : "=s"(pp) : "s"(__builtin_amdgcn_kernarg_segment_ptr()), "s"(ko) : "memory");
    const u64 p0 = ((u64)pp.y << 32) | pp.x, p1 = ((u64)pp.w << 32) | pp.z;
    if (lane < 32) asm volatile("s_mov_b32 m0, %3\n\ts_nop 0\n\tglobal_load_lds_dwordx4 %0, %1\n\ts_add_u32 m0, m0, 512\n\ts_nop 0\n\tglobal_load_lds_dwordx4 %0, %2"
                                ::"v"(o), "s"(p0), "s"(p1), "s"(lp) : "memory", "scc");
  } else if (w >= 6) { const u32 o = (u32)(tid - 384) * 16u, d = LWR + (w - 6) * 1024;
    asm volatile("s_mov_b32 m0, %2\n\ts_nop 0\n\tglobal_load_lds_dwordx4 %0, %1" ::"v"(o), "s"(g_tabs.wr), "s"(d) : "memory");
  } else {
    u64 pc; u32 len; asm volatile("s_getpc_b64 %0\n.Lfk_beg:\n\ts_mov_b32 %1, .Lfk_end-.Lfk_beg" : "=s"(pc), "=s"(len));
    const u32 ln = (idx & 15) * 3 + w - 3, o = min((ln * 4 + (lane & 3)) * 128u, len), o2 = min(ln, 39u) * 128u;
    if (lane < 4) asm volatile("s_mov_b32 m0, %4\n\ts_nop 0\n\tglobal_load_lds_dword %0, %1\n\tglobal_load_lds_dword %2, %3" ::"v"(o), "s"(pc), "v"(o2), "s"(g_tabs.lt), "n"(SP + KD) : "memory");
  }
  const char* ws = a.ws;
  u64 ep; u32 sl, sk_;
  { const u32 b4 = bid * 4;
    asm volatile("s_load_dwordx2 %0, %3, 0x0\n\ts_load_dword %1, %4, %6\n\ts_load_dword %2, %5, %6" : "=&s"(ep), "=&s"(sl), "=&s"(sk_) : "s"(ws), "s"(a.ldt), "s"(a.dsk), "s"(b4) : "memory"); }
  v4u xr0, xr1;
  if (tw) { MARK(KT); GLD("global_load_dwordx4", xr0, xo, a.x, 0, ""); GLD("global_load_dwordx4", xr1, xo, a.x, 512, ""); }
  const v4u rx = make_srd(a.x, 2048 * 1024), rw = make_srd(a.w1, 512 * 3584), r2 = make_srd(a.w2, 1792 * 1024);
#define XS(c) (u32)(((c) ^ rot) * 128)
#define WS_(c) (u32)(((c) ^ rot) * (64 * 3584))
#define DMA_X(c) do { DMA(vx0, rx, XS(c), 0); DMA(vx1, rx, XS(c), 1024); } while (0)
#define DMA_W(c) do { DMA(vw0, rw, WS_(c), 2048); DMA(vw1, rw, WS_(c), 3072); } while (0)
  if (gw) { MARK(GW); M0SET(w4k, 0); DMA(vx0, rx, sx0, 0); DMA(vx1, rx, sx0, 1024); DMA(vw0, rw, sw0, 2048); DMA(vw1, rw, sw0, 3072); M0SET(w4k, 32768); DMA(vw0, rw, sw1, 2048); DMA(vw1, rw, sw1, 3072); }
  const int cg = (xcd & 1) * 14 + min((xcd >> 1) * 4 + (idx - 28), 13);
  v4u wr_[6];
  if (!gw) { MARK(LN); const u32 o2 = (u32)(64 * w + (lane >> 3)) * 3584u + 128u * cg + 16u * (lane & 7);
#pragma unroll
    for (int i = 0; i < 6; i++) GLD("global_load_dwordx4", wr_[i], o2, (const char*)a.w1 + i * 8 * 3584, 0, ""); }
  STAMP(16);
  const int mw = w & 3, nh = w >> 2, q4 = l15 >> 2;
  u32 xa[2][4], wa[2][2], ksa, kta;
  { const int row = 32 * mw + l31;
#pragma unroll
    for (int r = 0; r < 4; r++) { xa[0][r] = (row >> 4) * 4096 + (row & 15) * 128 + 16 * ((2 * r + hh) ^ ((row >> 1) & 7)); TIE(xa[0][r]); }
#pragma unroll
    for (int nl = 0; nl < 2; nl++) { wa[0][nl] = hh * 4096 + 2048 + q4 * 256 + 64 * ((2 * nh + nl) ^ q4) + 32 * (g & 1) + 8 * (lane & 3); TIE(wa[0][nl]); }
    const u32 o = (2 * g + (q4 >> 1)) * 64 + 8 * (lane & 1), o1 = o + 16 * (((lane >> 1) & 3) ^ g);
    ksa = LSS + ch * 16384 + (w & 1) * 8192 + o1;
    kta = (w & 2) ? LT1 + o + 16 * ((2 * ch + (q4 & 1)) ^ g) : LTS + ch * 8192 + o1;
    TIE(ksa); TIE(kta); }
  u32 t2o = (u32)((bid * 8 + w) * 7 + min(lane, 6)) * 128u, uso = WS_U + (u32)(tid >> 1) * 8192u + (u32)(oct * 8 + 4 * (tid & 1)) * 4u;
  TIE(t2o); TIE(uso);
  asm volatile("s_waitcnt lgkmcnt(0)" : "+s"(ep), "+s"(sl), "+s"(sk_)::"memory");
  STAMP(17);
  float dt = rbf(expf(ch ? bfhi(sl) : bf2f(sl))); TIE(dt);
  float k00 = __uint_as_float((tid & 255) == 0 ? (ch ? sk_ & 0xffff0000u : sk_ << 16) : 0u); TIE(k00);
  SB();
  if (w < 2) VMW(6); else if (w == 2) VMW(8);
  STAMP(19);
  BAR();
  STAMP(1);
  if (gw) { MARK(GW); DMA_X(1); }
  if (lane < 7) asm volatile("s_mov_b32 m0, %2\n\ts_nop 0\n\tglobal_load_lds_dword %0, %1" ::"v"(t2o), "s"(a.w2), "n"(SP + KD) : "memory");

  const u32 tb = SP + (u32)ch * TBS;
  {
    const int md = (w & 1) * 64 + lane;
    const u32 pp = LP + (u32)(ch * 128 + md) * 2u, dk = md * 64 + 16 * ((lane >> 1) & 3);
    float arl = dt * bf2f(lds_ld<u16>(pp)) * 1.4426950408889634f; arl = fminf(fmaxf(arl, -1e30f), 1e30f);
    float f = dt * bf2f(lds_ld<u16>(pp + 512)) * 0.15915494309189535f; f -= rintf(f);
    const f2 af = {arl, f};
    if (!tw) {
      MARK(KS);
      const float br = dt * bf2f(lds_ld<u16>(pp + 1024)), bi = dt * bf2f(lds_ld<u16>(pp + 1536)), cr = bf2f(lds_ld<u16>(pp + 2048)), ci = bf2f(lds_ld<u16>(pp + 2560));
      const f2 z8 = cexp2(af * 8.f), z16 = cmul(z8, z8);
      f2 g0 = {cr * br - ci * bi, cr * bi + ci * br}, g1 = cmul(g0, z8);
      f2 zi = {-z16.y, z16.y}; TIE(zi);
      const u32 d = LSS + ch * 16384 + dk;
#pragma unroll
      for (int c = 0; c < 4; c++) {
        v4u re, im;
#pragma unroll
        for (int i = 0; i < 4; i++) { re[i] = pk2(g0.x, g1.x); im[i] = pk2(g0.y, g1.y);
          if (c < 3 || i < 3) { g0 = f2{g0.y, g0.x} * zi + g0 * z16.x; g1 = f2{g1.y, g1.x} * zi + g1 * z16.x; } }
        lds_st<v4u>((d ^ (16 * (c & 1))) + (c >> 1) * 8192, re); lds_st<v4u>((d ^ (32 + 16 * (c & 1))) + (c >> 1) * 8192, im);
      }
    } else {
      MARK(KT);
      const f2 z1 = cexp2(af), y1 = cexp2(af * 256.f);
      const f2 z2 = cmul(z1, z1), y2 = cmul(y1, y1), z3 = cmul(z2, z1), z4 = cmul(z2, z2), y3 = cmul(y2, y1), y4 = cmul(y2, y2), z5 = cmul(z4, z1), z6 = cmul(z4, z2), z7 = cmul(z4, z3);
      const u32 d = LTS + ch * 8192 + dk, d1 = (LT1 + dk) ^ (32 * ch);
      lds_st<v4u>(d, v4u{pk2(1.f, z1.x), pk2(z2.x, z3.x), pk2(z4.x, z5.x), pk2(z6.x, z7.x)});
      lds_st<v4u>(d ^ 32, v4u{pk2(0.f, -z1.y), pk2(-z2.y, -z3.y), pk2(-z4.y, -z5.y), pk2(-z6.y, -z7.y)});
      const f2 a2 = cmul(y1, z2), a4 = cmul(y1, z4), a6 = cmul(y1, z6), b4 = cmul(y2, z4), c4 = cmul(y3, z4);
      lds_st<v4u>(d ^ 16, v4u{pk2(y1.x, a2.x), pk2(a4.x, a6.x), pk2(y2.x, b4.x), pk2(y3.x, c4.x)});
      lds_st<v4u>(d ^ 48, v4u{pk2(-y1.y, -a2.y), pk2(-a4.y, -a6.y), pk2(-y2.y, -b4.y), pk2(-y3.y, -c4.y)});
      const f2 y5 = cmul(y4, y1), y6 = cmul(y4, y2), y7 = cmul(y4, y3), y8 = cmul(y4, y4);
      lds_st<v4u>(d1, v4u{pk2(y4.x, y5.x), pk2(y6.x, y7.x), pk2(y8.x, 0.f), 0u});
      lds_st<v4u>(d1 ^ 16, v4u{pk2(-y4.y, -y5.y), pk2(-y6.y, -y7.y), pk2(-y8.y, 0.f), 0u});
      SB(); VMW(7);
      TIE(xr0); TIE(xr1);
      float ss = 0.f, sq = 0.f, ss2 = 0.f, sq2 = 0.f;
#pragma unroll
      for (int e = 0; e < 4; e++) { const u32 v = xr0[e], v1 = xr1[e]; ss = dot2(v, 0x3f803f80u, ss); sq = dot2(v, v, sq); ss2 = dot2(v1, 0x3f803f80u, ss2); sq2 = dot2(v1, v1, sq2); }
      ss = sum32(ss + ss2); sq = sum32(sq + sq2);
      float mb, sd; ln_stats(ss, sq, mb, sd);
      if (l31 == 0) { const u32 o = WS_S + (u32)(oct * 8 + lr) * 8u; const v2u v = {pk2(-mb, -mb), pk2(-mb, sd)}; GST("global_store_dwordx2", o, v, ws, 0, "sc1"); }
      const float rstd = __builtin_amdgcn_rcpf(sd), nmr = -mb * rstd;
      v4u o0, o1;
#pragma unroll
      for (int e = 0; e < 4; e++) { const u32 v = xr0[e], v1 = xr1[e]; const f2 t = f2{bf2f(v), bfhi(v)} * rstd + nmr, t1 = f2{bf2f(v1), bfhi(v1)} * rstd + nmr; o0[e] = pk2(t.x, t.y); o1[e] = pk2(t1.x, t1.y); }
      lds_st<v4u>(LXN + lr * 1040 + l31 * 16, o0); lds_st<v4u>(LXN + lr * 1040 + 512 + l31 * 16, o1);
    }
  }
  STAMP(2);
#define LT_DMA() do { const u32 d_ = LLT + min(w, 5) * 1024, o_ = (u32)min(tid, 319) * 16u; asm volatile("s_mov_b32 m0, %2\n\ts_nop 0\n\tglobal_load_lds_dwordx4 %0, %1" ::"v"(o_), "s"(g_tabs.lt), "s"(d_) : "memory"); } while (0)
  if (w >= 6) VMW(7);
  LGW0(); BAR();
  STAMP(18);
  {
    const v2u wa_ = lds_ld<v2u>(LWR + (tid & 255) * 8);
    const u32 ra = LXN + (tid & 1) * 4160 + (tid >> 1) * 4;
    const v4u un = {lds_ld<u32>(ra), lds_ld<u32>(ra + 1040), lds_ld<u32>(ra + 2080), lds_ld<u32>(ra + 3120)};
    v4u A[8], B[8];
#pragma unroll
    for (int ks = 0; ks < 8; ks++) { A[ks] = lds_tr2(kta + ks * 1024, kta + ks * 1024 + 512); B[ks] = lds_tr2(ksa + ks * 1024, ksa + ks * 1024 + 512); }
    SB();
    GST("global_store_dwordx4", uso, un, ws, 0, "sc1");
    f4 k0 = {k00, 0, 0, 0}, k1 = {0, 0, 0, 0};
#pragma unroll
    for (int ks = 0; ks < 8; ks++) { if (ks & 1) k1 = mfma(A[ks], B[ks], k1); else k0 = mfma(A[ks], B[ks], k0); }
    k0 += k1;
    const u32 p0 = pk2(k0[0], k0[1]), p1 = pk2(k0[2], k0[3]);
    if (lane < ((w & 2) ? 17 : 64)) {
      lds_st<u16>(tb + (wa_.x & 0xffffu), (u16)p0); lds_st<u16>(tb + (wa_.x >> 16), (u16)(p0 >> 16));
      lds_st<u16>(tb + (wa_.y & 0xffffu), (u16)p1); lds_st<u16>(tb + (wa_.y >> 16), (u16)(p1 >> 16)); }
  }
  v2u tag;
  { u64 z = (ep + 0x9E3779B97F4A7C15ull) * 0xBF58476D1CE4E5B9ull; z ^= z >> 29; tag.x = (u32)z; tag.y = (u32)(z >> 32); }
  u32 v2[4], ta2[2], ta2h[2];
  const u32 seq = (u32)(0x65432106543210ull >> (4 * (nt == 7 ? 3 : nt)));
#define FR(f) ((seq >> (4 * (f))) & 7u)
#define W2_SETUP_A() do { _Pragma("unroll") for (int i = 0; i < 4; i++) { v2[i] = (u32)(224 * w + (lane >> 3)) * 1024u + 128u * nt + 16u * ((lane & 7) ^ (4 * (g & 1))) + i * (8192 - 1024) + 4096u; TIE(v2[i]); } } while (0)
#define W2_SETUP_B() do { _Pragma("unroll") for (int nl = 0; nl < 2; nl++) { ta2[nl] = w4k + (4 * hh + q4) * 128 + 64 * ((nl ^ (w & 1)) ^ ((q4 >> 1) & 1)) + 32 * (g & 1) + 8 * (lane & 3); ta2h[nl] = ta2[nl] + 65536; TIE(ta2[nl]); TIE(ta2h[nl]); } } while (0)
#define DMA_2I(f, i) DMA(v2[i], r2, FR(f) << 15, (i) * 1024)
#define DMA_2(f) do { M0SET(w4k, ((f) & 3) * 32768); DMA_2I(f, 0); DMA_2I(f, 1); DMA_2I(f, 2); DMA_2I(f, 3); } while (0)
  v4u wf2[3][2][2];
#define W2FRAG(slot, s2, nl) lds_tr2(((slot) & 2 ? ta2h[nl] : ta2[nl]) + ((slot) & 1) * 32768 + (s2) * 2048, ((slot) & 2 ? ta2h[nl] : ta2[nl]) + ((slot) & 1) * 32768 + (s2) * 2048 + 1024)
#define W2_READ(f) do { _Pragma("unroll") for (int s_ = 0; s_ < 2; s_++) _Pragma("unroll") for (int n_ = 0; n_ < 2; n_++) wf2[f][s_][n_] = W2FRAG(f, s_, n_); } while (0)
#define W2_TIE(f) do { LGW0(); _Pragma("unroll") for (int s_ = 0; s_ < 2; s_++) _Pragma("unroll") for (int n_ = 0; n_ < 2; n_++) TIE(wf2[f][s_][n_]); } while (0)
#define K_PAD() do { if (w < 2) lds_st<v4u>(lane < 62 ? LU + w * LUS + lane * 16 : SP + w * TBS + (lane - 62) * 16, v4u{0, 0, 0, 0}); } while (0)
#define K_FIX() do { if ((w & 3) == 3) { const u32 fx_ = lds_ld<u32>(LLT + 4096 + lane * 16 + 8); lds_st<u16>(tb + (fx_ >> 16), lds_ld<u16>(tb + (fx_ & 0xffffu))); } } while (0)
#define U_FLAG() do { if (tid == 448) { const u32 o_ = WS_UF + (u32)oct * 8u; GST("global_store_dwordx2", o_, tag, ws, 0, "sc1"); } } while (0)
  const u32 uo_ = WS_U + (u32)bid * 8192u + (u32)tid * 16u;
  v4u uu;
  u32 hfo = 0;

  if (gw) {
    MARK(GW);
    f16v acc[2];
    v4u xf[2][2], wf[2][2][2];
    v2u fl;
#define G1_RD(st, r_) do { constexpr int p_ = ((st) >> 2) & 1; constexpr u32 sb_ = (((st) >> 1) & 1) * 32768, wo_ = sb_ + ((st) & 1) * 16384 + (r_) * 8192; \
      xf[(st) & 1][r_] = lds_ld<v4u>(xa[p_][2 * ((st) & 1) + (r_)] + sb_); \
      wf[(st) & 1][0][r_] = lds_tr2(wa[p_][0] + wo_, wa[p_][0] + wo_ + 1024); wf[(st) & 1][1][r_] = lds_tr2(wa[p_][1] + wo_, wa[p_][1] + wo_ + 1024); } while (0)
#define G1_MM(st, n_, r_) acc[n_] = mfma32(wf[(st) & 1][n_][r_], xf[(st) & 1][r_], (st) + (r_) == 0 ? f16v{0} : acc[n_])
#define G1_STEP(st, X2, X3) do { \
      G1_MM(st, 0, 0); if ((st) < 15) G1_RD((st) + 1, 0); SG(0x8, 1); SG(0x100, 5); SB(); \
      G1_MM(st, 1, 0); if ((st) < 15) G1_RD((st) + 1, 1); SG(0x8, 1); SG(0x100, 5); SB(); \
      G1_MM(st, 0, 1); SB(); X2; SB(); \
      G1_MM(st, 1, 1); SB(); X3; SB(); } while (0)
#define G1_SYNC(NV) do { VMW(NV); LGW0(); BAR(); SB(); } while (0)
    v2u st2, cq0, cq1; u32 afo;
    G1_SYNC(6);
    STAMP(3);
    G1_RD(0, 0); G1_RD(0, 1); SB();
    M0SET(w4k, 65536); DMA_X(2);
    G1_STEP(0, DMA_W(2), { M0SET(w4k, 98304); DMA_X(3); });
    G1_SYNC(8); STAMP(20);
    G1_STEP(1, { DMA_W(3); _Pragma("unroll") for (int r = 0; r < 4; r++) { xa[1][r] = xa[0][r] + 65536; TIE(xa[1][r]); } },
               { M0SET(w4k, 0); DMA_X(4); wa[1][0] = wa[0][0] + 65536; wa[1][1] = wa[0][1] + 65536; TIE(wa[1][0]); TIE(wa[1][1]); });
    G1_STEP(2, DMA(vw0, rw, WS_(4), 2048), DMA(vw1, rw, WS_(4), 3072));
    G1_SYNC(8); STAMP(21);
    G1_STEP(3, { U_FLAG(); M0SET(w4k, 32768); DMA(vx0, rx, XS(5), 0); }, DMA(vx1, rx, XS(5), 1024));
    G1_STEP(4, DMA(vw0, rw, WS_(5), 2048), DMA(vw1, rw, WS_(5), 3072));
    G1_SYNC(8); STAMP(22);
    G1_STEP(5, { M0SET(w4k, 65536); DMA(vx0, rx, XS(6), 0); W2_SETUP_A(); }, { DMA(vx1, rx, XS(6), 1024); K_PAD(); });
    G1_STEP(6, { DMA(vw0, rw, WS_(6), 2048); }, { DMA(vw1, rw, WS_(6), 3072); LT_DMA(); });
    G1_SYNC(9); STAMP(23);
    G1_STEP(7, { M0SET(w4k, 98304); DMA(vx0, rx, XS(7), 0); W2_SETUP_B(); },
               { DMA(vx1, rx, XS(7), 1024); afo = hh ? ((lane & 4) ? WS_CF + (u32)(2 * ft + nh) * 8u : WS_UF + (u32)(rt * 16 + mw * 4 + (lane & 3)) * 8u) : WS_UF + (u32)(32 * w + lane) * 8u; TIE(afo); });
    G1_STEP(8, DMA(vw0, rw, WS_(7), 2048), DMA(vw1, rw, WS_(7), 3072));
    G1_SYNC(9); STAMP(24);
    G1_STEP(9, { GLD("global_load_dwordx2", fl, afo, ws, 0, "sc1"); M0SET(w4k, 0); DMA_2I(0, 0); }, DMA_2I(0, 1));
    G1_STEP(10, DMA_2I(0, 2), DMA_2I(0, 3));
    G1_SYNC(9); STAMP(25);
    const u32 so = WS_S + (u32)(rt * 128 + mw * 32 + l31) * 8u, co = WS_C + (u32)(ft * 128 + nh * 64 + l31) * 8u;
    G1_STEP(11, K_FIX(), { VMW(4); TIE(fl); if (tag_miss(fl, tag)) { MARK(SLOW); poll(afo, ws, tag); }
                           GLD("global_load_dwordx2", st2, so, ws, 0, "sc1"); GLD("global_load_dwordx2", cq0, co, ws, 0, "sc1"); });
    G1_STEP(12, { GLD("global_load_dwordx2", cq1, co, ws, 256, "sc1"); GLD("global_load_dwordx4", uu, uo_, ws, 0, "sc1"); }, { M0SET(w4k, 32768); DMA_2I(1, 0); DMA_2I(1, 1); });
    G1_SYNC(63); STAMP(26);
    G1_STEP(13, { DMA_2I(1, 2); DMA_2I(1, 3); }, { M0SET(w4k, 65536); DMA_2I(2, 0); DMA_2I(2, 1); });
    G1_STEP(14, DMA_2I(2, 2), DMA_2I(2, 3));
    LGW0(); BAR(); SB();
    G1_STEP(15, {}, {});
    VMW(8); TIE(st2); TIE(cq0); TIE(cq1); TIE(uu);
    STAMP(4);
    const float sd = bfhi(st2.y);
    { const u32 km = hh ? 0u : ~0u; const v4u sb = {st2.x & km, st2.y & km, 0u, 0u};
      acc[0] = mfma32(v4u{cq0.x, cq0.y, 0u, 0u}, sb, acc[0]); acc[1] = mfma32(v4u{cq1.x, cq1.y, 0u, 0u}, sb, acc[1]); }
    W2_READ(0);
    DMA_2(3);
    const float rstd = __builtin_amdgcn_rcpf(sd), k1 = -2.3022082f * rstd, k2 = -0.10294324f * rstd * rstd * rstd;
    const int M32 = rt * 4 + mw;
    const char* hb = ws + WS_H + (size_t)(M32 * 112 + ft * 8 + nh * 4) * 1024;
    const u32 ho_ = (u32)lane * 16u;
#pragma unroll
    for (int nl = 0; nl < 2; nl++) {
#pragma unroll
      for (int s = 0; s < 2; s++) {
        v4u hp;
#pragma unroll
        for (int e = 0; e < 4; e++) {
          const f2 v = {acc[nl][8 * s + 2 * e], acc[nl][8 * s + 2 * e + 1]};
          const f2 z = v * (v * v * k2 + k1);
          const f2 d = f2{__builtin_amdgcn_exp2f(z.x), __builtin_amdgcn_exp2f(z.y)} * sd + sd;
          const f2 o = v * f2{__builtin_amdgcn_rcpf(d.x), __builtin_amdgcn_rcpf(d.y)};
          hp[e] = pk2(o.x, o.y);
        }
        GST("global_store_dwordx4", ho_, hp, hb, nl * 2048 + s * 1024, "sc1");
        if (nl == 0 && s == 0) { W2_TIE(0); DMA_2(4); }
      }
    }
    hfo = WS_HF + (u32)(M32 * 32 + ft * 2 + nh) * 8u;
    STAMP(5);
    VMW(12);
  } else {
    MARK(LN);
    v4u wr6, wr7; u32 pb1;
    { const u32 o2 = (u32)(64 * w + (lane >> 3)) * 3584u + 128u * cg + 16u * (lane & 7);
      GLD("global_load_dwordx4", wr6, o2, (const char*)a.w1 + 48 * 3584, 0, ""); GLD("global_load_dwordx4", wr7, o2, (const char*)a.w1 + 56 * 3584, 0, "");
      const u32 o3 = (u32)(cg * 64 + lane) * 2u; GLD("global_load_ushort", pb1, o3, a.b1, 0, ""); }
    W2_SETUP_A(); W2_SETUP_B();
    VMW(0); TIE(pb1);
    { v4u se; f4 ca = {0, 0, 0, 0};
#pragma unroll
      for (int i = 0; i < 4; i++) se[i] = (l15 >> 1) == i ? (0x3f80u << (16 * (l15 & 1))) : 0u;
#pragma unroll
      for (int i = 0; i < 6; i++) { TIE(wr_[i]); ca = mfma(se, wr_[i], ca); }
      TIE(wr6); TIE(wr7); ca = mfma(se, wr6, ca); ca = mfma(se, wr7, ca);
      if (lane < 32) lds_st<f4>((w * 2 + (l15 >> 3)) * 256 + (lane & 7) * 32 + g * 16, ca); }
    LGW0(); BAR();
    U_FLAG();
    if (w == 0) {
      float c = 0.f;
#pragma unroll
      for (int i = 0; i < 16; i++) c += lds_ld<float>(i * 256 + lane * 4);
      const float c0 = rbf(c), c1 = rbf(c - c0), c2 = c - c0 - c1;
      const u32 o = WS_C + (u32)(cg * 64 + lane) * 8u; const v2u v = {pk2(c0, c1), pk2(c2, bf2f(pb1))}; GST("global_store_dwordx2", o, v, ws, 0, "sc1");
      VMW(0);
      if (lane == 0) { const u32 o_ = WS_CF + (u32)cg * 8u; GST("global_store_dwordx2", o_, tag, ws, 0, "sc1"); }
    }
    STAMP(4);
    LT_DMA(); K_PAD();
    DMA_2(0); DMA_2(1); DMA_2(2); DMA_2(3);
    poll(WS_UF + (u32)(32 * w + l31) * 8u, ws, tag);
    GLD("global_load_dwordx4", uu, uo_, ws, 0, "sc1");
    W2_READ(0); W2_TIE(0); DMA_2(4);
    BAR();
    K_FIX();
    STAMP(5);
    VMW(4); TIE(uu);
  }
  v4u hf[14][2];
  const u32 ho = (u32)lane * 16u;
  const v4u rh_ = make_srd(ws + WS_H, 64 * 112 * 1024);
  const u32 hv0 = (u32)((2 * mt + (w >> 2)) * 112 + 14 * w) * 1024u + ho + 4096u, hv1 = (u32)((2 * mt + 1 - (w >> 2)) * 112 + 14 * w) * 1024u + ho + 4096u;
#define H_LD(s, ml) asm volatile("buffer_load_dwordx4 %0, %1, %2, %3 offen offset:%4 sc1" : "=v"(hf[s][ml]) : "v"((ml) ? hv1 : hv0), "s"(rh_), "s"(FR((s) >> 1) << 11), "n"(((s) & 1) * 1024) : "memory")
  u32 yfo;
  {
    const int th = w & 1, rh = (w >> 1) & 1;
    lds_st<v2u>(LU + 992 + tid * 8, v2u{prm(uu.y, uu.x, 0x05040100u), prm(uu.w, uu.z, 0x05040100u)});
    lds_st<v2u>(LU + LUS + 992 + tid * 8, v2u{prm(uu.y, uu.x, 0x07060302u), prm(uu.w, uu.z, 0x07060302u)});
    CvS cs_; cs_.s01 = lds_ld<v4u>(LLT + ho); cs_.s2 = lds_ld<v4u>(LLT + 1024 + ho); cs_.s3 = lds_ld<v4u>(LLT + 2048 + ho);
    v4u sx = lds_ld<v4u>(LLT + 3072 - 1024 * rh + ho); v2u ad = lds_ld<v2u>(LLT + 4096 + ho);
    W2_READ(1); W2_READ(2);
    W2_TIE(1); W2_TIE(2); TIE(cs_.s01); TIE(cs_.s2); TIE(cs_.s3); TIE(sx); TIE(ad);
    BAR();
    STAMP(8);
    f4 ca[4] = {{0, 0, 0, 0}, {0, 0, 0, 0}, {0, 0, 0, 0}, {0, 0, 0, 0}};
    CvA ca_;
    ca_.ua = LU + (u32)ch * LUS + 448 - 192 - 256 * rh + 512 * th + 32 * l15 + 16 * g;
    ca_.a0 = tb + (cs_.s01.w & 0xffffu) + 256 * rh; ca_.a1 = tb + (cs_.s01.w >> 16) + 128 * rh; ca_.a2 = tb + (ad.x & 0xffffu) + 64 * rh; ca_.a3 = tb + (ad.x >> 16) + 32 * rh;
    ConvRaw c0, c1;
    v4u x1, x2, x4;
    conv_load<0>(c0, ca_);
    { const u32 ax = rh ? ca_.a3 : tb + ad.y; x4 = perm4(lds_ld<u32>(ax + 4), lds_ld<u32>(ax), sx); }
    if (rh) {
      u32 f[3]; for (int i = 0; i < 3; i++) f[i] = lds_ld<u32>(ca_.a1 + 4 * i);
      x1 = frag1(f, cs_.s01.y, cs_.s01.z); x2 = perm4(lds_ld<u32>(ca_.a2 + 4), lds_ld<u32>(ca_.a2), cs_.s2);
    } else {
      u32 e[5], f[3]; for (int i = 0; i < 5; i++) e[i] = lds_ld<u32>(ca_.a0 + 512 + 4 * i); for (int i = 0; i < 3; i++) f[i] = lds_ld<u32>(ca_.a1 + 256 + 4 * i);
      x1 = frag0(e, cs_.s01.x); x2 = frag1(f, cs_.s01.y, cs_.s01.z);
    }
    SB();
    VMW(0);
    if (gw && lane == 0) GST("global_store_dwordx2", hfo, tag, ws, 0, "sc1");
    STAMP(6);
    DMA_2(5); SB();
    conv_load<1>(c1, ca_); conv_mma<0>(ca, c0, cs_, x1, x2, x4); CONV_SCHED(); SB();
    DMA_2(6); SB();
    conv_load<2>(c0, ca_); conv_mma<1>(ca, c1, cs_, x1, x2, x4); CONV_SCHED(); SB();
    v2u fl; const u32 hpo = WS_HF + (u32)((2 * mt + ((lane >> 2) & 1)) * 32 + ((7 * w) >> 1) + (lane & 3)) * 8u;
    GLD("global_load_dwordx2", fl, hpo, ws, 0, "sc1"); SB();
    conv_load<3>(c1, ca_); conv_mma<2>(ca, c0, cs_, x1, x2, x4); CONV_SCHED(); SB();
    STAMP(28);
    VMW(0); TIE(fl);
    if (!tag_miss(fl, tag)) {
      const u32 o_ = ((lane & 16) ? hv1 : hv0) - ho + l15 * 128;
      if (lane < 32) asm volatile("s_mov_b32 m0, %3\n\ts_nop 0\n\tbuffer_load_dword %0, %1, %2 offen sc1 lds" ::"v"(o_), "s"(rh_), "s"(FR(0) << 11), "n"(LCB) : "memory");
    }
    SB();
    conv_mma<3>(ca, c1, cs_, x1, x2, x4); SB();
    GLD("global_load_dwordx2", fl, hpo, ws, 0, "sc1"); SB();
    STAMP(9);
    f4 k0, k1;
    if (rh) { lds_st<f4>(LEX + w * 2048 + ho, ca[0]); lds_st<f4>(LEX + w * 2048 + 1024 + ho, ca[1]); k0 = ca[2]; k1 = ca[3]; }
    else { lds_st<f4>(LEX + w * 2048 + ho, ca[2]); lds_st<f4>(LEX + w * 2048 + 1024 + ho, ca[3]); k0 = ca[0]; k1 = ca[1]; }
    LGW0(); BAR();
    k0 += lds_ld<f4>(LEX + (w ^ 2) * 2048 + ho); k1 += lds_ld<f4>(LEX + (w ^ 2) * 2048 + 1024 + ho);
    const int i0 = 4 * rh + th;
    const u32 o = WS_Y + (u32)dch * 4096u + i0 * 512 + 32 * l15 + 8 * g;
    { v2u v = {pk2(k0[0], k0[1]), pk2(k0[2], k0[3])}; GST("global_store_dwordx2", o, v, ws, 0, "sc1"); }
    { v2u v = {pk2(k1[0], k1[1]), pk2(k1[2], k1[3])}; GST("global_store_dwordx2", o, v, ws, 1024, "sc1"); }
    yfo = WS_YF + (u32)((i0 + 2 * (lane & 1)) * 512 + dch) * 8u;
    STAMP(10);
    VMW(2); TIE(fl);
    if (tag_miss(fl, tag)) { MARK(SLOW); poll(hpo, ws, tag); }
    STAMP(11);
  }
  {
#pragma unroll
    for (int s = 0; s < 4; s++) { H_LD(s, 0); H_LD(s, 1); }
    f16v c2[2][2];
#pragma unroll
    for (int i = 0; i < 16; i++) { c2[0][0][i] = 0.f; c2[0][1][i] = 0.f; c2[1][0][i] = 0.f; c2[1][1][i] = 0.f; }
    v4u wn[2][2], xr, b2v, yf; v2u fl;
    const u32 ypo = WS_YF + (u32)((mt >> 2) * 512 + nt * 64 + (w & 1) * 32 + l31) * 8u;
    const u32 oo = (u32)(mt * 64 + (tid >> 3)) * 1024u + nt * 128 + (tid & 7) * 16;
#define W2OP(s, nl) ((s) < 6 ? wf2[(s) < 6 ? (s) >> 1 : 0][(s) & 1][nl] : wn[(s) & 1][nl])
#define G2_STEP(s, NV, X) do { VMW(NV); TIE(hf[s][0]); TIE(hf[s][1]); \
      c2[0][0] = mfma32(W2OP(s, 0), hf[s][0], c2[0][0]); SB(); if ((s) + 4 < 14) H_LD((s) + 4, 0); SB(); \
      c2[1][0] = mfma32(W2OP(s, 1), hf[s][0], c2[1][0]); \
      if ((s) >= 5 && (s) < 13) { wn[((s) + 1) & 1][0] = W2FRAG((((s) + 1) >> 1) & 3, ((s) + 1) & 1, 0); wn[((s) + 1) & 1][1] = W2FRAG((((s) + 1) >> 1) & 3, ((s) + 1) & 1, 1); } SG(0x8, 1); SB(); \
      c2[0][1] = mfma32(W2OP(s, 0), hf[s][1], c2[0][1]); SB(); if ((s) + 4 < 14) H_LD((s) + 4, 1); SB(); \
      c2[1][1] = mfma32(W2OP(s, 1), hf[s][1], c2[1][1]); SB(); X; SB(); } while (0)
    VMW(8); if (lane < 2) GST("global_store_dwordx2", yfo, tag, ws, 0, "sc1");
    STAMP(29);
    G2_STEP(0, 7, {});
    G2_STEP(1, 7, {});
    G2_STEP(2, 7, {});
    G2_STEP(3, 7, {});
    STAMP(30);
    G2_STEP(4, 6, {});
    G2_STEP(5, 6, {});
    G2_STEP(6, 6, {});
    G2_STEP(7, 6, {});
    G2_STEP(8, 6, {});
    G2_STEP(9, 6, { GLD("global_load_dwordx4", xr, oo, a.x, 0, ""); const u32 o2 = nt * 128 + (tid & 7) * 16; GLD("global_load_dwordx4", b2v, o2, a.b2, 0, ""); GLD("global_load_dwordx2", fl, ypo, ws, 0, "sc1"); });
    STAMP(31);
    G2_STEP(10, 9, {});
    G2_STEP(11, 7, {});
    G2_STEP(12, 5, {});
    G2_STEP(13, 3, {});
    STAMP(32);
    VMW(0); TIE(fl); if (tag_miss(fl, tag)) { MARK(SLOW); poll(ypo, ws, tag); }
    { const u32 o = WS_Y + (u32)(nt * 64 + (w & 1) * 32 + l31) * 4096u + (u32)(mt * 64 + (w >> 1) * 16 + hh * 8) * 2u; GLD("global_load_dwordx4", yf, o, ws, 0, "sc1"); }
    STAMP(12);
    v4u id;
    { const int e = l31 - 16 * ((w >> 1) & 1) - 8 * hh;
#pragma unroll
      for (int i = 0; i < 4; i++) id[i] = (e >> 1) == i ? (0x3f80u << (16 * (e & 1))) : 0u; }
#define P_PUT(nl, ml) do { const int row_ = 32 * ((ml) ^ (w >> 2)) + l31; _Pragma("unroll") for (int qd = 0; qd < 4; qd++) \
      lds_st<f4>((row_ >> 4) * 32768 + w4k + (row_ & 15) * 256 + 16 * ((8 * ((nl) ^ (w & 1)) + 2 * qd + hh) ^ (row_ & 7)), \
                 f4{c2[nl][ml][4 * qd], c2[nl][ml][4 * qd + 1], c2[nl][ml][4 * qd + 2], c2[nl][ml][4 * qd + 3]}); } while (0)
    P_PUT(1, 0); P_PUT(0, 1); P_PUT(1, 1); SB();
    VMW(0); TIE(yf); TIE(xr); TIE(b2v);
    c2[0][0] = mfma32(yf, id, c2[0][0]);
    P_PUT(0, 0);
    STAMP(13);
    LGW0(); BAR();
    const int row = tid >> 3, c8 = tid & 7;
    const u32 ra = (row >> 4) * 32768 + (row & 15) * 256 + 16 * ((2 * c8) ^ (row & 7)), rb = ra ^ 16u;
    f4 s0 = lds_ld<f4>(ra), s1 = lds_ld<f4>(rb);
#pragma unroll
    for (int ww = 1; ww < 8; ww++) { s0 += lds_ld<f4>(ra + ww * 4096); s1 += lds_ld<f4>(rb + ww * 4096); }
    v4u o;
    o.x = pk2(s0[0] + bf2f(b2v.x) + bf2f(xr.x), s0[1] + bfhi(b2v.x) + bfhi(xr.x));
    o.y = pk2(s0[2] + bf2f(b2v.y) + bf2f(xr.y), s0[3] + bfhi(b2v.y) + bfhi(xr.y));
    o.z = pk2(s1[0] + bf2f(b2v.z) + bf2f(xr.z), s1[1] + bfhi(b2v.z) + bfhi(xr.z));
    o.w = pk2(s1[2] + bf2f(b2v.w) + bf2f(xr.w), s1[3] + bfhi(b2v.w) + bfhi(xr.w));
    GST("global_store_dwordx4", oo, o, a.out, 0, "");
  }
  STAMP(14);
  if (bid == 255) { poll(WS_YF + (u32)tid * 8u, ws, tag); BAR(); if (tid == 0) { u32 z = 0; GST("global_store_dwordx2", z, tag, ws, 0, "sc1"); } }
  asm volatile(".Lfk_end:");
}

extern "C" void kernel_launch(void* const* d_in, const int* in_sizes, int n_in, void* d_out, int out_size, void* d_ws, size_t ws_size, hipStream_t stream) {
  Args a;
  a.x = (const u16*)d_in[0]; a.are = (const u16*)d_in[1]; a.aim = (const u16*)d_in[2]; a.bre = (const u16*)d_in[3]; a.bim = (const u16*)d_in[4];
  a.cre = (const u16*)d_in[5]; a.cim = (const u16*)d_in[6]; a.ldt = (const u16*)d_in[7]; a.dsk = (const u16*)d_in[8];
  a.w1 = (const u16*)d_in[10]; a.b1 = (const u16*)d_in[11]; a.w2 = (const u16*)d_in[12]; a.b2 = (const u16*)d_in[13];
  a.out = (u16*)d_out; a.ws = (char*)d_ws;
  (void)hipFuncSetAttribute((const void*)fused_kernel, hipFuncAttributeMaxDynamicSharedMemorySize, LDS_BYTES);
  fused_kernel<<<256, 512, LDS_BYTES, stream>>>(a);
}
```

```cpp
#include <hip/hip_runtime.h>
#include <stdint.h>

typedef unsigned u32; typedef unsigned long long u64; typedef unsigned short u16;
typedef u32 v4u __attribute__((ext_vector_type(4)));
typedef u32 v2u __attribute__((ext_vector_type(2)));
typedef float f4 __attribute__((ext_vector_type(4)));
typedef float f2 __attribute__((ext_vector_type(2)));
typedef float f16v __attribute__((ext_vector_type(16)));
typedef short s4 __attribute__((ext_vector_type(4)));
typedef __bf16 bf8 __attribute__((ext_vector_type(8)));
typedef __bf16 bf2 __attribute__((ext_vector_type(2)));
#define LAS __attribute__((address_space(3)))
#define DI __device__ __forceinline__

constexpr u32 WS_CF = 4096, WS_S = 16384, WS_C = 32768;
constexpr u32 WS_UF = 65536, WS_ST = 131072  , WS_HF = 262144, WS_YF = 524288;
constexpr u32 WS_U = 1u << 20, US = 8192 + 128, YS = 4096 + 128, WS_Y = WS_U + 256 * US, WS_W = WS_Y + 512 * YS, WS_H = WS_W + (2u << 20), WS_END = WS_H + 64 * 112 * 1024;
constexpr u32 SP = 131072, KA = 0, KB = 640, KC = 960, KQ = 1232, KD = 1616, TBS = 1664;
constexpr u32 LXN = 65536, LSS = 98304, LTS = SP + 2 * TBS, LT1 = LTS + 16384, LWR = SP + 30720;
constexpr u32 LU = SP + 2 * TBS, LUS = 5120, LLT = LU + 2 * LUS, LEX = SP + 16384;
constexpr u32 LDS_BYTES = 163840;
static_assert(LT1 + 8192 <= LWR && LTS % 64 == 0 && LLT <= LEX, "LDS map");

constexpr int rne(int j, int sh) { int q = j >> sh, rem = j & ((1 << sh) - 1), h = 1 << (sh - 1); if (rem > h || (rem == h && (q & 1))) q++; return q << sh; }
constexpr int rnd_bf(int j) { return j <= 256 ? j : rne(j, j < 512 ? 1 : j < 1024 ? 2 : 3); }
constexpr int i1(int j) { return (rne(j, 1) - 256) / 2; }
constexpr int ic(int j) { return (rnd_bf(j) - 512) / 4; }
constexpr u32 sel1(int i) { return i < 0 ? 0x0c0cu : (u32)(2 * i) | (u32)(2 * i + 1) << 8; }
constexpr u32 sel(int lo, int hi) { return sel1(lo) | sel1(hi) << 16; }
constexpr int a3(int m) { return m ? KQ + 128 + 2 * (m - 1) : KC + 256; }
struct alignas(128) Tabs { u32 wr[512]; u32 lt[6 * 256]; };
constexpr Tabs make_tabs() {
  Tabs t{};
  for (int tl = 0; tl < 4; tl++) for (int l = 0; l < 64; l++) {
    const int sg = (l & 15) + 16 * (tl & 1), g = l >> 4; int ad[4] = {};
    for (int r = 0; r < 4; r++)
      ad[r] = !(tl >> 1) ? (g < 2 ? KA + 2 * (16 + 8 * sg + 4 * g + r) : g == 2 ? KB + 2 * (4 * sg + r) : KC + 2 * (2 * sg + (r & 1) + 64 * (r >> 1)))
                         : (g == 0 ? a3(sg + 32 * r) : (g == 1 && r == 0 && sg == 0) ? a3(128) : KD);
    t.wr[(tl * 64 + l) * 2] = (u32)ad[0] | (u32)ad[1] << 16; t.wr[(tl * 64 + l) * 2 + 1] = (u32)ad[2] | (u32)ad[3] << 16;
  }
  for (int l = 0; l < 64; l++) {
    const int o = 16 + (l & 15) - 8 * (l >> 4);
    const int a0 = 16 + o - 7, j1 = 32 * 9 + o, j2 = 32 * 17 + o, jx = 32 * 32 + o;
    const int b1 = i1(j1 - 7) & ~1, b2 = ic(j2 - 7) & ~1, bx = jx - 7 <= 1028 ? ic(jx - 7) & ~1 : 0;
    t.lt[l * 4 + 0] = sel((a0 & 1) + 1, a0 & 1);
    t.lt[l * 4 + 1] = sel(i1(j1 - 4) - b1, i1(j1 - 5) - b1);
    t.lt[l * 4 + 2] = sel(i1(j1 - 6) - b1, i1(j1 - 7) - b1);
    t.lt[l * 4 + 3] = (u32)(KA + 2 * (a0 & ~1)) | (u32)(KB + 2 * b1 - 32) << 16;
    for (int i = 0; i < 4; i++) {
      t.lt[256 + l * 4 + i] = sel(ic(j2 - 2 * i) - b2, ic(j2 - 2 * i - 1) - b2);
      t.lt[512 + l * 4 + i] = sel(jx - 2 * i <= 1028 ? ic(jx - 2 * i) - bx : -1, jx - 2 * i - 1 <= 1028 ? ic(jx - 2 * i - 1) - bx : -1);
      const int p = o - 2 * i, q = o + 8 - 2 * i;
      t.lt[768 + l * 4 + i] = (p >= 5 && p <= 11 ? 0x3f80u : 0u) | (p - 1 >= 5 && p - 1 <= 11 ? 0x3f800000u : 0u);
      t.lt[1024 + l * 4 + i] = (q >= 12 && q <= 20 ? 0x3f80u : 0u) | (q - 1 >= 12 && q - 1 <= 20 ? 0x3f800000u : 0u);
    }
    t.lt[1280 + l * 4 + 0] = (u32)(KC + 2 * b2 - 16) | (u32)(KC + 2 * bx) << 16;
    t.lt[1280 + l * 4 + 1] = l < 32 ? (u32)(KB + 2 * i1(256 + l)) | (u32)(KA + 2 * (16 + 256 + l)) << 16
                           : l < 49 ? (u32)(KC + 2 * ic(512 + 2 * (l - 32))) | (u32)(KB + 2 * (128 + l - 32)) << 16 : (u32)KD | (u32)KD << 16;
  }
  return t;
}
__device__ const Tabs g_tabs = make_tabs();

struct Args { const u16 *are, *aim, *bre, *bim, *cre, *cim, *x, *w1, *w2; char* ws; const u16 *ldt, *dsk, *b1, *b2; u16* out; const u16* pad; };

DI float bf2f(u32 v) { return __uint_as_float(v << 16); }
DI float bfhi(u32 v) { return __uint_as_float(v & 0xffff0000u); }
DI u32 pk2(float a, float b) { bf2 c; c[0] = (__bf16)a; c[1] = (__bf16)b; return __builtin_bit_cast(u32, c); }
DI float rbf(float a) { return bf2f(pk2(a, a) & 0xffffu); }
DI f4 mfma(v4u a, v4u b, f4 c) { return __builtin_amdgcn_mfma_f32_16x16x32_bf16(__builtin_bit_cast(bf8, a), __builtin_bit_cast(bf8, b), c, 0, 0, 0); }
DI f16v mfma32(v4u a, v4u b, f16v c) { return __builtin_amdgcn_mfma_f32_32x32x16_bf16(__builtin_bit_cast(bf8, a), __builtin_bit_cast(bf8, b), c, 0, 0, 0); }
DI float dot2(u32 a, u32 b, float c) { return __builtin_amdgcn_fdot2_f32_bf16(__builtin_bit_cast(bf2, a), __builtin_bit_cast(bf2, b), c, false); }
template <class T> DI T lds_ld(u32 a) { return *(const LAS T*)(size_t)a; }
template <class T> DI void lds_st(u32 a, T v) { *(LAS T*)(size_t)a = v; }
DI v2u lds_tr(u32 a) { return __builtin_bit_cast(v2u, __builtin_amdgcn_ds_read_tr16_b64_v4i16((LAS s4*)(size_t)a)); }
DI v4u lds_tr2(u32 a, u32 b) { v2u x = lds_tr(a), y = lds_tr(b); return v4u{x.x, x.y, y.x, y.y}; }
DI v4u lds_2x64(u32 a) { v2u x = lds_ld<v2u>(a); asm volatile(""); v2u y = lds_ld<v2u>(a + 8); return v4u{x.x, x.y, y.x, y.y}; }
DI float xor32_add(float v) { auto r = __builtin_amdgcn_permlane32_swap(__float_as_uint(v), __float_as_uint(v), false, false); return __uint_as_float(r[0]) + __uint_as_float(r[1]); }
#define DPP_ADD(v, ctrl) v += __uint_as_float(__builtin_amdgcn_update_dpp(0u, __float_as_uint(v), ctrl, 0xf, 0xf, true))
DI float sum32(float v) {
  DPP_ADD(v, 0xB1); DPP_ADD(v, 0x4E); DPP_ADD(v, 0x141); DPP_ADD(v, 0x140);
  auto r = __builtin_amdgcn_permlane16_swap(__float_as_uint(v), __float_as_uint(v), false, false); return __uint_as_float(r[0]) + __uint_as_float(r[1]);
}
DI void cmul2(f2& r0, f2 a0, f2 w0, f2& r1, f2 a1, f2 w1) {
  asm("v_pk_mul_f32 %0, %2, %3 op_sel:[0,0] op_sel_hi:[1,0]\n\tv_pk_mul_f32 %1, %4, %5 op_sel:[0,0] op_sel_hi:[1,0]\n\t"
      "v_pk_fma_f32 %0, %2, %3, %0 op_sel:[1,1,0] op_sel_hi:[0,1,1] neg_lo:[1,0,0]\n\tv_pk_fma_f32 %1, %4, %5, %1 op_sel:[1,1,0] op_sel_hi:[0,1,1] neg_lo:[1,0,0]"
      : "=&v"(r0), "=&v"(r1) : "v"(a0), "v"(w0), "v"(a1), "v"(w1));
}
DI f2 cexp2(f2 x) {
  float e = __builtin_amdgcn_exp2f(x.x), c = __builtin_amdgcn_cosf(x.y), s = __builtin_amdgcn_sinf(x.y);
  return f2{c, s} * e;
}
DI void ln_stats(float s, float q, float& mb, float& sd) {
  const float mean = s * (1.f / 512.f);
  const float var = fmaxf(q * (1.f / 512.f) - mean * mean, 0.f);
  sd = rbf(__builtin_amdgcn_sqrtf(rbf(rbf(var) + 1e-5f))); mb = rbf(mean);
}
#define VMW(n) asm volatile("s_waitcnt vmcnt(%0)" ::"n"(n) : "memory")
#define LGW0() asm volatile("s_waitcnt lgkmcnt(0)" ::: "memory")
#define BAR() asm volatile("s_barrier" ::: "memory")
#define TIE(x) asm volatile("" : "+v"(x))
#define MARK(x) asm volatile("; PATH_" #x)
#define SB() __builtin_amdgcn_sched_barrier(0)
#define SG(m, n) __builtin_amdgcn_sched_group_barrier(m, n, 0)
#define GLD(op, dst, voff, sbase, imm, fl) asm volatile(op " %0, %1, %2 offset:%3 " fl : "=v"(dst) : "v"(voff), "s"(sbase), "n"(imm) : "memory")
#define GST(op, voff, src, sbase, imm, fl) asm volatile(op " %0, %1, %2 offset:%3 " fl "\n\ts_nop 1" ::"v"(voff), "v"(src), "s"(sbase), "n"(imm) : "memory")
#define M0SET(lbase, limm) asm volatile("s_add_u32 m0, %0, %1\n\ts_nop 0" ::"s"(lbase), "n"(limm) : "memory", "scc")
#define DMA(voff, srd, soff, off) asm volatile("buffer_load_dwordx4 %0, %1, %2 offen offset:%3 lds" ::"v"(voff), "s"(srd), "s"(soff), "n"(off) : "memory")
DI v4u make_srd(const void* p, u32 recs) { const u64 a_ = (u64)p - 4096; return v4u{(u32)a_, (u32)(a_ >> 32), recs, 0x20000u}; }
#define STIE(x) asm volatile("" : "+s"(x))
DI bool tag_miss(v2u f, v2u tag) { return __builtin_expect(__builtin_amdgcn_ballot_w64(f.x != tag.x || f.y != tag.y) != 0, 0); }
DI void poll(u32 voff, const char* ws, v2u tag) {
#pragma nounroll
  for (int it = 0; it < (1 << 16); it++) {
    v2u f;
    asm volatile("global_load_dwordx2 %0, %1, %2 sc1\n\ts_waitcnt vmcnt(0)" : "=v"(f) : "v"(voff), "s"(ws) : "memory");
    if (__builtin_amdgcn_ballot_w64(f.x != tag.x || f.y != tag.y) == 0) break;
    __builtin_amdgcn_s_sleep(6);
  }
}
#define STAMP(i)

struct CvA { u32 ua, a0, a1, a2; };
struct CvS { v4u s01, s2; };
struct ConvRaw { v4u A[8]; u32 e[5], f[3], q[2][2]; };
DI u32 prm(u32 hi, u32 lo, u32 s) { return __builtin_amdgcn_perm(hi, lo, s); }
DI v4u perm4(u32 hi, u32 lo, v4u s) { return v4u{prm(hi, lo, s.x), prm(hi, lo, s.y), prm(hi, lo, s.z), prm(hi, lo, s.w)}; }
DI v4u frag0(const u32 (&e)[5], u32 s) { return v4u{prm(e[4], e[3], s), prm(e[3], e[2], s), prm(e[2], e[1], s), prm(e[1], e[0], s)}; }
DI v4u frag1(const u32 (&f)[3], u32 sa, u32 sb) { return v4u{prm(f[2], f[1], sa), prm(f[2], f[1], sb), prm(f[1], f[0], sa), prm(f[1], f[0], sb)}; }
template <int RR, int P> DI void conv_load(ConvRaw& c, const CvA& a) {
  if (P == 0) {
#pragma unroll
    for (int i = 0; i < 5; i++) if (RR) c.e[i] = lds_ld<u32>(a.a0 + 64 * RR + 4 * i);
    c.A[7] = lds_ld<v4u>(a.ua + 64 * (3 - RR) + 512 * 7); c.A[5] = lds_ld<v4u>(a.ua + 64 * (3 - RR) + 512 * 5);
  }
  if (P == 1) {
    c.A[3] = lds_ld<v4u>(a.ua + 64 * (3 - RR) + 512 * 3); c.A[1] = lds_ld<v4u>(a.ua + 64 * (3 - RR) + 512);
    if (RR) {
#pragma unroll
      for (int i = 0; i < 3; i++) c.f[i] = lds_ld<u32>(a.a1 + 32 * RR + 4 * i);
    }
  }
  if (P == 2) {
#pragma unroll
    for (int i = 6; i >= 0; i -= 2) c.A[i] = lds_ld<v4u>(a.ua + 64 * (3 - RR) + 512 * i);
  }
  if (P == 3) {
#pragma unroll
    for (int m = 2; m < 4; m++) if (RR || m != 2) {
      const u32 p = a.a2 + 16 * RR + 128 * (m - 2);
      c.q[m - 2][0] = lds_ld<u32>(p); c.q[m - 2][1] = lds_ld<u32>(p + 4);
    }
  }
}
template <int RR, int M> DI v4u conv_b(const ConvRaw& c, const CvS& s, v4u x1, v4u x2) {
  return (RR == 0 && M == 1) ? x1 : (RR == 0 && M == 2) ? x2 : M == 1 ? frag1(c.f, s.s01.y, s.s01.z) : perm4(c.q[M < 2 ? 0 : M - 2][1], c.q[M < 2 ? 0 : M - 2][0], s.s2);
}
template <int M, bool Z> DI void conv_mm(f4 (&acc)[4], const ConvRaw& c, v4u B) {
#pragma unroll
  for (int t = 3; t >= 0; t--) if (2 * t + 1 >= M) acc[t] = mfma(B, c.A[2 * t + 1 - M], Z ? f4{0, 0, 0, 0} : acc[t]);
}
#define CV_SG(n) do { _Pragma("unroll") for (int i_ = 0; i_ < (n); i_++) { SG(0x8, 1); SG(0x100, 2); SG(0x2, 2); } SB(); } while (0)
#define CV_ROUND(RR, c, n, X0, X1, X2, X3) do { \
    v4u b1_ = conv_b<RR, 1>(c, cs_, x1, x2); if ((RR) < 3) conv_load<((RR) + 1) & 3, 0>(n, ca_); conv_mm<0, (RR) == 0>(ca, c, b0); CV_SG(4); TIE(b1_); X0; SB(); \
    v4u b2_ = conv_b<RR, 2>(c, cs_, x1, x2); if ((RR) < 3) conv_load<((RR) + 1) & 3, 1>(n, ca_); conv_mm<1, false>(ca, c, b1_); CV_SG(4); TIE(b2_); X1; SB(); \
    v4u b3_ = conv_b<RR, 3>(c, cs_, x1, x2); if ((RR) < 3) conv_load<((RR) + 1) & 3, 2>(n, ca_); conv_mm<2, false>(ca, c, b2_); CV_SG(3); TIE(b3_); X2; SB(); \
    if ((RR) < 3) { conv_load<((RR) + 1) & 3, 3>(n, ca_); b0 = frag0(n.e, cs_.s01.x); } \
    conv_mm<3, false>(ca, c, b3_); if ((RR) == 0) conv_mm<4, false>(ca, c, x4); CV_SG((RR) == 0 ? 5 : 3); TIE(b0); X3; SB(); } while (0)

typedef u32 v16u __attribute__((ext_vector_type(16)));
#define KP(v, i) ((const char*)(((u64)v[2 * (i) + 1] << 32) | v[2 * (i)]))
__global__ __launch_bounds__(512, 2) void fused_kernel(Args) {
  STAMP(0);
  v16u k0_, k1_;
  asm volatile("s_load_dwordx16 %0, %2, 0x0\n\ts_load_dwordx16 %1, %2, 0x40" : "=&s"(k0_), "=&s"(k1_) : "s"(__builtin_amdgcn_kernarg_segment_ptr()) : "memory");
  const int tid = threadIdx.x, lane = tid & 63, w = __builtin_amdgcn_readfirstlane(tid >> 6), l15 = lane & 15, g = lane >> 4, l31 = lane & 31, hh = lane >> 5;
  const int bid = blockIdx.x, xcd = bid & 7, idx = bid >> 3;
  const int oct = xcd * 32 + idx;
  const int ch = w >> 2, dch = 2 * oct + ch;
  const bool tw = ((w >> 1) ^ ch) & 1;
  const int lr = 4 * ch + 2 * (w & 1) + hh;
  const int md = (w & 1) * 64 + lane;
  u32 xo = (u32)(oct * 8 + lr) * 1024u + (u32)l31 * 16u, po = (u32)(dch * 128 + md) * 2u;
  TIE(xo); TIE(po);
  if (w < 2) { const u32 o = (u32)tid * 16u, d = LWR + w * 1024;
    asm volatile("s_mov_b32 m0, %2\n\ts_nop 0\n\tglobal_load_lds_dwordx4 %0, %1" ::"v"(o), "s"(g_tabs.wr), "s"(d) : "memory");
  } else if (w < 4) {
    u64 pc; u32 len; asm volatile("s_getpc_b64 %0\n.Lfk_beg:\n\ts_mov_b32 %1, .Lfk_end-.Lfk_beg" : "=s"(pc), "=s"(len));
    const u32 ln = (idx & 15) * 2 + w - 2, o = min((ln * 6 + lane) * 128u, len), o2 = min(ln * 2 + (lane & 1), 47u) * 128u;
    if (lane < 6) asm volatile("s_mov_b32 m0, %4\n\ts_nop 0\n\tglobal_load_lds_dword %0, %1\n\tglobal_load_lds_dword %2, %3" ::"v"(o), "s"(pc), "v"(o2), "s"(g_tabs.lt), "n"(SP + KD) : "memory");
  }
  const bool gw = __builtin_expect(idx < 28, 1);
  const int j4 = (idx * 37) >> 8, i7 = idx - 7 * j4;
  const int rt = (xcd >> 1) * 4 + j4, ft = (xcd & 1) * 7 + i7, rot = i7 ^ (2 * j4);
  const int mt = xcd * 4 + (idx >> 3), nt = idx & 7;
  const u32 w4k = (u32)w * 4096u;
  const int cg = (xcd & 1) * 14 + min((xcd >> 1) * 4 + (idx - 28), 13);
  u32 vx0, vx1, vw0, vw1, xs0, xs1, ws0, ws1, ws2;
  if (gw) {
    vx0 = (u32)(rt * 128 + w * 16 + (lane >> 3)) * 1024u + 16u * ((lane & 7) ^ (lane >> 4)) + 4096u; vx1 = (vx0 ^ 64u) + 8192u - 1024u;
    vw0 = (u32)(8 * w + g) * 3584u + 256u * ft + 16u * (l15 ^ (4 * g)) + 4096u - 2048u; vw1 = vw0 + 4 * 3584 - 1024;
  } else { vx0 = (u32)(64 * w + (lane >> 3)) * 3584u + 128u * cg + 16u * (lane & 7) + 4096u; vx1 = vx0 + 8 * 3584 - 1024; vw0 = vx0 + 16 * 3584 - 2048; vw1 = vx0 + 24 * 3584 - 3072; }
  xs0 = gw ? rot * 128 : 0; xs1 = gw ? (rot ^ 1) * 128 : 32 * 3584; ws0 = gw ? rot * (64 * 3584) : 0; ws1 = gw ? (rot ^ 1) * (64 * 3584) : 32 * 3584; ws2 = gw ? (rot ^ 2) * (64 * 3584) : 0;
  const int fq = bid * 7 + w;
  u32 wo = (u32)((fq >> 4) * 16 + (lane >> 2)) * 1024u + (fq & 15) * 64u + (lane & 3) * 16u + (w < 7 ? 4096u : 0x40000000u);
  TIE(vx0); TIE(vx1); TIE(vw0); TIE(vw1); TIE(wo);
  asm volatile("s_waitcnt lgkmcnt(0)" : "+s"(k0_), "+s"(k1_)::"memory");
  struct { const char *are, *aim, *bre, *bim, *cre, *cim, *x, *w1, *w2, *ws, *ldt, *dsk, *b1, *b2, *out; } a =
    {KP(k0_, 0), KP(k0_, 1), KP(k0_, 2), KP(k0_, 3), KP(k0_, 4), KP(k0_, 5), KP(k0_, 6), KP(k0_, 7), KP(k1_, 0), KP(k1_, 1), KP(k1_, 2), KP(k1_, 3), KP(k1_, 4), KP(k1_, 5), KP(k1_, 6)};
  const char* ws = a.ws;
  u32 pa, pb, pc_, pd, pe, pg; v4u xr0, xr1;
  GLD("global_load_short_d16_hi", pa, po, a.are, 0, ""); GLD("global_load_short_d16_hi", pb, po, a.aim, 0, "");
  u64 ep; u32 sl, sk_;
  { const u32 b4 = oct * 4;
    asm volatile("s_load_dwordx2 %0, %3, 0x0\n\ts_load_dword %1, %4, %6\n\ts_load_dword %2, %5, %6" : "=&s"(ep), "=&s"(sl), "=&s"(sk_) : "s"(ws), "s"(a.ldt), "s"(a.dsk), "s"(b4) : "memory"); }
  if (tw) { MARK(KT); GLD("global_load_dwordx4", xr0, xo, a.x, 0, ""); GLD("global_load_dwordx4", xr1, xo, a.x, 512, ""); }
  else { MARK(KS); GLD("global_load_short_d16_hi", pc_, po, a.bre, 0, ""); GLD("global_load_short_d16_hi", pd, po, a.bim, 0, ""); GLD("global_load_short_d16_hi", pe, po, a.cre, 0, ""); GLD("global_load_short_d16_hi", pg, po, a.cim, 0, ""); }
  SB();
  const v4u rw = make_srd(a.w1, 512 * 3584 + 4096), rx = make_srd(gw ? a.x : a.w1, gw ? 2048 * 1024 + 4096 : 512 * 3584 + 4096), r2 = make_srd(a.w2, 1792 * 1024 + 4096), rs = make_srd(ws, WS_END + 4096);
#define XS(c) (u32)(((c) ^ rot) * 128)
#define WS_(c) (u32)(((c) ^ rot) * (64 * 3584))
#define DMA_X(c) do { DMA(vx0, rx, XS(c), 0); DMA(vx1, rx, XS(c), 1024); } while (0)
#define DMA_W(c) do { DMA(vw0, rw, WS_(c), 2048); DMA(vw1, rw, WS_(c), 3072); } while (0)
  M0SET(w4k, 32768); DMA(wo, r2, 0, 0);
  BAR();
  M0SET(w4k, 0); DMA(vx0, rx, xs0, 0); DMA(vx1, rx, xs0, 1024); DMA(vw0, rw, ws0, 2048); DMA(vw1, rw, ws0, 3072);
  STAMP(16);
  const int mw = w & 3, nh = w >> 2, q4 = l15 >> 2;
  u32 xa[2][4], wa[2][2], ksa, kta;
  { const int row = 32 * mw + l31;
#pragma unroll
    for (int r = 0; r < 4; r++) { xa[0][r] = (row >> 4) * 4096 + (row & 15) * 128 + 16 * ((2 * r + hh) ^ ((row >> 1) & 7)); TIE(xa[0][r]); }
#pragma unroll
    for (int nl = 0; nl < 2; nl++) { wa[0][nl] = hh * 4096 + 2048 + q4 * 256 + 64 * ((2 * nh + nl) ^ q4) + 32 * (g & 1) + 8 * (lane & 3); TIE(wa[0][nl]); }
    const u32 o = (2 * g + (q4 >> 1)) * 64 + 8 * (lane & 1), o1 = o + 16 * (((lane >> 1) & 3) ^ g);
    ksa = LSS + ch * 16384 + (w & 1) * 8192 + o1;
    kta = (w & 2) ? LT1 + o + 16 * ((2 * ch + (q4 & 1)) ^ g) : LTS + ch * 8192 + o1;
    TIE(ksa); TIE(kta); }
  u32 wra = 32768 + w4k + (4 * hh + q4) * 64 + 32 * (g & 1) + 8 * (lane & 3), wso = WS_W + (u32)((((fq >> 1) & 7) * 112 + (fq >> 4)) * 2 + (fq & 1)) * 1024u + lane * 16u + (w < 7 ? 4096u : 0x40000000u), uso = WS_U + (u32)oct * US + (u32)tid * 16u;
  TIE(wra); TIE(wso); TIE(uso);
  asm volatile("s_waitcnt lgkmcnt(0)" : "+s"(ep), "+s"(sl), "+s"(sk_)::"memory");
  v2u tag;
  { u64 z = (ep + 0x9E3779B97F4A7C15ull) * 0xBF58476D1CE4E5B9ull; z ^= z >> 29; STIE(z); tag.x = (u32)z; tag.y = (u32)(z >> 32); }
  float dt;
  { const float xx = __uint_as_float(ch ? sl & 0xffff0000u : sl << 16), t = xx * 1.4426950216293335f, r = __builtin_fmaf(xx, 1.9259629911266175e-8f, __builtin_fmaf(xx, 1.4426950216293335f, -t));
    const float e = __builtin_amdgcn_exp2f(t); dt = rbf(__builtin_fmaf(e * r, 0.6931471805599453f, e)); TIE(dt); }
  float k00 = __uint_as_float((tid & 255) == 0 ? (ch ? sk_ & 0xffff0000u : sk_ << 16) : 0u); TIE(k00);
  SB();
  VMW(5); TIE(pa); TIE(pb);
  STAMP(1);
  v4u wt; v2u sv = {0, 0};
#define GAP_A() do { SB(); M0SET(w4k, 32768); DMA(vw0, rw, ws1, 2048); DMA(vw1, rw, ws1, 3072); SB(); } while (0)
#define GAP_B() do { SB(); VMW(6); wt = lds_tr2(wra, wra + 512); TIE(wt); DMA(vx0, rx, xs1, 0); DMA(vx1, rx, xs1, 1024); SB(); } while (0)

  const u32 tb = SP + (u32)ch * TBS;
  {
    const u32 dk = md * 64 + 16 * ((lane >> 1) & 3);
    float arl = dt * bfhi(pa) * 1.4426950408889634f; arl = fminf(fmaxf(arl, -1e30f), 1e30f);
    float f = dt * bfhi(pb) * 0.15915494309189535f; f -= rintf(f);
    const f2 af = {arl, f};
    if (!tw) {
      MARK(KS);
      TIE(pc_); TIE(pd); TIE(pe); TIE(pg);
      const f2 z8 = cexp2(af * 8.f), bb = f2{bfhi(pc_), bfhi(pd)} * dt, cc = {bfhi(pe), bfhi(pg)};
      f2 z16, g0, g1, t_;
      cmul2(z16, z8, z8, g0, cc, bb); cmul2(g1, g0, z8, t_, g0, z8);
      const u32 d = LSS + ch * 16384 + dk;
#pragma unroll
      for (int c = 0; c < 4; c++) {
        v4u re, im;
#pragma unroll
        for (int i = 0; i < 4; i++) { f2 n0 = g0, n1 = g1; if (c < 3 || i < 3) cmul2(n0, g0, z16, n1, g1, z16); re[i] = pk2(g0.x, g1.x); im[i] = pk2(g0.y, g1.y); g0 = n0; g1 = n1; }
        lds_st<v4u>((d ^ (16 * (c & 1))) + (c >> 1) * 8192, re); lds_st<v4u>((d ^ (32 + 16 * (c & 1))) + (c >> 1) * 8192, im);
        if (c == 0) GAP_A();
        if (c == 2) GAP_B();
      }
    } else {
      MARK(KT);
      TIE(xr0); TIE(xr1);
      float ss = 0.f, sq = 0.f, ss2 = 0.f, sq2 = 0.f;
#pragma unroll
      for (int e = 0; e < 4; e++) { const u32 v = xr0[e], v1 = xr1[e]; ss = dot2(v, 0x3f803f80u, ss); sq = dot2(v, v, sq); ss2 = dot2(v1, 0x3f803f80u, ss2); sq2 = dot2(v1, v1, sq2); }
      ss = sum32(ss + ss2); sq = sum32(sq + sq2);
      float mb, sd; ln_stats(ss, sq, mb, sd);
      const f2 z1 = cexp2(af), y1 = cexp2(af * 256.f);
      f2 z2, y2, z3, z4, y3, y4, z5, z6, z7, a2, a4, a6, b4, c4, y5, y6, y7, y8;
      cmul2(z2, z1, z1, y2, y1, y1); cmul2(z3, z2, z1, z4, z2, z2); cmul2(y3, y2, y1, y4, y2, y2); cmul2(z5, z4, z1, z6, z4, z2); cmul2(z7, z4, z3, a2, y1, z2);
      const u32 d = LTS + ch * 8192 + dk, d1 = (LT1 + dk) ^ (32 * ch);
      lds_st<v4u>(d, v4u{pk2(1.f, z1.x), pk2(z2.x, z3.x), pk2(z4.x, z5.x), pk2(z6.x, z7.x)});
      lds_st<v4u>(d ^ 32, v4u{pk2(0.f, -z1.y), pk2(-z2.y, -z3.y), pk2(-z4.y, -z5.y), pk2(-z6.y, -z7.y)});
      sv = v2u{pk2(-mb, -mb), pk2(-mb, sd)};
      GAP_A();
      cmul2(a4, y1, z4, a6, y1, z6); cmul2(b4, y2, z4, c4, y3, z4); cmul2(y5, y4, y1, y6, y4, y2); cmul2(y7, y4, y3, y8, y4, y4);
      lds_st<v4u>(d ^ 16, v4u{pk2(y1.x, a2.x), pk2(a4.x, a6.x), pk2(y2.x, b4.x), pk2(y3.x, c4.x)});
      lds_st<v4u>(d ^ 48, v4u{pk2(-y1.y, -a2.y), pk2(-a4.y, -a6.y), pk2(-y2.y, -b4.y), pk2(-y3.y, -c4.y)});
      lds_st<v4u>(d1, v4u{pk2(y4.x, y5.x), pk2(y6.x, y7.x), pk2(y8.x, y8.x), 0u});
      lds_st<v4u>(d1 ^ 16, v4u{pk2(-y4.y, -y5.y), pk2(-y6.y, -y7.y), pk2(-y8.y, -y8.y), 0u});
      GAP_B();
      const float rstd = __builtin_amdgcn_rcpf(sd), nmr = -mb * rstd;
      v4u o0, o1;
#pragma unroll
      for (int e = 0; e < 4; e++) { const u32 v = xr0[e], v1 = xr1[e]; const f2 t = f2{bf2f(v), bfhi(v)} * rstd + nmr, t1 = f2{bf2f(v1), bfhi(v1)} * rstd + nmr; o0[e] = pk2(t.x, t.y); o1[e] = pk2(t1.x, t1.y); }
      lds_st<v4u>(LXN + (lr  __H0__ ) + ((lr + l31) << 4), o0); lds_st<v4u>(LXN + 512 + (lr << 12) + ((lr + l31) << 4), o1);
    }
  }
  STAMP(2);
#define LT_DMA() do { const u32 d_ = LLT + min(w, 6) * 1024; asm volatile("s_mov_b32 m0, %2\n\ts_nop 0\n\tglobal_load_lds_dwordx4 %0, %1" ::"v"(lto), "s"(g_tabs.lt), "s"(d_) : "memory"); } while (0)
  M0SET(w4k, 65536); DMA(vw0, rw, ws2, 2048); DMA(vw1, rw, ws2, 3072);
  asm volatile("buffer_store_dwordx4 %0, %1, %2, 0 offen sc1\n\ts_nop 1" ::"v"(wt), "v"(wso), "s"(rs) : "memory");
  { const u32 o = (tw && l31 == 0) ? WS_S + 4096 + (u32)(oct * 8 + lr) * 8u : 0x40000000u; asm volatile("buffer_store_dwordx2 %0, %1, %2, 0 offen sc1" ::"v"(sv), "v"(o), "s"(rs) : "memory"); }
  VMW(8);
  LGW0(); BAR();
  STAMP(18);
  u32 ta2, ta2h, afo, so, co, uo_, lto, ufs; v2u tagv = tag;
#define FLAG_ST(o) asm volatile("buffer_store_dwordx2 %0, %1, %2, 0 offen sc1" ::"v"(tagv), "v"(o), "s"(rs) : "memory")
  {
    const v2u wa_ = lds_ld<v2u>(LWR + (tid & 255) * 8);
    const u32 ra = LXN + (tid & 1) * 16448 + (tid >> 1) * 4;
    const v4u un = {lds_ld<u32>(ra), lds_ld<u32>(ra + 4112), lds_ld<u32>(ra + 8224), lds_ld<u32>(ra + 12336)};
    v4u A[8], B[8];
#pragma unroll
    for (int ks = 0; ks < 8; ks++) { A[ks] = lds_tr2(kta + ks * 1024, kta + ks * 1024 + 512); B[ks] = lds_tr2(ksa + ks * 1024, ksa + ks * 1024 + 512); }
    SB();
  ta2 = w4k + lane * 16 + (w & 1) * 1024; ta2h = ta2 + 65536; TIE(ta2); TIE(ta2h);
#pragma unroll
  for (int r = 0; r < 4; r++) { xa[1][r] = xa[0][r] + 65536; TIE(xa[1][r]); }
  wa[1][0] = wa[0][0] + 65536; wa[1][1] = wa[0][1] + 65536; TIE(wa[1][0]); TIE(wa[1][1]);
  afo = hh ? WS_CF + (u32)(2 * ft + nh) * 128u : WS_UF + (u32)(32 * w + lane) * 128u;
  ufs = tid == 448 ? WS_UF + 4096 + (u32)oct * 128u : 0x40000000u; TIE(ufs); TIE(tagv);
  so = WS_S + (u32)(rt * 128 + mw * 32 + l31) * 8u; co = WS_C + (u32)(ft * 128 + nh * 64 + l31) * 8u; uo_ = WS_U + (u32)(tid >> 1) * US + (u32)oct * 32u + (tid & 1) * 16u; lto = (u32)min(tid, 383) * 16u;
  TIE(afo); TIE(so); TIE(co); TIE(uo_); TIE(lto);
    SB();
    GST("global_store_dwordx4", uso, un, ws, 0, "sc1");
    f4 k0 = {k00, 0, 0, 0}, k1 = {0, 0, 0, 0};
#pragma unroll
    for (int ks = 0; ks < 8; ks++) { if (ks & 1) k1 = mfma(A[ks], B[ks], k1); else k0 = mfma(A[ks], B[ks], k0); }
    k0 += k1;
    const u32 p0 = pk2(k0[0], k0[1]), p1 = pk2(k0[2], k0[3]);
    if (lane < ((w & 2) ? 17 : 64)) {
      lds_st<u16>(tb + (wa_.x & 0xffffu), (u16)p0); lds_st<u16>(tb + (wa_.x >> 16), (u16)(p0 >> 16));
      lds_st<u16>(tb + (wa_.y & 0xffffu), (u16)p1); lds_st<u16>(tb + (wa_.y >> 16), (u16)(p1 >> 16)); }
  }
  const u32 seq = (u32)(0x65432106543210ull >> (4 * (nt == 7 ? 3 : nt)));
#define FR(f) ((seq >> (4 * (f))) & 7u)
  const u32 ho = (u32)lane * 16u, w2s = WS_W + 4096 + (u32)(nt * 112 + 14 * w) * 2048u;
#define DMA_2I(f, i) asm volatile("buffer_load_dwordx4 %0, %1, %2 offen offset:%3 sc1 lds" ::"v"(ho), "s"(rs), "s"(w2s + (FR(f) << 12)), "n"((i) * 1024) : "memory")
#define DMA_2(f) do { M0SET(w4k, ((f) & 3) * 32768); DMA_2I(f, 0); DMA_2I(f, 1); DMA_2I(f, 2); DMA_2I(f, 3); } while (0)
  v4u wf2[3][2][2];
#define W2FRAG(slot, s2, nl) lds_ld<v4u>((((slot) & 2 ? ta2h : ta2) ^ ((nl) * 1024)) + ((slot) & 1) * 32768 + (s2) * 2048)
#define W2_READ(f) do { _Pragma("unroll") for (int s_ = 0; s_ < 2; s_++) _Pragma("unroll") for (int n_ = 0; n_ < 2; n_++) wf2[f][s_][n_] = W2FRAG(f, s_, n_); } while (0)
#define W2_TIE(f) do { LGW0(); _Pragma("unroll") for (int s_ = 0; s_ < 2; s_++) _Pragma("unroll") for (int n_ = 0; n_ < 2; n_++) TIE(wf2[f][s_][n_]); } while (0)
  const int th = w & 1, rh = (w >> 1) & 1;
  CvS cs_; v4u sx, t7, t9; u32 ad;
#define LT_READ() do { cs_.s01 = lds_ld<v4u>(LLT + ho); cs_.s2 = lds_ld<v4u>(LLT + 1024 + ho); sx = lds_ld<v4u>(LLT + 2048 + ho); \
    t7 = lds_ld<v4u>(LLT + 3072 + ho); t9 = lds_ld<v4u>(LLT + 4096 + ho); ad = lds_ld<u32>(LLT + 5120 + ho); } while (0)
  CvA ca_; ConvRaw c0, c1; v4u x1, x2, x4, b0, y1_, y2_; u32 pe_[5], pf_[3], pq_[2];
#define CV_ADDR() do { ca_.ua = LU + (u32)ch * LUS + 448 - 192 - 256 * rh + 512 * th + 32 * l15 + 16 * g; \
    ca_.a0 = tb + (cs_.s01.w & 0xffffu) + 256 * rh; ca_.a1 = tb + (cs_.s01.w >> 16) + 128 * rh; ca_.a2 = tb + (ad & 0xffffu) + 64 * rh; } while (0)
#define CV_PRE() do { const u32 p1_ = rh ? ca_.a2 : ca_.a0 + 512, p2_ = ca_.a1 + (rh ? 0 : 256), p3_ = tb + (ad >> 16), p4_ = tb + KQ + 128 - 12 + 64 * (th - rh) + 4 * l15 - 16 * g; \
    _Pragma("unroll") for (int i = 0; i < 5; i++) { pe_[i] = lds_ld<u32>(p1_ + 4 * i); c0.e[i] = lds_ld<u32>(ca_.a0 + 4 * i); } \
    _Pragma("unroll") for (int i = 0; i < 3; i++) pf_[i] = lds_ld<u32>(p2_ + 4 * i); pq_[0] = lds_ld<u32>(p3_); pq_[1] = lds_ld<u32>(p3_ + 4); \
    _Pragma("unroll") for (int i = 0; i < 4; i++) { y1_[i] = lds_ld<u32>(p4_ + 12 - 4 * i); y2_[i] = lds_ld<u32>(p4_ + 128 + 12 - 4 * i); } \
    conv_load<0, 3>(c0, ca_); \
    b0 = frag0(c0.e, cs_.s01.x); \
    if (rh) { x1 = frag1(pf_, cs_.s01.y, cs_.s01.z); x2 = perm4(pe_[1], pe_[0], cs_.s2); x4 = v4u{0, 0, 0, 0}; } \
    else { x1 = frag0(pe_, cs_.s01.x); x2 = frag1(pf_, cs_.s01.y, cs_.s01.z); x4 = perm4(pq_[1], pq_[0], sx); } TIE(b0); TIE(x1); TIE(x2); TIE(x4); TIE(y1_); TIE(y2_); TIE(t7); TIE(t9); } while (0)
#define K_PAD() do { if (w < 2) lds_st<v4u>(lane < 62 ? LU + w * LUS + ho : SP + w * TBS + ho - 992, v4u{0, 0, 0, 0}); else if (tid < 144) lds_st<v4u>(SP + (lane >> 3) * TBS + KQ + (lane & 7) * 16, v4u{0, 0, 0, 0}); } while (0)
#define U_PUT() do { lds_st<v2u>(LU + 992 + tid * 8, v2u{prm(uu.y, uu.x, 0x05040100u), prm(uu.w, uu.z, 0x05040100u)}); \
    lds_st<v2u>(LU + LUS + 992 + tid * 8, v2u{prm(uu.y, uu.x, 0x07060302u), prm(uu.w, uu.z, 0x07060302u)}); } while (0)
  v4u uu;

  if (gw) {
    MARK(GW);
    f16v acc[2];
    v4u xf[2][2], wf[2][2][2];
    v2u fl;
#define G1_RD(st, r_) do { constexpr int p_ = ((st) >> 2) & 1; constexpr u32 sb_ = (((st) >> 1) & 1) * 32768, wo_ = sb_ + ((st) & 1) * 16384 + (r_) * 8192; \
      xf[(st) & 1][r_] = lds_ld<v4u>(xa[p_][2 * ((st) & 1) + (r_)] + sb_); \
      wf[(st) & 1][0][r_] = lds_tr2(wa[p_][0] + wo_, wa[p_][0] + wo_ + 1024); wf[(st) & 1][1][r_] = lds_tr2(wa[p_][1] + wo_, wa[p_][1] + wo_ + 1024); } while (0)
#define G1_MM(st, n_, r_) acc[n_] = mfma32(wf[(st) & 1][n_][r_], xf[(st) & 1][r_], (st) + (r_) == 0 ? f16v{0} : acc[n_])
#define G1_STEP(st, X2, X3) do { \
      G1_MM(st, 0, 0); if ((st) < 15) G1_RD((st) + 1, 0); SG(0x8, 1); SG(0x100, 5); SB(); \
      G1_MM(st, 1, 0); if ((st) < 15) G1_RD((st) + 1, 1); SG(0x8, 1); SG(0x100, 5); SB(); \
      G1_MM(st, 0, 1); SB(); X2; SB(); \
      G1_MM(st, 1, 1); SB(); X3; SB(); } while (0)
#define G1_SYNC(NV) do { VMW(NV); LGW0(); BAR(); SB(); } while (0)
    v2u st2, cq0, cq1; u32 fx, fv;
    STAMP(3);
    G1_RD(0, 0); G1_RD(0, 1); SB();
    G1_STEP(0, {}, {});
    G1_SYNC(5); STAMP(20);
    G1_STEP(1, { DMA_X(2); LT_DMA(); }, { M0SET(w4k, 98304); DMA(vx0, rx, XS(3), 0); });
    G1_STEP(2, { DMA(vx1, rx, XS(3), 1024); DMA(vw0, rw, WS_(3), 2048); }, DMA(vw1, rw, WS_(3), 3072));
    G1_SYNC(5); STAMP(21);
    G1_STEP(3, { M0SET(w4k, 0); DMA_X(4); }, { DMA(vw0, rw, WS_(4), 2048); FLAG_ST(ufs); });
    G1_STEP(4, { DMA(vw1, rw, WS_(4), 3072); M0SET(w4k, 32768); DMA(vx0, rx, XS(5), 0); }, DMA(vx1, rx, XS(5), 1024));
    G1_SYNC(7); STAMP(22);
    G1_STEP(5, DMA_W(5), { M0SET(w4k, 65536); DMA(vx0, rx, XS(6), 0); });
    G1_STEP(6, { DMA(vx1, rx, XS(6), 1024); DMA(vw0, rw, WS_(6), 2048); }, DMA(vw1, rw, WS_(6), 3072));
    G1_SYNC(8); STAMP(23);
    G1_STEP(7, { M0SET(w4k, 98304); DMA(vx0, rx, XS(7), 0); }, DMA(vx1, rx, XS(7), 1024));
    G1_STEP(8, DMA(vw0, rw, WS_(7), 2048), DMA(vw1, rw, WS_(7), 3072));
    G1_SYNC(8); STAMP(24);
    G1_STEP(9, { GLD("global_load_dwordx2", fl, afo, ws, 0, "sc1"); fx = lds_ld<u32>(LLT + 5120 + lane * 16 + 4); }, K_PAD());
    G1_STEP(10, fv = lds_ld<u16>(tb + (fx & 0xffffu)), {});
    VMW(0); TIE(fl); if (tag_miss(fl, tag)) { MARK(SLOW); poll(afo, ws, tag); }
    G1_SYNC(63); STAMP(25);
    G1_STEP(11, { GLD("global_load_dwordx2", st2, so, ws, 0, "sc1"); GLD("global_load_dwordx2", cq0, co, ws, 0, "sc1"); lds_st<u16>(tb + (fx >> 16), (u16)fv); },
                { GLD("global_load_dwordx2", cq1, co, ws, 256, "sc1"); GLD("global_load_dwordx4", uu, uo_, ws, 0, "sc1"); });
    G1_STEP(12, { M0SET(w4k, 0); DMA_2I(0, 0); }, DMA_2I(0, 1));
    G1_STEP(13, DMA_2I(0, 2), DMA_2I(0, 3));
    G1_STEP(14, { M0SET(w4k, 32768); DMA_2I(1, 0); }, { DMA_2I(1, 1); VMW(6); TIE(st2); TIE(cq0); TIE(cq1); TIE(uu); U_PUT(); });
    LGW0(); BAR(); SB(); STAMP(26);
    G1_STEP(15, { DMA_2I(1, 2); DMA_2I(1, 3); }, { M0SET(w4k, 65536); DMA_2I(2, 0); DMA_2I(2, 1); });
    DMA_2I(2, 2); DMA_2I(2, 3);
    STAMP(4);
    __builtin_amdgcn_s_setprio(2);
    const float sd = bfhi(st2.y);
    { const u32 km = hh ? 0u : ~0u; const v4u sb = {st2.x & km, st2.y & km, 0u, 0u};
      acc[0] = mfma32(v4u{cq0.x, cq0.y, 0u, 0u}, sb, acc[0]); acc[1] = mfma32(v4u{cq1.x, cq1.y, 0u, 0u}, sb, acc[1]); }
    const float rstd = __builtin_amdgcn_rcpf(sd), k1 = -2.3022082f * rstd, k2 = -0.10294324f * rstd * rstd * rstd;
    const int M32 = rt * 4 + mw;
    const char* hb = ws + WS_H + (size_t)(M32 * 112 + ft * 8 + nh * 4) * 1024;
#pragma unroll
    for (int nl = 0; nl < 2; nl++) {
#pragma unroll
      for (int s = 0; s < 2; s++) {
        v4u hp;
#pragma unroll
        for (int e = 0; e < 4; e++) {
          const f2 v = {acc[nl][8 * s + 2 * e], acc[nl][8 * s + 2 * e + 1]};
          const f2 z = v * (v * v * k2 + k1);
          const f2 d = f2{__builtin_amdgcn_exp2f(z.x), __builtin_amdgcn_exp2f(z.y)} * sd + sd;
          const f2 o = v * f2{__builtin_amdgcn_rcpf(d.x), __builtin_amdgcn_rcpf(d.y)};
          hp[e] = pk2(o.x, o.y);
        }
        GST("global_store_dwordx4", ho, hp, hb, nl * 2048 + s * 1024, "sc1");
      }
    }
    __builtin_amdgcn_s_setprio(0);
    STAMP(5);
    VMW(12);
    u32 hfs = lane == 0 ? WS_HF + 4096 + (u32)(M32 * 32 + ft * 2 + nh) * 128u : 0x40000000u; TIE(hfs);
    LT_READ(); W2_READ(0); CV_ADDR(); CV_PRE();
    VMW(0);
    FLAG_ST(hfs);
    STAMP(7);
    LGW0(); BAR();
  } else {
    MARK(LN);
    u32 pb1; { const u32 o3 = (u32)(cg * 64 + lane) * 2u; GLD("global_load_ushort", pb1, o3, a.b1, 0, ""); }
    VMW(4);
    { v4u se; f4 ca = {0, 0, 0, 0};
#pragma unroll
      for (int i = 0; i < 4; i++) se[i] = (l15 >> 1) == i ? (0x3f80u << (16 * (l15 & 1))) : 0u;
#pragma unroll
      for (int i = 0; i < 8; i++) ca = mfma(se, lds_ld<v4u>(w4k + ho + (i >> 2) * 32768 + (i & 3) * 1024), ca);
      if (lane < 32) lds_st<f4>(w4k + (l15 >> 3) * 256 + (lane & 7) * 32 + g * 16, ca); }
    LGW0(); BAR();
    if (w == 0) {
      float c = 0.f;
#pragma unroll
      for (int i = 0; i < 16; i++) c += lds_ld<float>((i >> 1) * 4096 + (i & 1) * 256 + lane * 4);
      const float c0 = rbf(c), c1 = rbf(c - c0), c2 = c - c0 - c1;
      VMW(0); TIE(pb1);
      const u32 o = WS_C + (u32)(cg * 64 + lane) * 8u; const v2u v = {pk2(c0, c1), pk2(c2, bf2f(pb1))}; GST("global_store_dwordx2", o, v, ws, 0, "sc1");
    } else VMW(0);
    BAR();
    FLAG_ST(ufs);
    if (w == 0) { VMW(0); if (lane == 0) { const u32 o_ = WS_CF + (u32)cg * 128u; GST("global_store_dwordx2", o_, tag, ws, 0, "sc1"); } }
    STAMP(4);
    LT_DMA();
    __builtin_amdgcn_s_sleep(64);
    poll(WS_UF + (u32)(32 * w + l31) * 128u, ws, tag);
    BAR();
    GLD("global_load_dwordx4", uu, uo_, ws, 0, "sc1");
    DMA_2(0); DMA_2(1); DMA_2(2);
    { const u32 fx = lds_ld<u32>(LLT + 5120 + lane * 16 + 4); lds_st<u16>(tb + (fx >> 16), lds_ld<u16>(tb + (fx & 0xffffu))); }
    LT_READ(); K_PAD(); CV_ADDR();
    VMW(0); TIE(uu); U_PUT(); W2_READ(0);
    LGW0(); BAR();
    CV_PRE();
    STAMP(5);
  }
  v4u hf[14][2];
  const u32 hv0 = (u32)((2 * mt + (w >> 2)) * 112 + 14 * w) * 1024u + ho + 4096u + WS_H, hv1 = (u32)((2 * mt + 1 - (w >> 2)) * 112 + 14 * w) * 1024u + ho + 4096u + WS_H;
#define H_LD(s, ml) asm volatile("buffer_load_dwordx4 %0, %1, %2, %3 offen offset:%4 sc1" : "=v"(hf[s][ml]) : "v"((ml) ? hv1 : hv0), "s"(rs), "s"(FR((s) >> 1) << 11), "n"(((s) & 1) * 1024) : "memory")
  u32 yfo;
  {
    f4 ca[4];
    W2_TIE(0);
    v4u za, zb;
    { const u32 p = LU + (u32)ch * LUS + 992 - 48 + 512 * rh + 32 * l15 + 16 * g;
      const v4u u0 = lds_ld<v4u>(p + 16), u1 = lds_ld<v4u>(p), u2 = lds_ld<v4u>(p + 1024 + 16), u3 = lds_ld<v4u>(p + 1024);
      conv_load<0, 0>(c0, ca_); conv_load<0, 1>(c0, ca_); conv_load<0, 2>(c0, ca_); SB();
      M0SET(w4k, 98304); DMA_2I(3, 0); DMA_2I(3, 1);
      const f4 z0 = mfma(u0, t7, f4{0, 0, 0, 0}), z1 = mfma(u1, t9, f4{0, 0, 0, 0}), z2 = mfma(u2, t7, f4{0, 0, 0, 0}), z3 = mfma(u3, t9, f4{0, 0, 0, 0});
      SB();
      STAMP(8);
      CV_ROUND(0, c0, c1, { DMA_2I(3, 2); DMA_2I(3, 3); }, { M0SET(w4k, 0); DMA_2I(4, 0); _Pragma("unroll") for (int i = 0; i < 4; i++) { za[i] = pk2(z0[i], z1[i]); zb[i] = pk2(z2[i], z3[i]); } TIE(za); TIE(zb); },
               { DMA_2I(4, 1); W2_READ(1); }, { DMA_2I(4, 2); DMA_2I(4, 3); }); }
    STAMP(6);
    v2u fl; const u32 hpo = WS_HF + (u32)((2 * mt + ((lane >> 2) & 1)) * 32 + ((7 * w) >> 1) + (lane & 3)) * 128u;
    CV_ROUND(1, c1, c0, { W2_TIE(1); M0SET(w4k, 32768); DMA_2I(5, 0); }, { DMA_2I(5, 1); W2_READ(2); }, { DMA_2I(5, 2); DMA_2I(5, 3); }, { W2_TIE(2); M0SET(w4k, 65536); DMA_2I(6, 0); });
    CV_ROUND(2, c0, c1, { GLD("global_load_dwordx2", fl, hpo, ws, 0, "sc1"); DMA_2I(6, 1); }, DMA_2I(6, 2), DMA_2I(6, 3), {});
    STAMP(28);
    CV_ROUND(3, c1, c0, { VMW(3); TIE(fl); if (tag_miss(fl, tag)) { MARK(SLOW); poll(hpo, ws, tag); } STAMP(11); H_LD(0, 0); H_LD(0, 1); }, { H_LD(1, 0); H_LD(1, 1); }, { H_LD(2, 0); H_LD(2, 1); }, {});
    ca[2] = mfma(za, y1_, ca[2]); ca[3] = mfma(za, y2_, ca[3]); ca[3] = mfma(zb, y1_, ca[3]);
    STAMP(9);
    f4 k0, k1;
    if (rh) { lds_st<f4>(LEX + w * 2048 + ho, ca[0]); lds_st<f4>(LEX + w * 2048 + 1024 + ho, ca[1]); k0 = ca[2]; k1 = ca[3]; }
    else { lds_st<f4>(LEX + w * 2048 + ho, ca[2]); lds_st<f4>(LEX + w * 2048 + 1024 + ho, ca[3]); k0 = ca[0]; k1 = ca[1]; }
    LGW0(); BAR();
    k0 += lds_ld<f4>(LEX + (w ^ 2) * 2048 + ho); k1 += lds_ld<f4>(LEX + (w ^ 2) * 2048 + 1024 + ho);
    const int i0 = 4 * rh + th;
    const u32 o = WS_Y + (u32)dch * YS + i0 * 512 + 32 * l15 + 8 * g;
    { v2u v = {pk2(k0[0], k0[1]), pk2(k0[2], k0[3])}; GST("global_store_dwordx2", o, v, ws, 0, "sc1"); }
    { v2u v = {pk2(k1[0], k1[1]), pk2(k1[2], k1[3])}; GST("global_store_dwordx2", o, v, ws, 1024, "sc1"); }
    yfo = lane == 0 ? WS_YF + 4096 + (u32)((w & 3) * 512 + dch) * 128u : 0x40000000u;
    STAMP(10);
    H_LD(3, 0); H_LD(3, 1);
  }
  {
    f16v c2[2][2];
    v4u wn[2][2], xr, b2v, yf; v2u fl;
    const u32 ypo = WS_YF + (u32)((((mt >> 3) & 2) + ((mt >> 2) & 1)) * 512 + nt * 64 + (w & 1) * 32 + l31) * 128u;
    const u32 oo = (u32)(mt * 64 + (tid >> 3)) * 1024u + nt * 128 + (tid & 7) * 16;
#define W2OP(s, nl) ((s) < 6 ? wf2[(s) < 6 ? (s) >> 1 : 0][(s) & 1][nl] : wn[(s) & 1][nl])
#define G2_STEP(s, NV, X) do { VMW(NV); TIE(hf[s][0]); TIE(hf[s][1]); \
      c2[0][0] = mfma32(W2OP(s, 0), hf[s][0], (s) == 0 ? f16v{0} : c2[0][0]); SB(); if ((s) + 4 < 14) H_LD((s) + 4, 0); SB(); \
      c2[1][0] = mfma32(W2OP(s, 1), hf[s][0], (s) == 0 ? f16v{0} : c2[1][0]); \
      if ((s) >= 5 && (s) < 13) { wn[((s) + 1) & 1][0] = W2FRAG((((s) + 1) >> 1) & 3, ((s) + 1) & 1, 0); wn[((s) + 1) & 1][1] = W2FRAG((((s) + 1) >> 1) & 3, ((s) + 1) & 1, 1); } SG(0x8, 1); SB(); \
      c2[0][1] = mfma32(W2OP(s, 0), hf[s][1], (s) == 0 ? f16v{0} : c2[0][1]); SB(); if ((s) + 4 < 14) H_LD((s) + 4, 1); SB(); \
      c2[1][1] = mfma32(W2OP(s, 1), hf[s][1], (s) == 0 ? f16v{0} : c2[1][1]); SB(); X; SB(); } while (0)
    if (w >= 4) __builtin_amdgcn_s_setprio(1);
    G2_STEP(0, 8, {});
    G2_STEP(1, 8, {});
    G2_STEP(2, 8, { VMW(8); FLAG_ST(yfo); });
    __builtin_amdgcn_s_setprio(0);
    STAMP(29);
    G2_STEP(3, 7, {});
    G2_STEP(4, 7, {});
    G2_STEP(5, 7, {});
    G2_STEP(6, 7, {});
    STAMP(30);
    G2_STEP(7, 6, {});
    G2_STEP(8, 6, {});
    G2_STEP(9, 6, { GLD("global_load_dwordx4", xr, oo, a.x, 0, ""); const u32 o2 = nt * 128 + (tid & 7) * 16; GLD("global_load_dwordx4", b2v, o2, a.b2, 0, ""); GLD("global_load_dwordx2", fl, ypo, ws, 0, "sc1"); });
    STAMP(31);
    G2_STEP(10, 9, {});
    G2_STEP(11, 7, {});
    G2_STEP(12, 5, {});
    G2_STEP(13, 3, {});
    STAMP(32);
    VMW(0); TIE(fl); if (tag_miss(fl, tag)) { MARK(SLOW); poll(ypo, ws, tag); }
    { const u32 o = WS_Y + (u32)(nt * 64 + (w & 1) * 32 + l31) * YS + (u32)(mt * 64 + (w >> 1) * 16 + hh * 8) * 2u; GLD("global_load_dwordx4", yf, o, ws, 0, "sc1"); }
    STAMP(12);
    v4u id;
    { const int e = l31 - 16 * ((w >> 1) & 1) - 8 * hh;
#pragma unroll
      for (int i = 0; i < 4; i++) id[i] = (e >> 1) == i ? (0x3f80u << (16 * (e & 1))) : 0u; }
#define P_PUT(nl, ml) do { const int row_ = 32 * ((ml) ^ (w >> 2)) + l31; _Pragma("unroll") for (int qd = 0; qd < 4; qd++) \
      lds_st<f4>((row_ >> 4) * 32768 + w4k + (row_ & 15) * 256 + 16 * ((8 * ((nl) ^ (w & 1)) + 2 * qd + hh) ^ (row_ & 7)), \
                 f4{c2[nl][ml][4 * qd], c2[nl][ml][4 * qd + 1], c2[nl][ml][4 * qd + 2], c2[nl][ml][4 * qd + 3]}); } while (0)
    P_PUT(1, 0); P_PUT(0, 1); P_PUT(1, 1); SB();
    VMW(0); TIE(yf); TIE(xr); TIE(b2v);
    c2[0][0] = mfma32(yf, id, c2[0][0]);
    P_PUT(0, 0);
    STAMP(13);
    LGW0(); BAR();
    const int row = tid >> 3, c8 = tid & 7;
    const u32 ra = (row >> 4) * 32768 + (row & 15) * 256 + 16 * ((2 * c8) ^ (row & 7)), rb = ra ^ 16u;
    f4 s0 = lds_ld<f4>(ra), s1 = lds_ld<f4>(rb);
#pragma unroll
    for (int ww = 1; ww < 8; ww++) { s0 += lds_ld<f4>(ra + ww * 4096); s1 += lds_ld<f4>(rb + ww * 4096); }
    v4u o;
    o.x = pk2(s0[0] + bf2f(b2v.x) + bf2f(xr.x), s0[1] + bfhi(b2v.x) + bfhi(xr.x));
    o.y = pk2(s0[2] + bf2f(b2v.y) + bf2f(xr.y), s0[3] + bfhi(b2v.y) + bfhi(xr.y));
    o.z = pk2(s1[0] + bf2f(b2v.z) + bf2f(xr.z), s1[1] + bfhi(b2v.z) + bfhi(xr.z));
    o.w = pk2(s1[2] + bf2f(b2v.w) + bf2f(xr.w), s1[3] + bfhi(b2v.w) + bfhi(xr.w));
    GST("global_store_dwordx4", oo, o, a.out, 0, "");
  }
  STAMP(14);
  if (bid == 255) { poll(WS_UF + (u32)(tid & 255) * 128u, ws, tag); BAR(); if (tid == 0) { u32 z = 0; GST("global_store_dwordx2", z, tag, ws, 0, "sc1"); } }
  asm volatile(".Lfk_end:");
}

extern "C" void kernel_launch(void* const* d_in, const int* in_sizes, int n_in, void* d_out, int out_size, void* d_ws, size_t ws_size, hipStream_t stream) {
  Args a;
  a.x = (const u16*)d_in[0]; a.are = (const u16*)d_in[1]; a.aim = (const u16*)d_in[2]; a.bre = (const u16*)d_in[3]; a.bim = (const u16*)d_in[4];
  a.cre = (const u16*)d_in[5]; a.cim = (const u16*)d_in[6]; a.ldt = (const u16*)d_in[7]; a.dsk = (const u16*)d_in[8];
  a.w1 = (const u16*)d_in[10]; a.b1 = (const u16*)d_in[11]; a.w2 = (const u16*)d_in[12]; a.b2 = (const u16*)d_in[13];
  a.out = (u16*)d_out; a.ws = (char*)d_ws; a.pad = a.x;
  (void)hipFuncSetAttribute((const void*)fused_kernel, hipFuncAttributeMaxDynamicSharedMemorySize, LDS_BYTES);
  fused_kernel<<<256, 512, LDS_BYTES, stream>>>(a);
}
```

```cpp
#include <hip/hip_runtime.h>
#include <stdint.h>

typedef unsigned u32; typedef unsigned long long u64; typedef unsigned short u16;
typedef u32 v4u __attribute__((ext_vector_type(4)));
typedef u32 v2u __attribute__((ext_vector_type(2)));
typedef float f4 __attribute__((ext_vector_type(4)));
typedef float f2 __attribute__((ext_vector_type(2)));
typedef float f16v __attribute__((ext_vector_type(16)));
typedef short s4 __attribute__((ext_vector_type(4)));
typedef __bf16 bf8 __attribute__((ext_vector_type(8)));
typedef __bf16 bf2 __attribute__((ext_vector_type(2)));
#define LAS __attribute__((address_space(3)))
#define DI __device__ __forceinline__

constexpr u32 WS_CF = 4096, WS_S = 16384, WS_C = 32768;
constexpr u32 WS_UF = 65536, WS_ST = 131072  , WS_HF = 262144, WS_YF = 524288;
constexpr u32 WS_U = 1u << 20, US = 8192 + 128, YS = 4096 + 128, WS_Y = WS_U + 256 * US, WS_W = WS_Y + 512 * YS, WS_H = WS_W + (2u << 20), WS_END = WS_H + 64 * 112 * 1024;
constexpr u32 SP = 131072, KA = 0, KB = 640, KC = 960, KQ = 1232, KD = 1616, TBS = 1664;
constexpr u32 LXN = 65536, LSS = 98304, LTS = SP + 2 * TBS, LT1 = LTS + 16384, LWR = SP + 30720;
constexpr u32 LU = SP + 2 * TBS, LUS = 5120, LLT = LU + 2 * LUS, LEX = SP + 16384;
constexpr u32 LDS_BYTES = 163840;
static_assert(LT1 + 8192 <= LWR && LTS % 64 == 0 && LLT <= LEX, "LDS map");

constexpr int rne(int j, int sh) { int q = j >> sh, rem = j & ((1 << sh) - 1), h = 1 << (sh - 1); if (rem > h || (rem == h && (q & 1))) q++; return q << sh; }
constexpr int rnd_bf(int j) { return j <= 256 ? j : rne(j, j < 512 ? 1 : j < 1024 ? 2 : 3); }
constexpr int i1(int j) { return (rne(j, 1) - 256) / 2; }
constexpr int ic(int j) { return (rnd_bf(j) - 512) / 4; }
constexpr u32 sel1(int i) { return i < 0 ? 0x0c0cu : (u32)(2 * i) | (u32)(2 * i + 1) << 8; }
constexpr u32 sel(int lo, int hi) { return sel1(lo) | sel1(hi) << 16; }
constexpr int a3(int m) { return m ? KQ + 128 + 2 * (m - 1) : KC + 256; }
struct alignas(128) Tabs { u32 wr[512]; u32 lt[6 * 256]; };
constexpr Tabs make_tabs() {
  Tabs t{};
  for (int tl = 0; tl < 4; tl++) for (int l = 0; l < 64; l++) {
    const int sg = (l & 15) + 16 * (tl & 1), g = l >> 4; int ad[4] = {};
    for (int r = 0; r < 4; r++)
      ad[r] = !(tl >> 1) ? (g < 2 ? KA + 2 * (16 + 8 * sg + 4 * g + r) : g == 2 ? KB + 2 * (4 * sg + r) : KC + 2 * (2 * sg + (r & 1) + 64 * (r >> 1)))
                         : (g == 0 ? a3(sg + 32 * r) : (g == 1 && r == 0 && sg == 0) ? a3(128) : KD);
    t.wr[(tl * 64 + l) * 2] = (u32)ad[0] | (u32)ad[1] << 16; t.wr[(tl * 64 + l) * 2 + 1] = (u32)ad[2] | (u32)ad[3] << 16;
  }
  for (int l = 0; l < 64; l++) {
    const int o = 16 + (l & 15) - 8 * (l >> 4);
    const int a0 = 16 + o - 7, j1 = 32 * 9 + o, j2 = 32 * 17 + o, jx = 32 * 32 + o;
    const int b1 = i1(j1 - 7) & ~1, b2 = ic(j2 - 7) & ~1, bx = jx - 7 <= 1028 ? ic(jx - 7) & ~1 : 0;
    t.lt[l * 4 + 0] = sel((a0 & 1) + 1, a0 & 1);
    t.lt[l * 4 + 1] = sel(i1(j1 - 4) - b1, i1(j1 - 5) - b1);
    t.lt[l * 4 + 2] = sel(i1(j1 - 6) - b1, i1(j1 - 7) - b1);
    t.lt[l * 4 + 3] = (u32)(KA + 2 * (a0 & ~1)) | (u32)(KB + 2 * b1 - 32) << 16;
    for (int i = 0; i < 4; i++) {
      t.lt[256 + l * 4 + i] = sel(ic(j2 - 2 * i) - b2, ic(j2 - 2 * i - 1) - b2);
      t.lt[512 + l * 4 + i] = sel(jx - 2 * i <= 1028 ? ic(jx - 2 * i) - bx : -1, jx - 2 * i - 1 <= 1028 ? ic(jx - 2 * i - 1) - bx : -1);
      const int p = o - 2 * i, q = o + 8 - 2 * i;
      t.lt[768 + l * 4 + i] = (p >= 5 && p <= 11 ? 0x3f80u : 0u) | (p - 1 >= 5 && p - 1 <= 11 ? 0x3f800000u : 0u);
      t.lt[1024 + l * 4 + i] = (q >= 12 && q <= 20 ? 0x3f80u : 0u) | (q - 1 >= 12 && q - 1 <= 20 ? 0x3f800000u : 0u);
    }
    t.lt[1280 + l * 4 + 0] = (u32)(KC + 2 * b2 - 16) | (u32)(KC + 2 * bx) << 16;
    t.lt[1280 + l * 4 + 1] = l < 32 ? (u32)(KB + 2 * i1(256 + l)) | (u32)(KA + 2 * (16 + 256 + l)) << 16
                           : l < 49 ? (u32)(KC + 2 * ic(512 + 2 * (l - 32))) | (u32)(KB + 2 * (128 + l - 32)) << 16 : (u32)KD | (u32)KD << 16;
  }
  return t;
}
__device__ const Tabs g_tabs = make_tabs();

struct Args { const u16 *are, *aim, *bre, *bim, *cre, *cim, *x, *w1, *w2; char* ws; const u16 *ldt, *dsk, *b1, *b2; u16* out; const u16* pad; };

DI float bf2f(u32 v) { return __uint_as_float(v << 16); }
DI float bfhi(u32 v) { return __uint_as_float(v & 0xffff0000u); }
DI u32 pk2(float a, float b) { bf2 c; c[0] = (__bf16)a; c[1] = (__bf16)b; return __builtin_bit_cast(u32, c); }
DI float rbf(float a) { return bf2f(pk2(a, a) & 0xffffu); }
DI f4 mfma(v4u a, v4u b, f4 c) { return __builtin_amdgcn_mfma_f32_16x16x32_bf16(__builtin_bit_cast(bf8, a), __builtin_bit_cast(bf8, b), c, 0, 0, 0); }
DI f16v mfma32(v4u a, v4u b, f16v c) { return __builtin_amdgcn_mfma_f32_32x32x16_bf16(__builtin_bit_cast(bf8, a), __builtin_bit_cast(bf8, b), c, 0, 0, 0); }
DI float dot2(u32 a, u32 b, float c) { return __builtin_amdgcn_fdot2_f32_bf16(__builtin_bit_cast(bf2, a), __builtin_bit_cast(bf2, b), c, false); }
template <class T> DI T lds_ld(u32 a) { return *(const LAS T*)(size_t)a; }
template <class T> DI void lds_st(u32 a, T v) { *(LAS T*)(size_t)a = v; }
DI v2u lds_tr(u32 a) { return __builtin_bit_cast(v2u, __builtin_amdgcn_ds_read_tr16_b64_v4i16((LAS s4*)(size_t)a)); }
DI v4u lds_tr2(u32 a, u32 b) { v2u x = lds_tr(a), y = lds_tr(b); return v4u{x.x, x.y, y.x, y.y}; }
DI v4u lds_2x64(u32 a) { v2u x = lds_ld<v2u>(a); asm volatile(""); v2u y = lds_ld<v2u>(a + 8); return v4u{x.x, x.y, y.x, y.y}; }
DI float xor32_add(float v) { auto r = __builtin_amdgcn_permlane32_swap(__float_as_uint(v), __float_as_uint(v), false, false); return __uint_as_float(r[0]) + __uint_as_float(r[1]); }
#define DPP_ADD(v, ctrl) v += __uint_as_float(__builtin_amdgcn_update_dpp(0u, __float_as_uint(v), ctrl, 0xf, 0xf, true))
DI float sum32(float v) {
  DPP_ADD(v, 0xB1); DPP_ADD(v, 0x4E); DPP_ADD(v, 0x141); DPP_ADD(v, 0x140);
  auto r = __builtin_amdgcn_permlane16_swap(__float_as_uint(v), __float_as_uint(v), false, false); return __uint_as_float(r[0]) + __uint_as_float(r[1]);
}
DI void cmul2(f2& r0, f2 a0, f2 w0, f2& r1, f2 a1, f2 w1) {
  asm("v_pk_mul_f32 %0, %2, %3 op_sel:[0,0] op_sel_hi:[1,0]\n\tv_pk_mul_f32 %1, %4, %5 op_sel:[0,0] op_sel_hi:[1,0]\n\t"
      "v_pk_fma_f32 %0, %2, %3, %0 op_sel:[1,1,0] op_sel_hi:[0,1,1] neg_lo:[1,0,0]\n\tv_pk_fma_f32 %1, %4, %5, %1 op_sel:[1,1,0] op_sel_hi:[0,1,1] neg_lo:[1,0,0]"
      : "=&v"(r0), "=&v"(r1) : "v"(a0), "v"(w0), "v"(a1), "v"(w1));
}
DI f2 cexp2(f2 x) {
  float e = __builtin_amdgcn_exp2f(x.x), c = __builtin_amdgcn_cosf(x.y), s = __builtin_amdgcn_sinf(x.y);
  return f2{c, s} * e;
}
DI void ln_stats(float s, float q, float& mb, float& sd) {
  const float mean = s * (1.f / 512.f);
  const float var = fmaxf(q * (1.f / 512.f) - mean * mean, 0.f);
  sd = rbf(__builtin_amdgcn_sqrtf(rbf(rbf(var) + 1e-5f))); mb = rbf(mean);
}
#define VMW(n) asm volatile("s_waitcnt vmcnt(%0)" ::"n"(n) : "memory")
#define LGW0() asm volatile("s_waitcnt lgkmcnt(0)" ::: "memory")
#define BAR() asm volatile("s_barrier" ::: "memory")
#define TIE(x) asm volatile("" : "+v"(x))
#define MARK(x) asm volatile("; PATH_" #x)
#define SB() __builtin_amdgcn_sched_barrier(0)
#define SG(m, n) __builtin_amdgcn_sched_group_barrier(m, n, 0)
#define GLD(op, dst, voff, sbase, imm, fl) asm volatile(op " %0, %1, %2 offset:%3 " fl : "=v"(dst) : "v"(voff), "s"(sbase), "n"(imm) : "memory")
#define GST(op, voff, src, sbase, imm, fl) asm volatile(op " %0, %1, %2 offset:%3 " fl "\n\ts_nop 1" ::"v"(voff), "v"(src), "s"(sbase), "n"(imm) : "memory")
#define M0SET(lbase, limm) asm volatile("s_add_u32 m0, %0, %1\n\ts_nop 0" ::"s"(lbase), "n"(limm) : "memory", "scc")
#define DMA(voff, srd, soff, off) asm volatile("buffer_load_dwordx4 %0, %1, %2 offen offset:%3 lds" ::"v"(voff), "s"(srd), "s"(soff), "n"(off) : "memory")
DI v4u make_srd(const void* p, u32 recs) { const u64 a_ = (u64)p - 4096; return v4u{(u32)a_, (u32)(a_ >> 32), recs, 0x20000u}; }
#define STIE(x) asm volatile("" : "+s"(x))
DI bool tag_miss(v2u f, v2u tag) { return __builtin_expect(__builtin_amdgcn_ballot_w64(f.x != tag.x || f.y != tag.y) != 0, 0); }
DI void poll(u32 voff, const char* ws, v2u tag) {
#pragma nounroll
  for (int it = 0; it < (1 << 16); it++) {
    v2u f;
    asm volatile("global_load_dwordx2 %0, %1, %2 sc1\n\ts_waitcnt vmcnt(0)" : "=v"(f) : "v"(voff), "s"(ws) : "memory");
    if (__builtin_amdgcn_ballot_w64(f.x != tag.x || f.y != tag.y) == 0) break;
    __builtin_amdgcn_s_sleep(6);
  }
}
#define STAMP(i)

struct CvA { u32 ua, a0, a1, a2; };
struct CvS { v4u s01, s2; };
struct ConvRaw { v4u A[8]; u32 e[5], f[3], q[2][2]; };
DI u32 prm(u32 hi, u32 lo, u32 s) { return __builtin_amdgcn_perm(hi, lo, s); }
DI v4u perm4(u32 hi, u32 lo, v4u s) { return v4u{prm(hi, lo, s.x), prm(hi, lo, s.y), prm(hi, lo, s.z), prm(hi, lo, s.w)}; }
DI v4u frag0(const u32 (&e)[5], u32 s) { return v4u{prm(e[4], e[3], s), prm(e[3], e[2], s), prm(e[2], e[1], s), prm(e[1], e[0], s)}; }
DI v4u frag1(const u32 (&f)[3], u32 sa, u32 sb) { return v4u{prm(f[2], f[1], sa), prm(f[2], f[1], sb), prm(f[1], f[0], sa), prm(f[1], f[0], sb)}; }
template <int RR, int P> DI void conv_load(ConvRaw& c, const CvA& a) {
  if (P == 0) {
#pragma unroll
    for (int i = 0; i < 5; i++) if (RR) c.e[i] = lds_ld<u32>(a.a0 + 64 * RR + 4 * i);
    c.A[7] = lds_ld<v4u>(a.ua + 64 * (3 - RR) + 512 * 7); c.A[5] = lds_ld<v4u>(a.ua + 64 * (3 - RR) + 512 * 5);
  }
  if (P == 1) {
    c.A[3] = lds_ld<v4u>(a.ua + 64 * (3 - RR) + 512 * 3); c.A[1] = lds_ld<v4u>(a.ua + 64 * (3 - RR) + 512);
    if (RR) {
#pragma unroll
      for (int i = 0; i < 3; i++) c.f[i] = lds_ld<u32>(a.a1 + 32 * RR + 4 * i);
    }
  }
  if (P == 2) {
#pragma unroll
    for (int i = 6; i >= 0; i -= 2) c.A[i] = lds_ld<v4u>(a.ua + 64 * (3 - RR) + 512 * i);
  }
  if (P == 3) {
#pragma unroll
    for (int m = 2; m < 4; m++) if (RR || m != 2) {
      const u32 p = a.a2 + 16 * RR + 128 * (m - 2);
      c.q[m - 2][0] = lds_ld<u32>(p); c.q[m - 2][1] = lds_ld<u32>(p + 4);
    }
  }
}
template <int RR, int M> DI v4u conv_b(const ConvRaw& c, const CvS& s, v4u x1, v4u x2) {
  return (RR == 0 && M == 1) ? x1 : (RR == 0 && M == 2) ? x2 : M == 1 ? frag1(c.f, s.s01.y, s.s01.z) : perm4(c.q[M < 2 ? 0 : M - 2][1], c.q[M < 2 ? 0 : M - 2][0], s.s2);
}
template <int M, bool Z> DI void conv_mm(f4 (&acc)[4], const ConvRaw& c, v4u B) {
#pragma unroll
  for (int t = 3; t >= 0; t--) if (2 * t + 1 >= M) acc[t] = mfma(B, c.A[2 * t + 1 - M], Z ? f4{0, 0, 0, 0} : acc[t]);
}
#define CV_SG(n) do { _Pragma("unroll") for (int i_ = 0; i_ < (n); i_++) { SG(0x8, 1); SG(0x100, 2); SG(0x2, 2); } SB(); } while (0)
#define CV_ROUND(RR, c, n, X0, X1, X2, X3) do { \
    v4u b1_ = conv_b<RR, 1>(c, cs_, x1, x2); if ((RR) < 3) conv_load<((RR) + 1) & 3, 0>(n, ca_); conv_mm<0, (RR) == 0>(ca, c, b0); CV_SG(4); TIE(b1_); X0; SB(); \
    v4u b2_ = conv_b<RR, 2>(c, cs_, x1, x2); if ((RR) < 3) conv_load<((RR) + 1) & 3, 1>(n, ca_); conv_mm<1, false>(ca, c, b1_); CV_SG(4); TIE(b2_); X1; SB(); \
    v4u b3_ = conv_b<RR, 3>(c, cs_, x1, x2); if ((RR) < 3) conv_load<((RR) + 1) & 3, 2>(n, ca_); conv_mm<2, false>(ca, c, b2_); CV_SG(3); TIE(b3_); X2; SB(); \
    if ((RR) < 3) { conv_load<((RR) + 1) & 3, 3>(n, ca_); b0 = frag0(n.e, cs_.s01.x); } \
    conv_mm<3, false>(ca, c, b3_); if ((RR) == 0) conv_mm<4, false>(ca, c, x4); CV_SG((RR) == 0 ? 5 : 3); TIE(b0); X3; SB(); } while (0)

typedef u32 v16u __attribute__((ext_vector_type(16)));
#define KP(v, i) ((const char*)(((u64)v[2 * (i) + 1] << 32) | v[2 * (i)]))
__global__ __launch_bounds__(512, 2) void fused_kernel(Args) {
  STAMP(0);
  v16u k0_, k1_;
  asm volatile("s_load_dwordx16 %0, %2, 0x0\n\ts_load_dwordx16 %1, %2, 0x40" : "=&s"(k0_), "=&s"(k1_) : "s"(__builtin_amdgcn_kernarg_segment_ptr()) : "memory");
  const int tid = threadIdx.x, lane = tid & 63, w = __builtin_amdgcn_readfirstlane(tid >> 6), l15 = lane & 15, g = lane >> 4, l31 = lane & 31, hh = lane >> 5;
  const int bid = blockIdx.x, xcd = bid & 7, idx = bid >> 3;
  const int oct = xcd * 32 + idx;
  const int ch = w >> 2, dch = 2 * oct + ch;
  const bool tw = ((w >> 1) ^ ch) & 1;
  const int lr = 4 * ch + 2 * (w & 1) + hh;
  const int md = (w & 1) * 64 + lane;
  u32 xo = (u32)(oct * 8 + lr) * 1024u + (u32)l31 * 16u, po = (u32)(dch * 128 + md) * 2u;
  TIE(xo); TIE(po);
  if (w < 2) { const u32 o = (u32)tid * 16u, d = LWR + w * 1024;
    asm volatile("s_mov_b32 m0, %2\n\ts_nop 0\n\tglobal_load_lds_dwordx4 %0, %1" ::"v"(o), "s"(g_tabs.wr), "s"(d) : "memory");
  } else if (w < 4) {
    u64 pc; u32 len; asm volatile("s_getpc_b64 %0\n.Lfk_beg:\n\ts_mov_b32 %1, .Lfk_end-.Lfk_beg" : "=s"(pc), "=s"(len));
    const u32 ln = (idx & 15) * 2 + w - 2, o = min((ln * 6 + lane) * 128u, len), o2 = min(ln * 2 + (lane & 1), 47u) * 128u;
    if (lane < 6) asm volatile("s_mov_b32 m0, %4\n\ts_nop 0\n\tglobal_load_lds_dword %0, %1\n\tglobal_load_lds_dword %2, %3" ::"v"(o), "s"(pc), "v"(o2), "s"(g_tabs.lt), "n"(SP + KD) : "memory");
  }
  const bool gw = __builtin_expect(idx < 28, 1);
  const int j4 = (idx * 37) >> 8, i7 = idx - 7 * j4;
  const int rt = (xcd >> 1) * 4 + j4, ft = (xcd & 1) * 7 + i7, rot = i7 ^ (2 * j4);
  const int mt = xcd * 4 + (idx >> 3), nt = idx & 7;
  const u32 w4k = (u32)w * 4096u;
  const int cg = (xcd & 1) * 14 + min((xcd >> 1) * 4 + (idx - 28), 13);
  u32 vx0, vx1, vw0, vw1, xs0, xs1, ws0, ws1, ws2;
  if (gw) {
    vx0 = (u32)(rt * 128 + w * 16 + (lane >> 3)) * 1024u + 16u * ((lane & 7) ^ (lane >> 4)) + 4096u; vx1 = (vx0 ^ 64u) + 8192u - 1024u;
    vw0 = (u32)(8 * w + g) * 3584u + 256u * ft + 16u * (l15 ^ (4 * g)) + 4096u - 2048u; vw1 = vw0 + 4 * 3584 - 1024;
  } else { vx0 = (u32)(64 * w + (lane >> 3)) * 3584u + 128u * cg + 16u * (lane & 7) + 4096u; vx1 = vx0 + 8 * 3584 - 1024; vw0 = vx0 + 16 * 3584 - 2048; vw1 = vx0 + 24 * 3584 - 3072; }
  xs0 = gw ? rot * 128 : 0; xs1 = gw ? (rot ^ 1) * 128 : 32 * 3584; ws0 = gw ? rot * (64 * 3584) : 0; ws1 = gw ? (rot ^ 1) * (64 * 3584) : 32 * 3584; ws2 = gw ? (rot ^ 2) * (64 * 3584) : 0;
  const int fq = bid * 7 + w;
  u32 wo = (u32)((fq >> 4) * 16 + 4 * hh) * 1024u + (fq & 15) * 64u + l31 * 2u + (w < 7 ? 4096u : 0x40000000u);
  TIE(vx0); TIE(vx1); TIE(vw0); TIE(vw1); TIE(wo);
  asm volatile("s_waitcnt lgkmcnt(0)" : "+s"(k0_), "+s"(k1_)::"memory");
  struct { const char *are, *aim, *bre, *bim, *cre, *cim, *x, *w1, *w2, *ws, *ldt, *dsk, *b1, *b2, *out; } a =
    {KP(k0_, 0), KP(k0_, 1), KP(k0_, 2), KP(k0_, 3), KP(k0_, 4), KP(k0_, 5), KP(k0_, 6), KP(k0_, 7), KP(k1_, 0), KP(k1_, 1), KP(k1_, 2), KP(k1_, 3), KP(k1_, 4), KP(k1_, 5), KP(k1_, 6)};
  const char* ws = a.ws;
  u32 pa, pb, pc_, pd, pe, pg; v4u xr0, xr1;
  GLD("global_load_short_d16_hi", pa, po, a.are, 0, ""); GLD("global_load_short_d16_hi", pb, po, a.aim, 0, "");
  u64 ep; u32 sl, sk_;
  { const u32 b4 = oct * 4;
    asm volatile("s_load_dwordx2 %0, %3, 0x0\n\ts_load_dword %1, %4, %6\n\ts_load_dword %2, %5, %6" : "=&s"(ep), "=&s"(sl), "=&s"(sk_) : "s"(ws), "s"(a.ldt), "s"(a.dsk), "s"(b4) : "memory"); }
  if (tw) { MARK(KT); GLD("global_load_dwordx4", xr0, xo, a.x, 0, ""); GLD("global_load_dwordx4", xr1, xo, a.x, 512, ""); }
  else { MARK(KS); GLD("global_load_short_d16_hi", pc_, po, a.bre, 0, ""); GLD("global_load_short_d16_hi", pd, po, a.bim, 0, ""); GLD("global_load_short_d16_hi", pe, po, a.cre, 0, ""); GLD("global_load_short_d16_hi", pg, po, a.cim, 0, ""); }
  SB();
  const v4u rw = make_srd(a.w1, 512 * 3584 + 4096), rx = make_srd(gw ? a.x : a.w1, gw ? 2048 * 1024 + 4096 : 512 * 3584 + 4096), r2 = make_srd(a.w2, 1792 * 1024 + 4096), rs = make_srd(ws, WS_END + 4096);
#define XS(c) (u32)(((c) ^ rot) * 128)
#define WS_(c) (u32)(((c) ^ rot) * (64 * 3584))
#define DMA_X(c) do { DMA(vx0, rx, XS(c), 0); DMA(vx1, rx, XS(c), 1024); } while (0)
#define DMA_W(c) do { DMA(vw0, rw, WS_(c), 2048); DMA(vw1, rw, WS_(c), 3072); } while (0)
  BAR();
  M0SET(w4k, 0); DMA(vx0, rx, xs0, 0); DMA(vx1, rx, xs0, 1024); DMA(vw0, rw, ws0, 2048); DMA(vw1, rw, ws0, 3072);
  STAMP(16);
  const int mw = w & 3, nh = w >> 2, q4 = l15 >> 2;
  u32 xa[2][4], wa[2][2], ksa, kta;
  { const int row = 32 * mw + l31;
#pragma unroll
    for (int r = 0; r < 4; r++) { xa[0][r] = (row >> 4) * 4096 + (row & 15) * 128 + 16 * ((2 * r + hh) ^ ((row >> 1) & 7)); TIE(xa[0][r]); }
#pragma unroll
    for (int nl = 0; nl < 2; nl++) { wa[0][nl] = hh * 4096 + 2048 + q4 * 256 + 64 * ((2 * nh + nl) ^ q4) + 32 * (g & 1) + 8 * (lane & 3); TIE(wa[0][nl]); }
    const u32 o = (2 * g + (q4 >> 1)) * 64 + 8 * (lane & 1), o1 = o + 16 * (((lane >> 1) & 3) ^ g);
    ksa = LSS + ch * 16384 + (w & 1) * 8192 + o1;
    kta = (w & 2) ? LT1 + o + 16 * ((2 * ch + (q4 & 1)) ^ g) : LTS + ch * 8192 + o1;
    TIE(ksa); TIE(kta); }
  u32 wso = WS_W + (u32)((((fq >> 1) & 7) * 112 + (fq >> 4)) * 2 + (fq & 1)) * 1024u + lane * 16u + (w < 7 ? 4096u : 0x40000000u), uso = WS_U + (u32)oct * US + (u32)tid * 16u;
  TIE(wso); TIE(uso);
  asm volatile("s_waitcnt lgkmcnt(0)" : "+s"(ep), "+s"(sl), "+s"(sk_)::"memory");
  v2u tag;
  { u64 z = (ep + 0x9E3779B97F4A7C15ull) * 0xBF58476D1CE4E5B9ull; z ^= z >> 29; STIE(z); tag.x = (u32)z; tag.y = (u32)(z >> 32); }
  float dt;
  { const float xx = __uint_as_float(ch ? sl & 0xffff0000u : sl << 16), t = xx * 1.4426950216293335f, r = __builtin_fmaf(xx, 1.9259629911266175e-8f, __builtin_fmaf(xx, 1.4426950216293335f, -t));
    const float e = __builtin_amdgcn_exp2f(t); dt = rbf(__builtin_fmaf(e * r, 0.6931471805599453f, e)); TIE(dt); }
  float k00 = __uint_as_float((tid & 255) == 0 ? (ch ? sk_ & 0xffff0000u : sk_ << 16) : 0u); TIE(k00);
  SB();
  M0SET(w4k, 32768); DMA(vw0, rw, ws1, 2048); DMA(vw1, rw, ws1, 3072);
  VMW(6); TIE(pa); TIE(pb);
  STAMP(1);
  v2u sv = {0, 0};

  const u32 tb = SP + (u32)ch * TBS;
  {
    const u32 dk = md * 64 + 16 * ((lane >> 1) & 3);
    float arl = dt * bfhi(pa) * 1.4426950408889634f; arl = fminf(fmaxf(arl, -1e30f), 1e30f);
    float f = dt * bfhi(pb) * 0.15915494309189535f; f -= rintf(f);
    const f2 af = {arl, f};
    if (!tw) {
      MARK(KS);
      TIE(pc_); TIE(pd); TIE(pe); TIE(pg);
      const f2 z8 = cexp2(af * 8.f), bb = f2{bfhi(pc_), bfhi(pd)} * dt, cc = {bfhi(pe), bfhi(pg)};
      f2 z16, g0, g1, t_;
      cmul2(z16, z8, z8, g0, cc, bb); cmul2(g1, g0, z8, t_, g0, z8);
      const u32 d = LSS + ch * 16384 + dk;
#pragma unroll
      for (int c = 0; c < 4; c++) {
        v4u re, im;
#pragma unroll
        for (int i = 0; i < 4; i++) { f2 n0 = g0, n1 = g1; if (c < 3 || i < 3) cmul2(n0, g0, z16, n1, g1, z16); re[i] = pk2(g0.x, g1.x); im[i] = pk2(g0.y, g1.y); g0 = n0; g1 = n1; }
        lds_st<v4u>((d ^ (16 * (c & 1))) + (c >> 1) * 8192, re); lds_st<v4u>((d ^ (32 + 16 * (c & 1))) + (c >> 1) * 8192, im);
      }
    } else {
      MARK(KT);
      TIE(xr0); TIE(xr1);
      float ss = 0.f, sq = 0.f, ss2 = 0.f, sq2 = 0.f;
#pragma unroll
      for (int e = 0; e < 4; e++) { const u32 v = xr0[e], v1 = xr1[e]; ss = dot2(v, 0x3f803f80u, ss); sq = dot2(v, v, sq); ss2 = dot2(v1, 0x3f803f80u, ss2); sq2 = dot2(v1, v1, sq2); }
      ss = sum32(ss + ss2); sq = sum32(sq + sq2);
      float mb, sd; ln_stats(ss, sq, mb, sd);
      const f2 z1 = cexp2(af), y1 = cexp2(af * 256.f);
      f2 z2, y2, z3, z4, y3, y4, z5, z6, z7, a2, a4, a6, b4, c4, y5, y6, y7, y8;
      cmul2(z2, z1, z1, y2, y1, y1); cmul2(z3, z2, z1, z4, z2, z2); cmul2(y3, y2, y1, y4, y2, y2); cmul2(z5, z4, z1, z6, z4, z2); cmul2(z7, z4, z3, a2, y1, z2);
      const u32 d = LTS + ch * 8192 + dk, d1 = (LT1 + dk) ^ (32 * ch);
      lds_st<v4u>(d, v4u{pk2(1.f, z1.x), pk2(z2.x, z3.x), pk2(z4.x, z5.x), pk2(z6.x, z7.x)});
      lds_st<v4u>(d ^ 32, v4u{pk2(0.f, -z1.y), pk2(-z2.y, -z3.y), pk2(-z4.y, -z5.y), pk2(-z6.y, -z7.y)});
      sv = v2u{pk2(-mb, -mb), pk2(-mb, sd)};
      cmul2(a4, y1, z4, a6, y1, z6); cmul2(b4, y2, z4, c4, y3, z4); cmul2(y5, y4, y1, y6, y4, y2); cmul2(y7, y4, y3, y8, y4, y4);
      lds_st<v4u>(d ^ 16, v4u{pk2(y1.x, a2.x), pk2(a4.x, a6.x), pk2(y2.x, b4.x), pk2(y3.x, c4.x)});
      lds_st<v4u>(d ^ 48, v4u{pk2(-y1.y, -a2.y), pk2(-a4.y, -a6.y), pk2(-y2.y, -b4.y), pk2(-y3.y, -c4.y)});
      lds_st<v4u>(d1, v4u{pk2(y4.x, y5.x), pk2(y6.x, y7.x), pk2(y8.x, y8.x), 0u});
      lds_st<v4u>(d1 ^ 16, v4u{pk2(-y4.y, -y5.y), pk2(-y6.y, -y7.y), pk2(-y8.y, -y8.y), 0u});
      const float rstd = __builtin_amdgcn_rcpf(sd), nmr = -mb * rstd;
      v4u o0, o1;
#pragma unroll
      for (int e = 0; e < 4; e++) { const u32 v = xr0[e], v1 = xr1[e]; const f2 t = f2{bf2f(v), bfhi(v)} * rstd + nmr, t1 = f2{bf2f(v1), bfhi(v1)} * rstd + nmr; o0[e] = pk2(t.x, t.y); o1[e] = pk2(t1.x, t1.y); }
      lds_st<v4u>(LXN + (lr  __H0__ ) + ((lr + l31) << 4), o0); lds_st<v4u>(LXN + 512 + (lr << 12) + ((lr + l31) << 4), o1);
    }
  }
  STAMP(2);
#define LT_DMA() do { const u32 d_ = LLT + min(w, 6) * 1024; asm volatile("s_mov_b32 m0, %2\n\ts_nop 0\n\tglobal_load_lds_dwordx4 %0, %1" ::"v"(lto), "s"(g_tabs.lt), "s"(d_) : "memory"); } while (0)
  DMA(vx0, rx, xs1, 0); DMA(vx1, rx, xs1, 1024);
  u32 wq[8];
#pragma unroll
  for (int j = 0; j < 8; j++) asm volatile("buffer_load_ushort %0, %1, %2, %3 offen offset:%4" : "=v"(wq[j]) : "v"(wo), "s"(r2), "s"((j >> 2) * 8192), "n"((j & 3) * 1024) : "memory");
  M0SET(w4k, 65536);
  STAMP(40);
  VMW(12);
  STAMP(41);
  LGW0(); BAR();
  STAMP(18);
  u32 ta2, ta2h, afo, so, co, uo_, lto, ufs; v2u tagv = tag;
#define FLAG_ST(o) asm volatile("buffer_store_dwordx2 %0, %1, %2, 0 offen sc1" ::"v"(tagv), "v"(o), "s"(rs) : "memory")
    f16v acc[2];
    v4u xf[2][2], wf[2][2][2];
#define G1_RD(st, r_) do { constexpr int sl_ = ((st) >> 1) % 3, p_ = sl_ >> 1; constexpr u32 sb_ = (sl_ & 1) * 32768, wo_ = sb_ + ((st) & 1) * 16384 + (r_) * 8192; \
      xf[(st) & 1][r_] = lds_ld<v4u>(xa[p_][2 * ((st) & 1) + (r_)] + sb_); \
      wf[(st) & 1][0][r_] = lds_tr2(wa[p_][0] + wo_, wa[p_][0] + wo_ + 1024); wf[(st) & 1][1][r_] = lds_tr2(wa[p_][1] + wo_, wa[p_][1] + wo_ + 1024); } while (0)
#define G1_MM(st, n_, r_) acc[n_] = mfma32(wf[(st) & 1][n_][r_], xf[(st) & 1][r_], (st) + (r_) == 0 ? f16v{0} : acc[n_])
#define G1_STEP(st, X2, X3) do { \
      G1_MM(st, 0, 0); if ((st) < 15) G1_RD((st) + 1, 0); SG(0x8, 1); SG(0x100, 5); SB(); \
      G1_MM(st, 1, 0); if ((st) < 15) G1_RD((st) + 1, 1); SG(0x8, 1); SG(0x100, 5); SB(); \
      G1_MM(st, 0, 1); SB(); X2; SB(); \
      G1_MM(st, 1, 1); SB(); X3; SB(); } while (0)
#define G1_SYNC(NV) do { VMW(NV); LGW0(); BAR(); SB(); } while (0)
  v4u kA[2], kB[2]; f4 kk[2]; v2u wa_;
#define K_RD(k) do { kA[(k) & 1] = lds_tr2(kta + (k) * 1024, kta + (k) * 1024 + 512); kB[(k) & 1] = lds_tr2(ksa + (k) * 1024, ksa + (k) * 1024 + 512); } while (0)
#define K_MM(k) do { kk[(k) & 1] = mfma(kA[(k) & 1], kB[(k) & 1], (k) == 0 ? f4{k00, 0, 0, 0} : (k) == 1 ? f4{0, 0, 0, 0} : kk[(k) & 1]); TIE(kk[(k) & 1]); } while (0)
#define K_PUT() do { const f4 k_ = kk[0] + kk[1]; const u32 p0 = pk2(k_[0], k_[1]), p1 = pk2(k_[2], k_[3]); if (lane < ((w & 2) ? 17 : 64)) { \
      lds_st<u16>(tb + (wa_.x & 0xffffu), (u16)p0); lds_st<u16>(tb + (wa_.x >> 16), (u16)(p0 >> 16)); \
      lds_st<u16>(tb + (wa_.y & 0xffffu), (u16)p1); lds_st<u16>(tb + (wa_.y >> 16), (u16)(p1 >> 16)); } } while (0)
  {
    G1_RD(0, 0); G1_RD(0, 1);
    const u32 ra = LXN + (tid & 1) * 16384 + (((tid >> 1) * 4) ^ ((tid & 1) * 64));
    const v4u un = {lds_ld<u32>(ra), lds_ld<u32>(ra + 4096), lds_ld<u32>(ra + 8192), lds_ld<u32>(ra + 12288)};
    SB();
    DMA(vw0, rw, ws2, 2048); DMA(vw1, rw, ws2, 3072);
    G1_STEP(0, { GST("global_store_dwordx4", uso, un, ws, 0, "sc1");
                 const u32 o = (tw && l31 == 0) ? WS_S + 4096 + (u32)(oct * 8 + lr) * 8u : 0x40000000u; asm volatile("buffer_store_dwordx2 %0, %1, %2, 0 offen sc1" ::"v"(sv), "v"(o), "s"(rs) : "memory"); }, K_RD(0));
  }
#define T_ST() do { v4u wt; _Pragma("unroll") for (int j = 0; j < 4; j++) { u32 t_; asm volatile("v_lshl_or_b32 %0, %2, 16, %1" : "=v"(t_) : "v"(wq[2 * j]), "v"(wq[2 * j + 1])); wt[j] = t_; } \
    asm volatile("buffer_store_dwordx4 %0, %1, %2, 0 offen sc1\n\ts_nop 1" ::"v"(wt), "v"(wso), "s"(rs) : "memory"); } while (0)
#define SET_A() do { _Pragma("unroll") for (int r = 0; r < 4; r++) { xa[1][r] = xa[0][r] + 65536; TIE(xa[1][r]); } wa[1][0] = wa[0][0] + 65536; wa[1][1] = wa[0][1] + 65536; TIE(wa[1][0]); TIE(wa[1][1]); } while (0)
#define SET_B() do { ufs = tid == 448 ? WS_UF + 4096 + (u32)oct * 128u : 0x40000000u; TIE(ufs); TIE(tagv); } while (0)
#define SET_C() do { ta2 = w4k + lane * 16 + (w & 1) * 1024; ta2h = ta2 + 65536; TIE(ta2); TIE(ta2h); uo_ = WS_U + (u32)(tid >> 1) * US + (u32)oct * 32u + (tid & 1) * 16u; TIE(uo_); } while (0)
#define SET_D() do { afo = WS_UF + (u32)(32 * w + lane) * 128u; const u32 t_ = WS_CF + (u32)(2 * ft + nh) * 128u; afo = hh ? t_ : afo; TIE(afo); } while (0)
#define SET_F() do { lto = (u32)min(tid, 383) * 16u; TIE(lto); wa_ = lds_ld<v2u>(LWR + (tid & 255) * 8); } while (0)
#define SET_E() do { so = WS_S + (u32)(rt * 128 + mw * 32 + l31) * 8u; co = WS_C + (u32)(ft * 128 + nh * 64 + l31) * 8u; TIE(so); TIE(co); } while (0)
  const u32 seq = (u32)(0x65432106543210ull >> (4 * (nt == 7 ? 3 : nt)));
#define FR(f) ((seq >> (4 * (f))) & 7u)
  const u32 ho = (u32)lane * 16u, w2s = WS_W + 4096 + (u32)(nt * 112 + 14 * w) * 2048u;
#define DMA_2I(f, i) asm volatile("buffer_load_dwordx4 %0, %1, %2 offen offset:%3 sc1 lds" ::"v"(ho), "s"(rs), "s"(w2s + (FR(f) << 12)), "n"((i) * 1024) : "memory")
#define SL2(f) ((0x1023 >> (4 * ((f) & 3))) & 3)
#define M0_2(f) M0SET(w4k, SL2(f) * 32768)
#define DMA_2(f) do { M0_2(f); DMA_2I(f, 0); DMA_2I(f, 1); DMA_2I(f, 2); DMA_2I(f, 3); } while (0)
  v4u wf2[3][2][2];
#define W2FRAG(f, s2, nl) lds_ld<v4u>(((SL2(f) & 2 ? ta2h : ta2) ^ ((nl) * 1024)) + (SL2(f) & 1) * 32768 + (s2) * 2048)
#define W2_READ(f) do { _Pragma("unroll") for (int s_ = 0; s_ < 2; s_++) _Pragma("unroll") for (int n_ = 0; n_ < 2; n_++) wf2[f][s_][n_] = W2FRAG(f, s_, n_); } while (0)
#define W2_TIE(f) do { LGW0(); _Pragma("unroll") for (int s_ = 0; s_ < 2; s_++) _Pragma("unroll") for (int n_ = 0; n_ < 2; n_++) TIE(wf2[f][s_][n_]); } while (0)
  const int th = w & 1, rh = (w >> 1) & 1;
  CvS cs_; v4u sx, t7, t9; u32 ad;
#define LT_READ() do { cs_.s01 = lds_ld<v4u>(LLT + ho); cs_.s2 = lds_ld<v4u>(LLT + 1024 + ho); sx = lds_ld<v4u>(LLT + 2048 + ho); \
    t7 = lds_ld<v4u>(LLT + 3072 + ho); t9 = lds_ld<v4u>(LLT + 4096 + ho); ad = lds_ld<u32>(LLT + 5120 + ho); } while (0)
  CvA ca_; ConvRaw c0, c1; v4u x1, x2, x4, b0, y1_, y2_; u32 pe_[5], pf_[3], pq_[2];
#define CV_ADDR() do { ca_.ua = LU + (u32)ch * LUS + 448 - 192 - 256 * rh + 512 * th + 32 * l15 + 16 * g; \
    ca_.a0 = tb + (cs_.s01.w & 0xffffu) + 256 * rh; ca_.a1 = tb + (cs_.s01.w >> 16) + 128 * rh; ca_.a2 = tb + (ad & 0xffffu) + 64 * rh; } while (0)
#define CV_PRE() do { const u32 p1_ = rh ? ca_.a2 : ca_.a0 + 512, p2_ = ca_.a1 + (rh ? 0 : 256), p3_ = tb + (ad >> 16), p4_ = tb + KQ + 128 - 12 + 64 * (th - rh) + 4 * l15 - 16 * g; \
    _Pragma("unroll") for (int i = 0; i < 5; i++) { pe_[i] = lds_ld<u32>(p1_ + 4 * i); c0.e[i] = lds_ld<u32>(ca_.a0 + 4 * i); } \
    _Pragma("unroll") for (int i = 0; i < 3; i++) pf_[i] = lds_ld<u32>(p2_ + 4 * i); pq_[0] = lds_ld<u32>(p3_); pq_[1] = lds_ld<u32>(p3_ + 4); \
    _Pragma("unroll") for (int i = 0; i < 4; i++) { y1_[i] = lds_ld<u32>(p4_ + 12 - 4 * i); y2_[i] = lds_ld<u32>(p4_ + 128 + 12 - 4 * i); } \
    conv_load<0, 3>(c0, ca_); \
    b0 = frag0(c0.e, cs_.s01.x); \
    if (rh) { x1 = frag1(pf_, cs_.s01.y, cs_.s01.z); x2 = perm4(pe_[1], pe_[0], cs_.s2); x4 = v4u{0, 0, 0, 0}; } \
    else { x1 = frag0(pe_, cs_.s01.x); x2 = frag1(pf_, cs_.s01.y, cs_.s01.z); x4 = perm4(pq_[1], pq_[0], sx); } TIE(b0); TIE(x1); TIE(x2); TIE(x4); TIE(y1_); TIE(y2_); TIE(t7); TIE(t9); } while (0)
#define K_PAD() do { if (w < 2) lds_st<v4u>(lane < 62 ? LU + w * LUS + ho : SP + w * TBS + ho - 992, v4u{0, 0, 0, 0}); else if (tid < 144) lds_st<v4u>(SP + (lane >> 3) * TBS + KQ + (lane & 7) * 16, v4u{0, 0, 0, 0}); } while (0)
#define U_PUT() do { lds_st<v2u>(LU + 992 + tid * 8, v2u{prm(uu.y, uu.x, 0x05040100u), prm(uu.w, uu.z, 0x05040100u)}); \
    lds_st<v2u>(LU + LUS + 992 + tid * 8, v2u{prm(uu.y, uu.x, 0x07060302u), prm(uu.w, uu.z, 0x07060302u)}); } while (0)
  v4u uu;

  if (gw) {
    MARK(GW);
    v2u fl;
    v2u st2, cq0, cq1; u32 fx, fv;
    STAMP(3);
    G1_SYNC(12); STAMP(20);
    G1_STEP(1, { DMA_X(2); K_MM(0); K_RD(1); }, { M0SET(w4k, 0); DMA(vx0, rx, XS(3), 0); });
    G1_STEP(2, { DMA(vx1, rx, XS(3), 1024); VMW(8); T_ST(); K_MM(1); K_RD(2); }, { DMA_W(3); SET_A(); });
    G1_SYNC(5); STAMP(21);
    G1_STEP(3, { M0SET(w4k, 32768); DMA(vx0, rx, XS(4), 0); K_MM(2); K_RD(3); }, { DMA(vx1, rx, XS(4), 1024); SET_B(); });
    G1_STEP(4, { DMA(vw0, rw, WS_(4), 2048); K_MM(3); K_RD(4); }, DMA(vw1, rw, WS_(4), 3072));
    G1_SYNC(4); STAMP(22);
    G1_STEP(5, { FLAG_ST(ufs); M0SET(w4k, 65536); DMA(vx0, rx, XS(5), 0); K_MM(4); K_RD(5); }, DMA(vx1, rx, XS(5), 1024));
    G1_STEP(6, { DMA(vw0, rw, WS_(5), 2048); K_MM(5); K_RD(6); }, DMA(vw1, rw, WS_(5), 3072));
    G1_SYNC(5); STAMP(23);
    G1_STEP(7, { M0SET(w4k, 0); DMA(vx0, rx, XS(6), 0); K_MM(6); K_RD(7); }, { DMA(vx1, rx, XS(6), 1024); SET_F(); });
    G1_STEP(8, { DMA(vw0, rw, WS_(6), 2048); K_MM(7); }, { DMA(vw1, rw, WS_(6), 3072); SET_D(); });
    G1_SYNC(4); STAMP(24);
    G1_STEP(9, { M0SET(w4k, 32768); DMA_X(7); }, { DMA_W(7); K_PUT(); });
    G1_STEP(10, LT_DMA(), { K_PAD(); GLD("global_load_dwordx2", fl, afo, ws, 0, "sc1"); });
    G1_SYNC(6); STAMP(25);
    G1_STEP(11, SET_E(), SET_C());
    G1_STEP(12, {}, { VMW(0); TIE(fl); if (tag_miss(fl, tag)) { MARK(SLOW); poll(afo, ws, tag); } });
    G1_SYNC(63); STAMP(26);
    GLD("global_load_dwordx2", st2, so, ws, 0, "sc1"); GLD("global_load_dwordx2", cq0, co, ws, 0, "sc1"); GLD("global_load_dwordx2", cq1, co, ws, 256, "sc1");
    G1_STEP(13, { M0_2(0); DMA_2I(0, 0); fx = lds_ld<u32>(LLT + 5120 + lane * 16 + 4); }, DMA_2I(0, 1));
    G1_STEP(14, { DMA_2I(0, 2); fv = lds_ld<u16>(tb + (fx & 0xffffu)); }, { DMA_2I(0, 3); GLD("global_load_dwordx4", uu, uo_, ws, 0, "sc1"); });
    G1_STEP(15, { M0_2(1); DMA_2I(1, 0); DMA_2I(1, 1); lds_st<u16>(tb + (fx >> 16), (u16)fv); }, { DMA_2I(1, 2); DMA_2I(1, 3); });
    VMW(9); TIE(st2); TIE(cq0); TIE(cq1);
    STAMP(4);
    __builtin_amdgcn_s_setprio(2);
    const float sd = bfhi(st2.y);
    { const u32 km = hh ? 0u : ~0u; const v4u sb = {st2.x & km, st2.y & km, 0u, 0u};
      acc[0] = mfma32(v4u{cq0.x, cq0.y, 0u, 0u}, sb, acc[0]); acc[1] = mfma32(v4u{cq1.x, cq1.y, 0u, 0u}, sb, acc[1]); }
    const float rstd = __builtin_amdgcn_rcpf(sd), k1 = -2.3022082f * rstd, k2 = -0.10294324f * rstd * rstd * rstd;
    const int M32 = rt * 4 + mw;
    const char* hb = ws + WS_H + (size_t)(M32 * 112 + ft * 8 + nh * 4) * 1024;
#pragma unroll
    for (int nl = 0; nl < 2; nl++) {
#pragma unroll
      for (int s = 0; s < 2; s++) {
        v4u hp;
#pragma unroll
        for (int e = 0; e < 4; e++) {
          const f2 v = {acc[nl][8 * s + 2 * e], acc[nl][8 * s + 2 * e + 1]};
          const f2 z = v * (v * v * k2 + k1);
          const f2 d = f2{__builtin_amdgcn_exp2f(z.x), __builtin_amdgcn_exp2f(z.y)} * sd + sd;
          const f2 o = v * f2{__builtin_amdgcn_rcpf(d.x), __builtin_amdgcn_rcpf(d.y)};
          hp[e] = pk2(o.x, o.y);
        }
        GST("global_store_dwordx4", ho, hp, hb, nl * 2048 + s * 1024, "sc1");
        if (nl == 0 && s == 0) { M0_2(2); DMA_2I(2, 0); DMA_2I(2, 1); }
        if (nl == 0 && s == 1) { DMA_2I(2, 2); DMA_2I(2, 3); }
      }
    }
    __builtin_amdgcn_s_setprio(0);
    STAMP(5);
    VMW(12); TIE(uu); U_PUT();
    u32 hfs = lane == 0 ? WS_HF + 4096 + (u32)(M32 * 32 + ft * 2 + nh) * 128u : 0x40000000u; TIE(hfs);
    LT_READ(); W2_READ(0); CV_ADDR(); CV_PRE();
    VMW(0);
    FLAG_ST(hfs);
    STAMP(7);
    LGW0(); BAR();
  } else {
    MARK(LN);
    SET_B(); SET_C();
    u32 pb1; { const u32 o3 = (u32)(cg * 64 + lane) * 2u; GLD("global_load_ushort", pb1, o3, a.b1, 0, ""); }
    VMW(5); T_ST();
    { v4u se; f4 ca = {0, 0, 0, 0};
#pragma unroll
      for (int i = 0; i < 4; i++) se[i] = (l15 >> 1) == i ? (0x3f80u << (16 * (l15 & 1))) : 0u;
#pragma unroll
      for (int i = 0; i < 8; i++) ca = mfma(se, lds_ld<v4u>(w4k + ho + (i >> 2) * 32768 + (i & 3) * 1024), ca);
      if (lane < 32) lds_st<f4>(w4k + (l15 >> 3) * 256 + (lane & 7) * 32 + g * 16, ca); }
    LGW0(); BAR();
    if (w == 0) {
      float c = 0.f;
#pragma unroll
      for (int i = 0; i < 16; i++) c += lds_ld<float>((i >> 1) * 4096 + (i & 1) * 256 + lane * 4);
      const float c0 = rbf(c), c1 = rbf(c - c0), c2 = c - c0 - c1;
      VMW(0); TIE(pb1);
      const u32 o = WS_C + (u32)(cg * 64 + lane) * 8u; const v2u v = {pk2(c0, c1), pk2(c2, bf2f(pb1))}; GST("global_store_dwordx2", o, v, ws, 0, "sc1");
    } else VMW(0);
    BAR();
    FLAG_ST(ufs);
    if (w == 0) { VMW(0); if (lane == 0) { const u32 o_ = WS_CF + (u32)cg * 128u; GST("global_store_dwordx2", o_, tag, ws, 0, "sc1"); } }
    STAMP(4);
    SET_F();
#pragma unroll
    for (int k = 0; k < 8; k++) { if (k < 7) K_RD(k + 1); K_MM(k); }
    K_PUT();
    LGW0(); BAR();
    LT_DMA();
    __builtin_amdgcn_s_sleep(40);
    poll(WS_UF + (u32)(32 * w + l31) * 128u, ws, tag);
    BAR();
    GLD("global_load_dwordx4", uu, uo_, ws, 0, "sc1");
    DMA_2(0); DMA_2(1); DMA_2(2);
    { const u32 fx = lds_ld<u32>(LLT + 5120 + lane * 16 + 4); lds_st<u16>(tb + (fx >> 16), lds_ld<u16>(tb + (fx & 0xffffu))); }
    LT_READ(); K_PAD(); CV_ADDR();
    VMW(0); TIE(uu); U_PUT(); W2_READ(0);
    LGW0(); BAR();
    CV_PRE();
    STAMP(5);
  }
  v4u hf[14][2];
  const u32 hv0 = (u32)((2 * mt + (w >> 2)) * 112 + 14 * w) * 1024u + ho + 4096u + WS_H, hv1 = (u32)((2 * mt + 1 - (w >> 2)) * 112 + 14 * w) * 1024u + ho + 4096u + WS_H;
#define H_LD(s, ml) asm volatile("buffer_load_dwordx4 %0, %1, %2, %3 offen offset:%4 sc1" : "=v"(hf[s][ml]) : "v"((ml) ? hv1 : hv0), "s"(rs), "s"(FR((s) >> 1) << 11), "n"(((s) & 1) * 1024) : "memory")
  u32 yfo;
  {
    f4 ca[4];
    W2_TIE(0);
    v4u za, zb;
    { const u32 p = LU + (u32)ch * LUS + 992 - 48 + 512 * rh + 32 * l15 + 16 * g;
      const v4u u0 = lds_ld<v4u>(p + 16), u1 = lds_ld<v4u>(p), u2 = lds_ld<v4u>(p + 1024 + 16), u3 = lds_ld<v4u>(p + 1024);
      conv_load<0, 0>(c0, ca_); conv_load<0, 1>(c0, ca_); conv_load<0, 2>(c0, ca_); SB();
      M0_2(3); DMA_2I(3, 0); DMA_2I(3, 1);
      const f4 z0 = mfma(u0, t7, f4{0, 0, 0, 0}), z1 = mfma(u1, t9, f4{0, 0, 0, 0}), z2 = mfma(u2, t7, f4{0, 0, 0, 0}), z3 = mfma(u3, t9, f4{0, 0, 0, 0});
      SB();
      STAMP(8);
      CV_ROUND(0, c0, c1, { DMA_2I(3, 2); DMA_2I(3, 3); }, { M0_2(4); DMA_2I(4, 0); _Pragma("unroll") for (int i = 0; i < 4; i++) { za[i] = pk2(z0[i], z1[i]); zb[i] = pk2(z2[i], z3[i]); } TIE(za); TIE(zb); },
               { DMA_2I(4, 1); W2_READ(1); }, { DMA_2I(4, 2); DMA_2I(4, 3); }); }
    STAMP(6);
    v2u fl; const u32 hpo = WS_HF + (u32)((2 * mt + ((lane >> 2) & 1)) * 32 + ((7 * w) >> 1) + (lane & 3)) * 128u;
    CV_ROUND(1, c1, c0, { W2_TIE(1); M0_2(5); DMA_2I(5, 0); }, { DMA_2I(5, 1); W2_READ(2); }, { DMA_2I(5, 2); DMA_2I(5, 3); }, { W2_TIE(2); M0_2(6); DMA_2I(6, 0); });
    CV_ROUND(2, c0, c1, { GLD("global_load_dwordx2", fl, hpo, ws, 0, "sc1"); DMA_2I(6, 1); }, DMA_2I(6, 2), DMA_2I(6, 3), {});
    STAMP(28);
    CV_ROUND(3, c1, c0, { VMW(3); TIE(fl); if (tag_miss(fl, tag)) { MARK(SLOW); poll(hpo, ws, tag); } STAMP(11); H_LD(0, 0); H_LD(0, 1); }, { H_LD(1, 0); H_LD(1, 1); }, { H_LD(2, 0); H_LD(2, 1); }, {});
    ca[2] = mfma(za, y1_, ca[2]); ca[3] = mfma(za, y2_, ca[3]); ca[3] = mfma(zb, y1_, ca[3]);
    STAMP(9);
    f4 k0, k1;
    if (rh) { lds_st<f4>(LEX + w * 2048 + ho, ca[0]); lds_st<f4>(LEX + w * 2048 + 1024 + ho, ca[1]); k0 = ca[2]; k1 = ca[3]; }
    else { lds_st<f4>(LEX + w * 2048 + ho, ca[2]); lds_st<f4>(LEX + w * 2048 + 1024 + ho, ca[3]); k0 = ca[0]; k1 = ca[1]; }
    LGW0(); BAR();
    k0 += lds_ld<f4>(LEX + (w ^ 2) * 2048 + ho); k1 += lds_ld<f4>(LEX + (w ^ 2) * 2048 + 1024 + ho);
    const int i0 = 4 * rh + th;
    const u32 o = WS_Y + (u32)dch * YS + i0 * 512 + 32 * l15 + 8 * g;
    { v2u v = {pk2(k0[0], k0[1]), pk2(k0[2], k0[3])}; GST("global_store_dwordx2", o, v, ws, 0, "sc1"); }
    { v2u v = {pk2(k1[0], k1[1]), pk2(k1[2], k1[3])}; GST("global_store_dwordx2", o, v, ws, 1024, "sc1"); }
    yfo = lane == 0 ? WS_YF + 4096 + (u32)((w & 3) * 512 + dch) * 128u : 0x40000000u;
    STAMP(10);
    H_LD(3, 0); H_LD(3, 1);
  }
  {
    f16v c2[2][2];
    v4u wn[2][2], xr, b2v, yf; v2u fl;
    const u32 ypo = WS_YF + (u32)((((mt >> 3) & 2) + ((mt >> 2) & 1)) * 512 + nt * 64 + (w & 1) * 32 + l31) * 128u;
    const u32 oo = (u32)(mt * 64 + (tid >> 3)) * 1024u + nt * 128 + (tid & 7) * 16;
#define W2OP(s, nl) ((s) < 6 ? wf2[(s) < 6 ? (s) >> 1 : 0][(s) & 1][nl] : wn[(s) & 1][nl])
#define G2_STEP(s, NV, X) do { VMW(NV); TIE(hf[s][0]); TIE(hf[s][1]); \
      c2[0][0] = mfma32(W2OP(s, 0), hf[s][0], (s) == 0 ? f16v{0} : c2[0][0]); SB(); if ((s) + 4 < 14) H_LD((s) + 4, 0); SB(); \
      c2[1][0] = mfma32(W2OP(s, 1), hf[s][0], (s) == 0 ? f16v{0} : c2[1][0]); \
      if ((s) >= 5 && (s) < 13) { wn[((s) + 1) & 1][0] = W2FRAG((((s) + 1) >> 1) & 3, ((s) + 1) & 1, 0); wn[((s) + 1) & 1][1] = W2FRAG((((s) + 1) >> 1) & 3, ((s) + 1) & 1, 1); } SG(0x8, 1); SB(); \
      c2[0][1] = mfma32(W2OP(s, 0), hf[s][1], (s) == 0 ? f16v{0} : c2[0][1]); SB(); if ((s) + 4 < 14) H_LD((s) + 4, 1); SB(); \
      c2[1][1] = mfma32(W2OP(s, 1), hf[s][1], (s) == 0 ? f16v{0} : c2[1][1]); SB(); X; SB(); } while (0)
    if (w >= 4) __builtin_amdgcn_s_setprio(1);
    G2_STEP(0, 8, {});
    G2_STEP(1, 8, {});
    G2_STEP(2, 8, { VMW(8); FLAG_ST(yfo); });
    __builtin_amdgcn_s_setprio(0);
    STAMP(29);
    G2_STEP(3, 7, {});
    G2_STEP(4, 7, {});
    G2_STEP(5, 7, {});
    G2_STEP(6, 7, {});
    STAMP(30);
    G2_STEP(7, 6, {});
    G2_STEP(8, 6, {});
    G2_STEP(9, 6, { GLD("global_load_dwordx4", xr, oo, a.x, 0, ""); const u32 o2 = nt * 128 + (tid & 7) * 16; GLD("global_load_dwordx4", b2v, o2, a.b2, 0, ""); GLD("global_load_dwordx2", fl, ypo, ws, 0, "sc1"); });
    STAMP(31);
    G2_STEP(10, 9, {});
    G2_STEP(11, 7, {});
    G2_STEP(12, 5, {});
    G2_STEP(13, 3, {});
    STAMP(32);
    VMW(0); TIE(fl); if (tag_miss(fl, tag)) { MARK(SLOW); poll(ypo, ws, tag); }
    { const u32 o = WS_Y + (u32)(nt * 64 + (w & 1) * 32 + l31) * YS + (u32)(mt * 64 + (w >> 1) * 16 + hh * 8) * 2u; GLD("global_load_dwordx4", yf, o, ws, 0, "sc1"); }
    STAMP(12);
    v4u id;
    { const int e = l31 - 16 * ((w >> 1) & 1) - 8 * hh;
#pragma unroll
      for (int i = 0; i < 4; i++) id[i] = (e >> 1) == i ? (0x3f80u << (16 * (e & 1))) : 0u; }
#define P_PUT(nl, ml) do { const int row_ = 32 * ((ml) ^ (w >> 2)) + l31; _Pragma("unroll") for (int qd = 0; qd < 4; qd++) \
      lds_st<f4>((row_ >> 4) * 32768 + w4k + (row_ & 15) * 256 + 16 * ((8 * ((nl) ^ (w & 1)) + 2 * qd + hh) ^ (row_ & 7)), \
                 f4{c2[nl][ml][4 * qd], c2[nl][ml][4 * qd + 1], c2[nl][ml][4 * qd + 2], c2[nl][ml][4 * qd + 3]}); } while (0)
    P_PUT(1, 0); P_PUT(0, 1); P_PUT(1, 1); SB();
    VMW(0); TIE(yf); TIE(xr); TIE(b2v);
    c2[0][0] = mfma32(yf, id, c2[0][0]);
    P_PUT(0, 0);
    STAMP(13);
    LGW0(); BAR();
    const int row = tid >> 3, c8 = tid & 7;
    const u32 ra = (row >> 4) * 32768 + (row & 15) * 256 + 16 * ((2 * c8) ^ (row & 7)), rb = ra ^ 16u;
    f4 s0 = lds_ld<f4>(ra), s1 = lds_ld<f4>(rb);
#pragma unroll
    for (int ww = 1; ww < 8; ww++) { s0 += lds_ld<f4>(ra + ww * 4096); s1 += lds_ld<f4>(rb + ww * 4096); }
    v4u o;
    o.x = pk2(s0[0] + bf2f(b2v.x) + bf2f(xr.x), s0[1] + bfhi(b2v.x) + bfhi(xr.x));
    o.y = pk2(s0[2] + bf2f(b2v.y) + bf2f(xr.y), s0[3] + bfhi(b2v.y) + bfhi(xr.y));
    o.z = pk2(s1[0] + bf2f(b2v.z) + bf2f(xr.z), s1[1] + bfhi(b2v.z) + bfhi(xr.z));
    o.w = pk2(s1[2] + bf2f(b2v.w) + bf2f(xr.w), s1[3] + bfhi(b2v.w) + bfhi(xr.w));
    GST("global_store_dwordx4", oo, o, a.out, 0, "");
  }
  STAMP(14);
  if (bid == 255) { poll(WS_UF + (u32)(tid & 255) * 128u, ws, tag); BAR(); if (tid == 0) { u32 z = 0; GST("global_store_dwordx2", z, tag, ws, 0, "sc1"); } }
  asm volatile(".Lfk_end:");
}

extern "C" void kernel_launch(void* const* d_in, const int* in_sizes, int n_in, void* d_out, int out_size, void* d_ws, size_t ws_size, hipStream_t stream) {
  Args a;
  a.x = (const u16*)d_in[0]; a.are = (const u16*)d_in[1]; a.aim = (const u16*)d_in[2]; a.bre = (const u16*)d_in[3]; a.bim = (const u16*)d_in[4];
  a.cre = (const u16*)d_in[5]; a.cim = (const u16*)d_in[6]; a.ldt = (const u16*)d_in[7]; a.dsk = (const u16*)d_in[8];
  a.w1 = (const u16*)d_in[10]; a.b1 = (const u16*)d_in[11]; a.w2 = (const u16*)d_in[12]; a.b2 = (const u16*)d_in[13];
  a.out = (u16*)d_out; a.ws = (char*)d_ws; a.pad = a.x;
  (void)hipFuncSetAttribute((const void*)fused_kernel, hipFuncAttributeMaxDynamicSharedMemorySize, LDS_BYTES);
  fused_kernel<<<256, 512, LDS_BYTES, stream>>>(a);
}
```

```cpp
#include <hip/hip_runtime.h>
#include <stdint.h>

typedef unsigned u32; typedef unsigned long long u64; typedef unsigned short u16;
typedef u32 v4u __attribute__((ext_vector_type(4)));
typedef u32 v2u __attribute__((ext_vector_type(2)));
typedef float f4 __attribute__((ext_vector_type(4)));
typedef float f2 __attribute__((ext_vector_type(2)));
typedef float f16v __attribute__((ext_vector_type(16)));
typedef short s4 __attribute__((ext_vector_type(4)));
typedef __bf16 bf8 __attribute__((ext_vector_type(8)));
typedef __bf16 bf2 __attribute__((ext_vector_type(2)));
#define LAS __attribute__((address_space(3)))
#define DI __device__ __forceinline__

constexpr u32 WS_CF = 4096, WS_C = 32768;
constexpr u32 WS_UF = 65536, WS_ST = 131072  , WS_HF = 262144, WS_YF = 524288;
constexpr u32 WS_U = 1u << 20, US = 8192 + 128, YS = 4096 + 128, WS_Y = WS_U + 256 * US, WS_W = WS_Y + 512 * YS, WS_H = WS_W + (2u << 20), WS_END = WS_H + 64 * 112 * 1024;
constexpr u32 SP = 131072, KA = 0, KB = 640, KC = 960, KQ = 1232, KD = 1616, TBS = 1664;
constexpr u32 LXN = 65536, LSS = 98304, LTS = SP + 2 * TBS + 1024, LT1 = LTS + 16384 + 1024, LWR = SP + 30720;
constexpr u32 LU = LTS - 992, LUS = LT1 - LTS, LLT = LTS + 4096, LEX = LLT, LEX2 = LT1 + 4096;
constexpr u32 LDS_BYTES = 163840;
static_assert(LT1 + 8192 <= LWR && LTS % 64 == 0 && LT1 % 64 == 0 && LEX + 6 * 2048 <= LT1 - 992 && LEX2 + 4096 <= LDS_BYTES, "LDS map");

constexpr int rne(int j, int sh) { int q = j >> sh, rem = j & ((1 << sh) - 1), h = 1 << (sh - 1); if (rem > h || (rem == h && (q & 1))) q++; return q << sh; }
constexpr int rnd_bf(int j) { return j <= 256 ? j : rne(j, j < 512 ? 1 : j < 1024 ? 2 : 3); }
constexpr int i1(int j) { return (rne(j, 1) - 256) / 2; }
constexpr int ic(int j) { return (rnd_bf(j) - 512) / 4; }
constexpr u32 sel1(int i) { return i < 0 ? 0x0c0cu : (u32)(2 * i) | (u32)(2 * i + 1) << 8; }
constexpr u32 sel(int lo, int hi) { return sel1(lo) | sel1(hi) << 16; }
constexpr int a3(int m) { return m ? KQ + 128 + 2 * (m - 1) : KC + 256; }
struct alignas(128) Tabs { u32 wr[512]; u32 lt[6 * 256]; };
constexpr Tabs make_tabs() {
  Tabs t{};
  for (int tl = 0; tl < 4; tl++) for (int l = 0; l < 64; l++) {
    const int sg = (l & 15) + 16 * (tl & 1), g = l >> 4; int ad[4] = {};
    for (int r = 0; r < 4; r++)
      ad[r] = !(tl >> 1) ? (g < 2 ? KA + 2 * (16 + 8 * sg + 4 * g + r) : g == 2 ? KB + 2 * (4 * sg + r) : KC + 2 * (2 * sg + (r & 1) + 64 * (r >> 1)))
                         : (g == 0 ? a3(sg + 32 * r) : (g == 1 && r == 0 && sg == 0) ? a3(128) : (int)(LT1 + 8192 - SP - TBS) + 4 * l);
    t.wr[(tl * 64 + l) * 2] = (u32)ad[0] | (u32)ad[1] << 16; t.wr[(tl * 64 + l) * 2 + 1] = (u32)ad[2] | (u32)ad[3] << 16;
  }
  for (int l = 0; l < 64; l++) {
    const int o = 16 + (l & 15) - 8 * (l >> 4);
    const int a0 = 16 + o - 7, j1 = 32 * 9 + o, j2 = 32 * 17 + o, jx = 32 * 32 + o;
    const int b1 = i1(j1 - 7) & ~1, b2 = ic(j2 - 7) & ~1, bx = jx - 7 <= 1028 ? ic(jx - 7) & ~1 : 0;
    t.lt[l * 4 + 0] = sel((a0 & 1) + 1, a0 & 1);
    t.lt[l * 4 + 1] = sel(i1(j1 - 4) - b1, i1(j1 - 5) - b1);
    t.lt[l * 4 + 2] = sel(i1(j1 - 6) - b1, i1(j1 - 7) - b1);
    t.lt[l * 4 + 3] = (u32)(KA + 2 * (a0 & ~1)) | (u32)(KB + 2 * b1 - 32) << 16;
    for (int i = 0; i < 4; i++) {
      t.lt[256 + l * 4 + i] = sel(ic(j2 - 2 * i) - b2, ic(j2 - 2 * i - 1) - b2);
      t.lt[512 + l * 4 + i] = sel(jx - 2 * i <= 1028 ? ic(jx - 2 * i) - bx : -1, jx - 2 * i - 1 <= 1028 ? ic(jx - 2 * i - 1) - bx : -1);
      const int p = o - 2 * i, q = o + 8 - 2 * i;
      t.lt[768 + l * 4 + i] = (p >= 5 && p <= 11 ? 0x3f80u : 0u) | (p - 1 >= 5 && p - 1 <= 11 ? 0x3f800000u : 0u);
      t.lt[1024 + l * 4 + i] = (q >= 12 && q <= 20 ? 0x3f80u : 0u) | (q - 1 >= 12 && q - 1 <= 20 ? 0x3f800000u : 0u);
    }
    t.lt[1280 + l * 4 + 0] = (u32)(KC + 2 * b2 - 16) | (u32)(KC + 2 * bx) << 16;
    t.lt[1280 + l * 4 + 1] = l < 32 ? (u32)(KB + 2 * i1(256 + l)) | (u32)(KA + 2 * (16 + 256 + l)) << 16
                           : l < 49 ? (u32)(KC + 2 * ic(512 + 2 * (l - 32))) | (u32)(KB + 2 * (128 + l - 32)) << 16 : (u32)KD | (u32)KD << 16;
  }
  return t;
}
__device__ const Tabs g_tabs = make_tabs();

struct Args { const u16 *are, *aim, *bre, *bim, *cre, *cim, *x, *w1, *w2; char* ws; const u16 *ldt, *dsk, *b1, *b2; u16* out; const u16* pad; };

DI float bf2f(u32 v) { return __uint_as_float(v << 16); }
DI float bfhi(u32 v) { return __uint_as_float(v & 0xffff0000u); }
DI u32 pk2(float a, float b) { bf2 c; c[0] = (__bf16)a; c[1] = (__bf16)b; return __builtin_bit_cast(u32, c); }
DI float rbf(float a) { return bf2f(pk2(a, a) & 0xffffu); }
DI f4 mfma(v4u a, v4u b, f4 c) { return __builtin_amdgcn_mfma_f32_16x16x32_bf16(__builtin_bit_cast(bf8, a), __builtin_bit_cast(bf8, b), c, 0, 0, 0); }
DI f16v mfma32(v4u a, v4u b, f16v c) { return __builtin_amdgcn_mfma_f32_32x32x16_bf16(__builtin_bit_cast(bf8, a), __builtin_bit_cast(bf8, b), c, 0, 0, 0); }
DI float dot2(u32 a, u32 b, float c) { return __builtin_amdgcn_fdot2_f32_bf16(__builtin_bit_cast(bf2, a), __builtin_bit_cast(bf2, b), c, false); }
template <class T> DI T lds_ld(u32 a) { return *(const LAS T*)(size_t)a; }
template <class T> DI void lds_st(u32 a, T v) { *(LAS T*)(size_t)a = v; }
DI v2u lds_tr(u32 a) { return __builtin_bit_cast(v2u, __builtin_amdgcn_ds_read_tr16_b64_v4i16((LAS s4*)(size_t)a)); }
DI v4u lds_tr2(u32 a, u32 b) { v2u x = lds_tr(a), y = lds_tr(b); return v4u{x.x, x.y, y.x, y.y}; }
DI v4u lds_2x64(u32 a) { v2u x = lds_ld<v2u>(a); asm volatile(""); v2u y = lds_ld<v2u>(a + 8); return v4u{x.x, x.y, y.x, y.y}; }
DI float xor32_add(float v) { auto r = __builtin_amdgcn_permlane32_swap(__float_as_uint(v), __float_as_uint(v), false, false); return __uint_as_float(r[0]) + __uint_as_float(r[1]); }
#define DPP_ADD(v, ctrl) v += __uint_as_float(__builtin_amdgcn_update_dpp(0u, __float_as_uint(v), ctrl, 0xf, 0xf, true))
DI float sum32(float v) {
  DPP_ADD(v, 0xB1); DPP_ADD(v, 0x4E); DPP_ADD(v, 0x141); DPP_ADD(v, 0x140);
  auto r = __builtin_amdgcn_permlane16_swap(__float_as_uint(v), __float_as_uint(v), false, false); return __uint_as_float(r[0]) + __uint_as_float(r[1]);
}
DI void cmul2(f2& r0, f2 a0, f2 w0, f2& r1, f2 a1, f2 w1) {
  asm("v_pk_mul_f32 %0, %2, %3 op_sel:[0,0] op_sel_hi:[1,0]\n\tv_pk_mul_f32 %1, %4, %5 op_sel:[0,0] op_sel_hi:[1,0]\n\t"
      "v_pk_fma_f32 %0, %2, %3, %0 op_sel:[1,1,0] op_sel_hi:[0,1,1] neg_lo:[1,0,0]\n\tv_pk_fma_f32 %1, %4, %5, %1 op_sel:[1,1,0] op_sel_hi:[0,1,1] neg_lo:[1,0,0]"
      : "=&v"(r0), "=&v"(r1) : "v"(a0), "v"(w0), "v"(a1), "v"(w1));
}
DI f2 cexp2(f2 x) {
  float e = __builtin_amdgcn_exp2f(x.x), c = __builtin_amdgcn_cosf(x.y), s = __builtin_amdgcn_sinf(x.y);
  return f2{c, s} * e;
}
DI void ln_stats(float s, float q, float& mb, float& sd) {
  const float mean = s * (1.f / 512.f);
  const float var = fmaxf(q * (1.f / 512.f) - mean * mean, 0.f);
  sd = rbf(__builtin_amdgcn_sqrtf(rbf(rbf(var) + 1e-5f))); mb = rbf(mean);
}
#define VMW(n) asm volatile("s_waitcnt vmcnt(%0)" ::"n"(n) : "memory")
#define LGW0() asm volatile("s_waitcnt lgkmcnt(0)" ::: "memory")
#define BAR() asm volatile("s_barrier" ::: "memory")
#define TIE(x) asm volatile("" : "+v"(x))
#define MARK(x) asm volatile("; PATH_" #x)
#define SB() __builtin_amdgcn_sched_barrier(0)
#define SG(m, n) __builtin_amdgcn_sched_group_barrier(m, n, 0)
#define GLD(op, dst, voff, sbase, imm, fl) asm volatile(op " %0, %1, %2 offset:%3 " fl : "=v"(dst) : "v"(voff), "s"(sbase), "n"(imm) : "memory")
#define GST(op, voff, src, sbase, imm, fl) asm volatile(op " %0, %1, %2 offset:%3 " fl "\n\ts_nop 1" ::"v"(voff), "v"(src), "s"(sbase), "n"(imm) : "memory")
#define M0SET(lbase, limm) asm volatile("s_add_u32 m0, %0, %1\n\ts_nop 0" ::"s"(lbase), "n"(limm) : "memory", "scc")
#define DMA(voff, srd, soff, off) asm volatile("buffer_load_dwordx4 %0, %1, %2 offen offset:%3 lds" ::"v"(voff), "s"(srd), "s"(soff), "n"(off) : "memory")
DI v4u make_srd(const void* p, u32 recs) { const u64 a_ = (u64)p - 4096; return v4u{(u32)a_, (u32)(a_ >> 32), recs, 0x20000u}; }
#define STIE(x) asm volatile("" : "+s"(x))
DI bool tag_miss(v2u f, v2u tag) { return __builtin_expect(__builtin_amdgcn_ballot_w64(f.x != tag.x || f.y != tag.y) != 0, 0); }
DI void poll(u32 voff, const char* ws, v2u tag) {
#pragma nounroll
  for (int it = 0; it < (1 << 16); it++) {
    v2u f;
    asm volatile("global_load_dwordx2 %0, %1, %2 sc1\n\ts_waitcnt vmcnt(0)" : "=v"(f) : "v"(voff), "s"(ws) : "memory");
    if (__builtin_amdgcn_ballot_w64(f.x != tag.x || f.y != tag.y) == 0) break;
    __builtin_amdgcn_s_sleep(6);
  }
}
#define STAMP(i)

struct CvA { u32 ua, a0, a1, a2; };
struct CvS { v4u s01, s2; };
struct ConvRaw { v4u A[8]; u32 e[5], f[3], q[2][2]; };
DI u32 prm(u32 hi, u32 lo, u32 s) { return __builtin_amdgcn_perm(hi, lo, s); }
DI v4u perm4(u32 hi, u32 lo, v4u s) { return v4u{prm(hi, lo, s.x), prm(hi, lo, s.y), prm(hi, lo, s.z), prm(hi, lo, s.w)}; }
DI v4u frag0(const u32 (&e)[5], u32 s) { return v4u{prm(e[4], e[3], s), prm(e[3], e[2], s), prm(e[2], e[1], s), prm(e[1], e[0], s)}; }
DI v4u frag1(const u32 (&f)[3], u32 sa, u32 sb) { return v4u{prm(f[2], f[1], sa), prm(f[2], f[1], sb), prm(f[1], f[0], sa), prm(f[1], f[0], sb)}; }
template <int RR, int P> DI void conv_load(ConvRaw& c, const CvA& a) {
  if (P == 0) {
#pragma unroll
    for (int i = 0; i < 5; i++) if (RR) c.e[i] = lds_ld<u32>(a.a0 + 64 * RR + 4 * i);
    c.A[7] = lds_ld<v4u>(a.ua + 64 * (3 - RR) + 512 * 7); c.A[5] = lds_ld<v4u>(a.ua + 64 * (3 - RR) + 512 * 5);
  }
  if (P == 1) {
    c.A[3] = lds_ld<v4u>(a.ua + 64 * (3 - RR) + 512 * 3); c.A[1] = lds_ld<v4u>(a.ua + 64 * (3 - RR) + 512);
    if (RR) {
#pragma unroll
      for (int i = 0; i < 3; i++) c.f[i] = lds_ld<u32>(a.a1 + 32 * RR + 4 * i);
    }
  }
  if (P == 2) {
#pragma unroll
    for (int i = 6; i >= 0; i -= 2) c.A[i] = lds_ld<v4u>(a.ua + 64 * (3 - RR) + 512 * i);
  }
  if (P == 3) {
#pragma unroll
    for (int m = 2; m < 4; m++) if (RR || m != 2) {
      const u32 p = a.a2 + 16 * RR + 128 * (m - 2);
      c.q[m - 2][0] = lds_ld<u32>(p); c.q[m - 2][1] = lds_ld<u32>(p + 4);
    }
  }
}
template <int RR, int M> DI v4u conv_b(const ConvRaw& c, const CvS& s, v4u x1, v4u x2) {
  return (RR == 0 && M == 1) ? x1 : (RR == 0 && M == 2) ? x2 : M == 1 ? frag1(c.f, s.s01.y, s.s01.z) : perm4(c.q[M < 2 ? 0 : M - 2][1], c.q[M < 2 ? 0 : M - 2][0], s.s2);
}
template <int M, bool Z> DI void conv_mm(f4 (&acc)[4], const ConvRaw& c, v4u B) {
#pragma unroll
  for (int t = 3; t >= 0; t--) if (2 * t + 1 >= M) acc[t] = mfma(B, c.A[2 * t + 1 - M], Z ? f4{0, 0, 0, 0} : acc[t]);
}
#define CV_SG(n) do { _Pragma("unroll") for (int i_ = 0; i_ < (n); i_++) { SG(0x8, 1); SG(0x100, 2); SG(0x2, 2); } SB(); } while (0)
#define CV_ROUND(RR, c, n, X0, X1, X2, X3) do { \
    v4u b1_ = conv_b<RR, 1>(c, cs_, x1, x2); if ((RR) < 3) conv_load<((RR) + 1) & 3, 0>(n, ca_); conv_mm<0, (RR) == 0>(ca, c, b0); CV_SG(4); TIE(b1_); X0; SB(); \
    v4u b2_ = conv_b<RR, 2>(c, cs_, x1, x2); if ((RR) < 3) conv_load<((RR) + 1) & 3, 1>(n, ca_); conv_mm<1, false>(ca, c, b1_); CV_SG(4); TIE(b2_); X1; SB(); \
    v4u b3_ = conv_b<RR, 3>(c, cs_, x1, x2); if ((RR) < 3) conv_load<((RR) + 1) & 3, 2>(n, ca_); conv_mm<2, false>(ca, c, b2_); CV_SG(3); TIE(b3_); X2; SB(); \
    if ((RR) < 3) { conv_load<((RR) + 1) & 3, 3>(n, ca_); b0 = frag0(n.e, cs_.s01.x); } \
    conv_mm<3, false>(ca, c, b3_); if ((RR) == 0) conv_mm<4, false>(ca, c, x4); CV_SG((RR) == 0 ? 5 : 3); TIE(b0); X3; SB(); } while (0)

typedef u32 v16u __attribute__((ext_vector_type(16)));
#define KP(v, i) ((const char*)(((u64)v[2 * (i) + 1] << 32) | v[2 * (i)]))
__global__ __launch_bounds__(512, 2) void fused_kernel(Args) {
  STAMP(0);
  u64 tst; asm volatile("s_memrealtime %0\n\ts_waitcnt lgkmcnt(0)" : "=s"(tst)::"memory");
  v16u k0_, k1_;
  asm volatile("s_load_dwordx16 %0, %2, 0x0\n\ts_load_dwordx16 %1, %2, 0x40" : "=&s"(k0_), "=&s"(k1_) : "s"(__builtin_amdgcn_kernarg_segment_ptr()) : "memory");
  const int tid = threadIdx.x, lane = tid & 63, w = __builtin_amdgcn_readfirstlane(tid >> 6), l15 = lane & 15, g = lane >> 4, l31 = lane & 31, hh = lane >> 5;
  const int bid = blockIdx.x, xcd = bid & 7, idx = bid >> 3;
  const int oct = xcd * 32 + idx;
  const int ch = w >> 2, dch = 2 * oct + ch;
  const bool tw = ((w >> 1) ^ ch) & 1;
  const int lr = 4 * ch + 2 * (w & 1) + hh;
  const int md = (w & 1) * 64 + lane;
  u32 xo = (u32)(oct * 8 + lr) * 1024u + (u32)l31 * 16u, po = (u32)(dch * 128 + md) * 2u;
  TIE(xo); TIE(po);
  if (w < 2) { const u32 o = (u32)tid * 16u, d = LWR + w * 1024;
    asm volatile("s_mov_b32 m0, %2\n\ts_nop 0\n\tglobal_load_lds_dwordx4 %0, %1" ::"v"(o), "s"(g_tabs.wr), "s"(d) : "memory");
  } else if (w < 4) {
    u64 pc; u32 len; asm volatile("s_getpc_b64 %0\n.Lfk_beg:\n\ts_mov_b32 %1, .Lfk_end-.Lfk_beg" : "=s"(pc), "=s"(len));
    const u32 ln = (idx & 15) * 2 + w - 2, o = min((ln * 6 + lane) * 128u, len), o2 = min(ln * 2 + (lane & 1), 47u) * 128u;
    if (lane < 6) asm volatile("s_mov_b32 m0, %4\n\ts_nop 0\n\tglobal_load_lds_dword %0, %1\n\tglobal_load_lds_dword %2, %3" ::"v"(o), "s"(pc), "v"(o2), "s"(g_tabs.lt), "n"(SP + KD) : "memory");
  }
  const bool gw = __builtin_expect(idx < 28, 1);
  const int j4 = (idx * 37) >> 8, i7 = idx - 7 * j4;
  const int rt = (xcd >> 1) * 4 + j4, ft = (xcd & 1) * 7 + i7, rot = i7 ^ (2 * j4);
  const int mt = xcd * 4 + (idx >> 3), nt = idx & 7;
  const u32 w4k = (u32)w * 4096u;
  const int cg = (xcd & 1) * 14 + min((xcd >> 1) * 4 + (idx - 28), 13);
  u32 vx0, vx1, vw0, vw1, xs0, xs1, ws0, ws1;
  if (gw) {
    vx0 = (u32)(rt * 128 + w * 16 + (lane >> 3)) * 1024u + 16u * ((lane & 7) ^ (lane >> 4)) + 4096u; vx1 = (vx0 ^ 64u) + 8192u - 1024u;
    vw0 = (u32)(8 * w + g) * 3584u + 256u * ft + 16u * (l15 ^ (4 * g)) + 4096u - 2048u; vw1 = vw0 + 4 * 3584 - 1024;
  } else { vx0 = (u32)(64 * w + (lane >> 3)) * 3584u + 128u * cg + 16u * (lane & 7) + 4096u; vx1 = vx0 + 8 * 3584 - 1024; vw0 = vx0 + 16 * 3584 - 2048; vw1 = vx0 + 24 * 3584 - 3072; }
  xs0 = gw ? rot * 128 : 0; xs1 = gw ? (rot ^ 1) * 128 : 32 * 3584; ws0 = gw ? rot * (64 * 3584) : 0; ws1 = gw ? (rot ^ 1) * (64 * 3584) : 32 * 3584;
  const int fq = bid * 7 + w;
  u32 wo = (u32)((fq >> 4) * 16 + (lane >> 2)) * 1024u + (fq & 15) * 64u + (lane & 3) * 16u + (w < 7 ? 4096u - 2048u : 0x40000000u);
  TIE(vx0); TIE(vx1); TIE(vw0); TIE(vw1); TIE(wo);
  asm volatile("s_waitcnt lgkmcnt(0)" : "+s"(k0_), "+s"(k1_)::"memory");
  struct { const char *are, *aim, *bre, *bim, *cre, *cim, *x, *w1, *w2, *ws, *ldt, *dsk, *b1, *b2, *out; } a =
    {KP(k0_, 0), KP(k0_, 1), KP(k0_, 2), KP(k0_, 3), KP(k0_, 4), KP(k0_, 5), KP(k0_, 6), KP(k0_, 7), KP(k1_, 0), KP(k1_, 1), KP(k1_, 2), KP(k1_, 3), KP(k1_, 4), KP(k1_, 5), KP(k1_, 6)};
  const char* ws = a.ws;
  u32 pa, pb, pc_, pd, pe, pg; v4u xr0, xr1;
  GLD("global_load_short_d16_hi", pa, po, a.are, 0, ""); GLD("global_load_short_d16_hi", pb, po, a.aim, 0, "");
  u64 ep; u32 sl, sk_;
  { const u32 b4 = oct * 4;
    asm volatile("s_load_dwordx2 %0, %3, 0x0\n\ts_load_dword %1, %4, %6\n\ts_load_dword %2, %5, %6" : "=&s"(ep), "=&s"(sl), "=&s"(sk_) : "s"(ws), "s"(a.ldt), "s"(a.dsk), "s"(b4) : "memory"); }
  if (tw) { MARK(KT); GLD("global_load_dwordx4", xr0, xo, a.x, 0, ""); GLD("global_load_dwordx4", xr1, xo, a.x, 512, ""); }
  else { MARK(KS); GLD("global_load_short_d16_hi", pc_, po, a.bre, 0, ""); GLD("global_load_short_d16_hi", pd, po, a.bim, 0, ""); GLD("global_load_short_d16_hi", pe, po, a.cre, 0, ""); GLD("global_load_short_d16_hi", pg, po, a.cim, 0, ""); }
  SB();
  const v4u rw = make_srd(a.w1, 512 * 3584 + 4096), rx = make_srd(gw ? a.x : a.w1, gw ? 2048 * 1024 + 4096 : 512 * 3584 + 4096), r2 = make_srd(a.w2, 1792 * 1024 + 4096), rs = make_srd(ws, WS_END + 4096);
#define XS(c) (u32)(((c) ^ rot) * 128)
#define WS_(c) (u32)(((c) ^ rot) * (64 * 3584))
#define DMA_X(c) do { DMA(vx0, rx, XS(c), 0); DMA(vx1, rx, XS(c), 1024); } while (0)
#define DMA_W(c) do { DMA(vw0, rw, WS_(c), 2048); DMA(vw1, rw, WS_(c), 3072); } while (0)
  if (tid == 0) { const u32 o_ = WS_ST + bid * 128u; const u32 v_ = (u32)tst; GST("global_store_dword", o_, v_, ws, 0, "sc1"); }
  BAR();
  M0SET(w4k, 0); DMA(vx0, rx, xs0, 0); DMA(vx1, rx, xs0, 1024); DMA(vw0, rw, ws0, 2048); DMA(vw1, rw, ws0, 3072);
  STAMP(16);
  const int mw = w & 3, nh = w >> 2, q4 = l15 >> 2;
  u32 xa[2][4], wa[2][2], ksa, kta;
  { const int row = 32 * mw + l31;
#pragma unroll
    for (int r = 0; r < 4; r++) { xa[0][r] = (row >> 4) * 4096 + (row & 15) * 128 + 16 * ((2 * r + hh) ^ ((row >> 1) & 7)); TIE(xa[0][r]); }
#pragma unroll
    for (int nl = 0; nl < 2; nl++) { wa[0][nl] = hh * 4096 + 2048 + q4 * 256 + 64 * ((2 * nh + nl) ^ q4) + 32 * (g & 1) + 8 * (lane & 3); TIE(wa[0][nl]); }
    const u32 o = (2 * g + (q4 >> 1)) * 64 + 8 * (lane & 1), o1 = o + 16 * (((lane >> 1) & 3) ^ g);
    ksa = LSS + ch * 16384 + (w & 1) * 8192 + o1;
    kta = (w & 2) ? LT1 + o + 16 * ((2 * ch + (q4 & 1)) ^ g) : LTS + ch * 8192 + o1;
    TIE(ksa); TIE(kta); }
  u32 wra = 65536 + 2048 + w4k + (4 * hh + q4) * 64 + 32 * (g & 1) + 8 * (lane & 3); TIE(wra);
  u32 wso = WS_W + (u32)((((fq >> 1) & 7) * 112 + (fq >> 4)) * 2 + (fq & 1)) * 1024u + lane * 16u + (w < 7 ? 4096u : 0x40000000u), uso = WS_U + (u32)oct * US + (u32)tid * 16u;
  TIE(wso); TIE(uso);
  if (w < 2) lds_st<v4u>(lane < 62 ? LU + w * LUS + lane * 16 : SP + w * TBS + lane * 16 - 992, v4u{0, 0, 0, 0});
  else if (tid < 144) lds_st<v4u>(SP + (lane >> 3) * TBS + KQ + (lane & 7) * 16, v4u{0, 0, 0, 0});
  asm volatile("s_waitcnt lgkmcnt(0)" : "+s"(ep), "+s"(sl), "+s"(sk_)::"memory");
  v2u tag;
  { u64 z = (ep + 0x9E3779B97F4A7C15ull) * 0xBF58476D1CE4E5B9ull; z ^= z >> 29; STIE(z); tag.x = (u32)z; tag.y = (u32)(z >> 32); }
  float dt;
  { const float xx = __uint_as_float(ch ? sl & 0xffff0000u : sl << 16), t = xx * 1.4426950216293335f, r = __builtin_fmaf(xx, 1.9259629911266175e-8f, __builtin_fmaf(xx, 1.4426950216293335f, -t));
    const float e = __builtin_amdgcn_exp2f(t); dt = rbf(__builtin_fmaf(e * r, 0.6931471805599453f, e)); TIE(dt); }
  float k00 = __uint_as_float((tid & 255) == 0 ? (ch ? sk_ & 0xffff0000u : sk_ << 16) : 0u); TIE(k00);
  SB();
  M0SET(w4k, 65536); DMA(wo, r2, 0, 2048);
  M0SET(w4k, 32768); DMA(vw0, rw, ws1, 2048); DMA(vw1, rw, ws1, 3072);
  VMW(7); TIE(pa); TIE(pb);
  STAMP(1);
  v4u un; v2u sv;
#define SYNC_A() do { LGW0(); BAR(); const u32 ra = LXN + (tid & 1) * 16384 + (((tid >> 1) * 4) ^ ((tid & 1) * 64)); \
    un = v4u{lds_ld<u32>(ra), lds_ld<u32>(ra + 4096), lds_ld<u32>(ra + 8192), lds_ld<u32>(ra + 12288)}; sv = lds_ld<v2u>(LXN + 1024 + (lane & 7) * 8); SB(); } while (0)
#define ST_A() do { SB(); DMA(vx0, rx, xs1, 0); DMA(vx1, rx, xs1, 1024); GST("global_store_dwordx4", uso, un, ws, 0, "sc1"); \
    const u32 o_ = tid < 8 ? WS_U + 4096 + 8192 + (u32)oct * US + tid * 8u : 0x40000000u; asm volatile("buffer_store_dwordx2 %0, %1, %2, 0 offen sc1" ::"v"(sv), "v"(o_), "s"(rs) : "memory"); SB(); } while (0)

  const u32 tb = SP + (u32)ch * TBS;
  {
    const u32 dk = md * 64 + 16 * ((lane >> 1) & 3);
#define AF_MAKE() float arl = dt * bfhi(pa) * 1.4426950408889634f; arl = fminf(fmaxf(arl, -1e30f), 1e30f); float f = dt * bfhi(pb) * 0.15915494309189535f; f -= rintf(f); const f2 af = {arl, f}
    if (!tw) {
      MARK(KS);
      AF_MAKE();
      TIE(pc_); TIE(pd); TIE(pe); TIE(pg);
      const f2 z8 = cexp2(af * 8.f), bb = f2{bfhi(pc_), bfhi(pd)} * dt, cc = {bfhi(pe), bfhi(pg)};
      f2 z16, g0, g1, t_;
      cmul2(z16, z8, z8, g0, cc, bb); cmul2(g1, g0, z8, t_, g0, z8);
      const u32 d = LSS + ch * 16384 + dk;
#pragma unroll
      for (int c = 0; c < 4; c++) {
        v4u re, im;
#pragma unroll
        for (int i = 0; i < 4; i++) { f2 n0 = g0, n1 = g1; if (c < 3 || i < 3) cmul2(n0, g0, z16, n1, g1, z16); re[i] = pk2(g0.x, g1.x); im[i] = pk2(g0.y, g1.y); g0 = n0; g1 = n1; }
        lds_st<v4u>((d ^ (16 * (c & 1))) + (c >> 1) * 8192, re); lds_st<v4u>((d ^ (32 + 16 * (c & 1))) + (c >> 1) * 8192, im);
        if (c == 1) SYNC_A();
        if (c == 2) ST_A();
      }
    } else {
      MARK(KT);
      __builtin_amdgcn_s_setprio(2);
      TIE(xr0); TIE(xr1);
      float ss = 0.f, sq = 0.f, ss2 = 0.f, sq2 = 0.f;
#pragma unroll
      for (int e = 0; e < 4; e++) { const u32 v = xr0[e], v1 = xr1[e]; ss = dot2(v, 0x3f803f80u, ss); sq = dot2(v, v, sq); ss2 = dot2(v1, 0x3f803f80u, ss2); sq2 = dot2(v1, v1, sq2); }
      ss = sum32(ss + ss2); sq = sum32(sq + sq2);
      float mb, sd; ln_stats(ss, sq, mb, sd);
      lds_st<v2u>(LXN + 1024 + lr * 8, v2u{pk2(-mb, -mb), pk2(-mb, sd)});
      const float rstd = __builtin_amdgcn_rcpf(sd), nmr = -mb * rstd;
      v4u o0, o1;
#pragma unroll
      for (int e = 0; e < 4; e++) { const u32 v = xr0[e], v1 = xr1[e]; const f2 t = f2{bf2f(v), bfhi(v)} * rstd + nmr, t1 = f2{bf2f(v1), bfhi(v1)} * rstd + nmr; o0[e] = pk2(t.x, t.y); o1[e] = pk2(t1.x, t1.y); }
      lds_st<v4u>(LXN + (lr << 12) + ((l31 ^ (4 * ch)) << 4), o0); lds_st<v4u>(LXN + 512 + (lr << 12) + ((l31 ^ (4 * ch)) << 4), o1);
      SYNC_A();
      AF_MAKE();
      const f2 z1 = cexp2(af), y1 = cexp2(af * 256.f);
      f2 z2, y2, z3, z4, y3, y4, z5, z6, z7, a2, a4, a6, b4, c4, y5, y6, y7, y8;
      cmul2(z2, z1, z1, y2, y1, y1);
      ST_A();
      cmul2(z3, z2, z1, z4, z2, z2); cmul2(y3, y2, y1, y4, y2, y2); cmul2(z5, z4, z1, z6, z4, z2); cmul2(z7, z4, z3, a2, y1, z2);
      const u32 d = LTS + ch * 8192 + dk, d1 = (LT1 + dk) ^ (32 * ch);
      lds_st<v4u>(d, v4u{pk2(1.f, z1.x), pk2(z2.x, z3.x), pk2(z4.x, z5.x), pk2(z6.x, z7.x)});
      lds_st<v4u>(d ^ 32, v4u{pk2(0.f, -z1.y), pk2(-z2.y, -z3.y), pk2(-z4.y, -z5.y), pk2(-z6.y, -z7.y)});
      cmul2(a4, y1, z4, a6, y1, z6); cmul2(b4, y2, z4, c4, y3, z4); cmul2(y5, y4, y1, y6, y4, y2); cmul2(y7, y4, y3, y8, y4, y4);
      lds_st<v4u>(d ^ 16, v4u{pk2(y1.x, a2.x), pk2(a4.x, a6.x), pk2(y2.x, b4.x), pk2(y3.x, c4.x)});
      lds_st<v4u>(d ^ 48, v4u{pk2(-y1.y, -a2.y), pk2(-a4.y, -a6.y), pk2(-y2.y, -b4.y), pk2(-y3.y, -c4.y)});
      lds_st<v4u>(d1, v4u{pk2(y4.x, y5.x), pk2(y6.x, y7.x), pk2(y8.x, y8.x), 0u});
      lds_st<v4u>(d1 ^ 16, v4u{pk2(-y4.y, -y5.y), pk2(-y6.y, -y7.y), pk2(-y8.y, -y8.y), 0u});
      __builtin_amdgcn_s_setprio(0);
    }
  }
  STAMP(2);
#define LT_DMA() do { const u32 d_ = LLT + min(w, 6) * 1024; asm volatile("s_mov_b32 m0, %2\n\ts_nop 0\n\tglobal_load_lds_dwordx4 %0, %1" ::"v"(lto), "s"(g_tabs.lt), "s"(d_) : "memory"); } while (0)
  STAMP(40);
  VMW(6);
  STAMP(41);
  { const v4u wt = lds_tr2(wra, wra + 512); asm volatile("buffer_store_dwordx4 %0, %1, %2, 0 offen sc1\n\ts_nop 1" ::"v"(wt), "v"(wso), "s"(rs) : "memory"); }
  LGW0(); BAR();
  STAMP(18);
  u32 ta2, ta2h, afo, so, co, uo_, lto, ufs; v2u tagv = tag;
#define FLAG_ST(o) asm volatile("buffer_store_dwordx2 %0, %1, %2, 0 offen sc1" ::"v"(tagv), "v"(o), "s"(rs) : "memory")
    f16v acc[2];
    v4u xf[2][2], wf[2][2][2];
#define G1_RD(st, r_) do { constexpr int sl_ = ((st) >> 1) % 3, p_ = sl_ >> 1; constexpr u32 sb_ = (sl_ & 1) * 32768, wo_ = sb_ + ((st) & 1) * 16384 + (r_) * 8192; \
      xf[(st) & 1][r_] = lds_ld<v4u>(xa[p_][2 * ((st) & 1) + (r_)] + sb_); \
      wf[(st) & 1][0][r_] = lds_tr2(wa[p_][0] + wo_, wa[p_][0] + wo_ + 1024); wf[(st) & 1][1][r_] = lds_tr2(wa[p_][1] + wo_, wa[p_][1] + wo_ + 1024); } while (0)
#define G1_MM(st, n_, r_) acc[n_] = mfma32(wf[(st) & 1][n_][r_], xf[(st) & 1][r_], (st) + (r_) == 0 ? f16v{0} : acc[n_])
#define G1_STEP(st, X2, X3) do { \
      G1_MM(st, 0, 0); if ((st) < 15) G1_RD((st) + 1, 0); SG(0x8, 1); SG(0x100, 5); SB(); \
      G1_MM(st, 1, 0); if ((st) < 15) G1_RD((st) + 1, 1); SG(0x8, 1); SG(0x100, 5); SB(); \
      G1_MM(st, 0, 1); SB(); X2; SB(); \
      G1_MM(st, 1, 1); SB(); X3; SB(); } while (0)
#define G1_SYNC(NV) do { VMW(NV); LGW0(); BAR(); SB(); } while (0)
  v4u kA[2], kB[2]; f4 kk[2]; v2u wa_;
#define K_RD(k) do { kA[(k) & 1] = lds_tr2(kta + (k) * 1024, kta + (k) * 1024 + 512); kB[(k) & 1] = lds_tr2(ksa + (k) * 1024, ksa + (k) * 1024 + 512); } while (0)
#define K_MM(k) do { kk[(k) & 1] = mfma(kA[(k) & 1], kB[(k) & 1], (k) == 0 ? f4{k00, 0, 0, 0} : (k) == 1 ? f4{0, 0, 0, 0} : kk[(k) & 1]); TIE(kk[(k) & 1]); } while (0)
#define K_PUT() do { const f4 k_ = kk[0] + kk[1]; const u32 p0 = pk2(k_[0], k_[1]), p1 = pk2(k_[2], k_[3]);  { \
      lds_st<u16>(tb + (wa_.x & 0xffffu), (u16)p0); lds_st<u16>(tb + (wa_.x >> 16), (u16)(p0 >> 16)); \
      lds_st<u16>(tb + (wa_.y & 0xffffu), (u16)p1); lds_st<u16>(tb + (wa_.y >> 16), (u16)(p1 >> 16)); } } while (0)
#define SET_A() do { _Pragma("unroll") for (int r = 0; r < 4; r++) { xa[1][r] = xa[0][r] + 65536; TIE(xa[1][r]); } wa[1][0] = wa[0][0] + 65536; wa[1][1] = wa[0][1] + 65536; TIE(wa[1][0]); TIE(wa[1][1]); } while (0)
#define SET_B() do { ufs = tid == 448 ? WS_UF + 4096 + (u32)oct * 128u : 0x40000000u; TIE(ufs); TIE(tagv); } while (0)
#define SET_C() do { ta2 = w4k + lane * 16 + (w & 1) * 1024; ta2h = ta2 + 65536; TIE(ta2); TIE(ta2h); uo_ = WS_U + (u32)(tid >> 1) * US + (u32)oct * 32u + (tid & 1) * 16u; TIE(uo_); } while (0)
#define SET_D() do { afo = WS_UF + (u32)(32 * w + lane) * 128u; const u32 t_ = WS_CF + (u32)(2 * ft + nh) * 128u; afo = hh ? t_ : afo; TIE(afo); } while (0)
#define SET_F() do { lto = (u32)min(tid, 383) * 16u; TIE(lto); wa_ = lds_ld<v2u>(LWR + (tid & 255) * 8); } while (0)
#define SET_E() do { so = WS_U + 8192 + (u32)(rt * 16 + mw * 4 + (l31 >> 3)) * US + (lane & 7) * 8u; co = WS_C + (u32)(ft * 128 + nh * 64 + l31) * 8u; TIE(so); TIE(co); } while (0)
  const u32 seq = (u32)(0x65432106543210ull >> (4 * (nt == 7 ? 3 : nt)));
#define FR(f) ((seq >> (4 * (f))) & 7u)
  const u32 ho = (u32)lane * 16u, w2s = WS_W + 4096 + (u32)(nt * 112 + 14 * w) * 2048u;
#define DMA_2I(f, i) asm volatile("buffer_load_dwordx4 %0, %1, %2 offen offset:%3 sc1 lds" ::"v"(ho), "s"(rs), "s"(w2s + (FR(f) << 12)), "n"((i) * 1024) : "memory")
#define SL2(f) ((0x1023 >> (4 * ((f) & 3))) & 3)
#define M0_2(f) M0SET(w4k, SL2(f) * 32768)
#define DMA_2(f) do { M0_2(f); DMA_2I(f, 0); DMA_2I(f, 1); DMA_2I(f, 2); DMA_2I(f, 3); } while (0)
  v4u wf2[3][2][2];
#define W2FRAG(f, s2, nl) lds_ld<v4u>(((SL2(f) & 2 ? ta2h : ta2) ^ ((nl) * 1024)) + (SL2(f) & 1) * 32768 + (s2) * 2048)
#define W2_READ(f) do { _Pragma("unroll") for (int s_ = 0; s_ < 2; s_++) _Pragma("unroll") for (int n_ = 0; n_ < 2; n_++) wf2[f][s_][n_] = W2FRAG(f, s_, n_); } while (0)
#define W2_TIE(f) do { LGW0(); _Pragma("unroll") for (int s_ = 0; s_ < 2; s_++) _Pragma("unroll") for (int n_ = 0; n_ < 2; n_++) TIE(wf2[f][s_][n_]); } while (0)
  const int th = w & 1, rh = (w >> 1) & 1;
  CvS cs_; v4u sx, t7, t9; u32 ad;
#define LT_READ() do { cs_.s01 = lds_ld<v4u>(LLT + ho); cs_.s2 = lds_ld<v4u>(LLT + 1024 + ho); sx = lds_ld<v4u>(LLT + 2048 + ho); \
    t7 = lds_ld<v4u>(LLT + 3072 + ho); t9 = lds_ld<v4u>(LLT + 4096 + ho); ad = lds_ld<u32>(LLT + 5120 + ho); } while (0)
  CvA ca_; ConvRaw c0, c1; v4u x1, x2, x4, b0, y1_, y2_; u32 pe_[5], pf_[3], pq_[2];
#define CV_ADDR() do { ca_.ua = LU + (u32)ch * LUS + 448 - 192 - 256 * rh + 512 * th + 32 * l15 + 16 * g; \
    ca_.a0 = tb + (cs_.s01.w & 0xffffu) + 256 * rh; ca_.a1 = tb + (cs_.s01.w >> 16) + 128 * rh; ca_.a2 = tb + (ad & 0xffffu) + 64 * rh; } while (0)
#define CV_PRE() do { const u32 p1_ = rh ? ca_.a2 : ca_.a0 + 512, p2_ = ca_.a1 + (rh ? 0 : 256), p3_ = tb + (ad >> 16), p4_ = tb + KQ + 128 - 12 + 64 * (th - rh) + 4 * l15 - 16 * g; \
    _Pragma("unroll") for (int i = 0; i < 5; i++) { pe_[i] = lds_ld<u32>(p1_ + 4 * i); c0.e[i] = lds_ld<u32>(ca_.a0 + 4 * i); } \
    _Pragma("unroll") for (int i = 0; i < 3; i++) pf_[i] = lds_ld<u32>(p2_ + 4 * i); pq_[0] = lds_ld<u32>(p3_); pq_[1] = lds_ld<u32>(p3_ + 4); \
    _Pragma("unroll") for (int i = 0; i < 4; i++) { y1_[i] = lds_ld<u32>(p4_ + 12 - 4 * i); y2_[i] = lds_ld<u32>(p4_ + 128 + 12 - 4 * i); } \
    conv_load<0, 3>(c0, ca_); \
    b0 = frag0(c0.e, cs_.s01.x); \
    if (rh) { x1 = frag1(pf_, cs_.s01.y, cs_.s01.z); x2 = perm4(pe_[1], pe_[0], cs_.s2); x4 = v4u{0, 0, 0, 0}; } \
    else { x1 = frag0(pe_, cs_.s01.x); x2 = frag1(pf_, cs_.s01.y, cs_.s01.z); x4 = perm4(pq_[1], pq_[0], sx); } TIE(b0); TIE(x1); TIE(x2); TIE(x4); TIE(y1_); TIE(y2_); TIE(t7); TIE(t9); } while (0)
#define U_PUT() do { lds_st<v2u>(LU + 992 + tid * 8, v2u{prm(uu.y, uu.x, 0x05040100u), prm(uu.w, uu.z, 0x05040100u)}); \
    lds_st<v2u>(LU + LUS + 992 + tid * 8, v2u{prm(uu.y, uu.x, 0x07060302u), prm(uu.w, uu.z, 0x07060302u)}); } while (0)
  v4u uu;

  if (gw) {
    MARK(GW);
    v2u fl;
    v2u st2, cq0, cq1; u32 fx, fv;
    G1_RD(0, 0); G1_RD(0, 1); SB();
    M0SET(w4k, 65536); DMA_X(2);
    G1_STEP(0, { DMA(vw0, rw, WS_(2), 2048); K_RD(0); }, DMA(vw1, rw, WS_(2), 3072));
    G1_SYNC(7); STAMP(20);
    G1_STEP(1, { M0SET(w4k, 0); DMA(vx0, rx, XS(3), 0); K_MM(0); K_RD(1); }, { DMA(vx1, rx, XS(3), 1024); SET_B(); });
    G1_STEP(2, { DMA(vw0, rw, WS_(3), 2048); K_MM(1); K_RD(2); }, { DMA(vw1, rw, WS_(3), 3072); SET_A(); });
    G1_SYNC(4); STAMP(21);
    G1_STEP(3, { FLAG_ST(ufs); M0SET(w4k, 32768); DMA(vx0, rx, XS(4), 0); K_MM(2); K_RD(3); }, DMA(vx1, rx, XS(4), 1024));
    G1_STEP(4, { DMA(vw0, rw, WS_(4), 2048); K_MM(3); K_RD(4); }, DMA(vw1, rw, WS_(4), 3072));
    G1_SYNC(5); STAMP(22);
    G1_STEP(5, { M0SET(w4k, 65536); DMA(vx0, rx, XS(5), 0); K_MM(4); K_RD(5); }, DMA(vx1, rx, XS(5), 1024));
    G1_STEP(6, { DMA(vw0, rw, WS_(5), 2048); K_MM(5); K_RD(6); }, DMA(vw1, rw, WS_(5), 3072));
    G1_SYNC(4); STAMP(23);
    G1_STEP(7, { M0SET(w4k, 0); DMA(vx0, rx, XS(6), 0); K_MM(6); K_RD(7); }, { DMA(vx1, rx, XS(6), 1024); SET_F(); });
    G1_STEP(8, { DMA(vw0, rw, WS_(6), 2048); K_MM(7); }, { DMA(vw1, rw, WS_(6), 3072); SET_D(); });
    G1_SYNC(4); STAMP(24);
    G1_STEP(9, { M0SET(w4k, 32768); DMA(vx0, rx, XS(7), 0); SET_E(); }, { DMA(vx1, rx, XS(7), 1024); SET_C(); });
    G1_STEP(10, DMA_W(7), { LT_DMA(); GLD("global_load_dwordx2", fl, afo, ws, 0, "sc1"); });
    G1_SYNC(6); STAMP(25);
    G1_STEP(11, K_PUT(), {});
    G1_STEP(12, {}, { VMW(0); TIE(fl); if (tag_miss(fl, tag)) { MARK(SLOW); poll(afo, ws, tag); } });
    G1_SYNC(63); STAMP(26);
    GLD("global_load_dwordx2", st2, so, ws, 0, "sc1"); GLD("global_load_dwordx2", cq0, co, ws, 0, "sc1"); GLD("global_load_dwordx2", cq1, co, ws, 256, "sc1");
    G1_STEP(13, { M0_2(0); DMA_2I(0, 0); fx = lds_ld<u32>(LLT + 5120 + lane * 16 + 4); }, DMA_2I(0, 1));
    G1_STEP(14, { DMA_2I(0, 2); fv = lds_ld<u16>(tb + (fx & 0xffffu)); }, { DMA_2I(0, 3); GLD("global_load_dwordx4", uu, uo_, ws, 0, "sc1"); });
    G1_STEP(15, { M0_2(1); DMA_2I(1, 0); DMA_2I(1, 1); lds_st<u16>(tb + (fx >> 16), (u16)fv); }, { DMA_2I(1, 2); DMA_2I(1, 3); });
    VMW(9); TIE(st2); TIE(cq0); TIE(cq1);
    STAMP(4);
    __builtin_amdgcn_s_setprio(2);
    const float sd = bfhi(st2.y);
    { const u32 km = hh ? 0u : ~0u; const v4u sb = {st2.x & km, st2.y & km, 0u, 0u};
      acc[0] = mfma32(v4u{cq0.x, cq0.y, 0u, 0u}, sb, acc[0]); acc[1] = mfma32(v4u{cq1.x, cq1.y, 0u, 0u}, sb, acc[1]); }
    const float rstd = __builtin_amdgcn_rcpf(sd), k1 = -2.3022082f * rstd, k2 = -0.10294324f * rstd * rstd * rstd;
    const int M32 = rt * 4 + mw;
    const char* hb = ws + WS_H + (size_t)(M32 * 112 + ft * 8 + nh * 4) * 1024;
#pragma unroll
    for (int nl = 0; nl < 2; nl++) {
#pragma unroll
      for (int s = 0; s < 2; s++) {
        v4u hp;
#pragma unroll
        for (int e = 0; e < 4; e++) {
          const f2 v = {acc[nl][8 * s + 2 * e], acc[nl][8 * s + 2 * e + 1]};
          const f2 z = v * (v * v * k2 + k1);
          const f2 d = f2{__builtin_amdgcn_exp2f(z.x), __builtin_amdgcn_exp2f(z.y)} * sd + sd;
          const f2 o = v * f2{__builtin_amdgcn_rcpf(d.x), __builtin_amdgcn_rcpf(d.y)};
          hp[e] = pk2(o.x, o.y);
        }
        GST("global_store_dwordx4", ho, hp, hb, nl * 2048 + s * 1024, "sc1");
        if (nl == 0 && s == 0) { M0_2(2); DMA_2I(2, 0); DMA_2I(2, 1); }
        if (nl == 0 && s == 1) { DMA_2I(2, 2); DMA_2I(2, 3); }
      }
    }
    __builtin_amdgcn_s_setprio(0);
    STAMP(5);
    VMW(12); TIE(uu); U_PUT();
    u32 hfs = lane == 0 ? WS_HF + 4096 + (u32)(M32 * 32 + ft * 2 + nh) * 128u : 0x40000000u; TIE(hfs);
    LT_READ(); W2_READ(0); CV_ADDR(); CV_PRE();
    VMW(0);
    FLAG_ST(hfs);
    STAMP(7);
    LGW0(); BAR();
  } else {
    MARK(LN);
    SET_B(); SET_C();
    u32 pb1; { const u32 o3 = (u32)(cg * 64 + lane) * 2u; GLD("global_load_ushort", pb1, o3, a.b1, 0, ""); }
    VMW(4);
    { v4u se; f4 ca = {0, 0, 0, 0};
#pragma unroll
      for (int i = 0; i < 4; i++) se[i] = (l15 >> 1) == i ? (0x3f80u << (16 * (l15 & 1))) : 0u;
#pragma unroll
      for (int i = 0; i < 8; i++) ca = mfma(se, lds_ld<v4u>(w4k + ho + (i >> 2) * 32768 + (i & 3) * 1024), ca);
      if (lane < 32) lds_st<f4>(w4k + (l15 >> 3) * 256 + (lane & 7) * 32 + g * 16, ca); }
    LGW0(); BAR();
    if (w == 0) {
      float c = 0.f;
#pragma unroll
      for (int i = 0; i < 16; i++) c += lds_ld<float>((i >> 1) * 4096 + (i & 1) * 256 + lane * 4);
      const float c0 = rbf(c), c1 = rbf(c - c0), c2 = c - c0 - c1;
      VMW(0); TIE(pb1);
      const u32 o = WS_C + (u32)(cg * 64 + lane) * 8u; const v2u v = {pk2(c0, c1), pk2(c2, bf2f(pb1))}; GST("global_store_dwordx2", o, v, ws, 0, "sc1");
    } else VMW(0);
    BAR();
    FLAG_ST(ufs);
    if (w == 0) { VMW(0); if (lane == 0) { const u32 o_ = WS_CF + (u32)cg * 128u; GST("global_store_dwordx2", o_, tag, ws, 0, "sc1"); } }
    STAMP(4);
    SET_F();
#pragma unroll
    for (int k = 0; k < 8; k++) { K_RD(k); K_MM(k); }
    BAR();
    K_PUT();
    LT_DMA();
    __builtin_amdgcn_s_sleep(40);
    poll(WS_UF + (u32)(32 * w + l31) * 128u, ws, tag);
    LGW0(); BAR();
    GLD("global_load_dwordx4", uu, uo_, ws, 0, "sc1");
    DMA_2(0); DMA_2(1); DMA_2(2);
    { const u32 fx = lds_ld<u32>(LLT + 5120 + lane * 16 + 4); lds_st<u16>(tb + (fx >> 16), lds_ld<u16>(tb + (fx & 0xffffu))); }
    LT_READ(); CV_ADDR();
    VMW(0); TIE(uu); U_PUT(); W2_READ(0);
    LGW0(); BAR();
    CV_PRE();
    STAMP(5);
  }
  v4u hf[14][2];
  const u32 hv0 = (u32)((2 * mt + (w >> 2)) * 112 + 14 * w) * 1024u + ho + 4096u + WS_H, hv1 = (u32)((2 * mt + 1 - (w >> 2)) * 112 + 14 * w) * 1024u + ho + 4096u + WS_H;
#define H_LD(s, ml) asm volatile("buffer_load_dwordx4 %0, %1, %2, %3 offen offset:%4 sc1" : "=v"(hf[s][ml]) : "v"((ml) ? hv1 : hv0), "s"(rs), "s"(FR((s) >> 1) << 11), "n"(((s) & 1) * 1024) : "memory")
  u32 yfo;
  {
    f4 ca[4];
    W2_TIE(0);
    v4u za, zb;
    { const u32 p = LU + (u32)ch * LUS + 992 - 48 + 512 * rh + 32 * l15 + 16 * g;
      const v4u u0 = lds_ld<v4u>(p + 16), u1 = lds_ld<v4u>(p), u2 = lds_ld<v4u>(p + 1024 + 16), u3 = lds_ld<v4u>(p + 1024);
      conv_load<0, 0>(c0, ca_); conv_load<0, 1>(c0, ca_); conv_load<0, 2>(c0, ca_); SB();
      M0_2(3); DMA_2I(3, 0); DMA_2I(3, 1);
      const f4 z0 = mfma(u0, t7, f4{0, 0, 0, 0}), z1 = mfma(u1, t9, f4{0, 0, 0, 0}), z2 = mfma(u2, t7, f4{0, 0, 0, 0}), z3 = mfma(u3, t9, f4{0, 0, 0, 0});
      SB();
      STAMP(8);
      CV_ROUND(0, c0, c1, { DMA_2I(3, 2); DMA_2I(3, 3); }, { M0_2(4); DMA_2I(4, 0); _Pragma("unroll") for (int i = 0; i < 4; i++) { za[i] = pk2(z0[i], z1[i]); zb[i] = pk2(z2[i], z3[i]); } TIE(za); TIE(zb); },
               { DMA_2I(4, 1); W2_READ(1); }, { DMA_2I(4, 2); DMA_2I(4, 3); }); }
    STAMP(6);
    v2u fl; const u32 hpo = WS_HF + (u32)((2 * mt + ((lane >> 2) & 1)) * 32 + ((7 * w) >> 1) + (lane & 3)) * 128u;
    CV_ROUND(1, c1, c0, { W2_TIE(1); M0_2(5); DMA_2I(5, 0); }, { DMA_2I(5, 1); W2_READ(2); }, { DMA_2I(5, 2); DMA_2I(5, 3); }, { W2_TIE(2); M0_2(6); DMA_2I(6, 0); });
    CV_ROUND(2, c0, c1, { GLD("global_load_dwordx2", fl, hpo, ws, 0, "sc1"); DMA_2I(6, 1); }, DMA_2I(6, 2), DMA_2I(6, 3), {});
    STAMP(28);
    CV_ROUND(3, c1, c0, { VMW(3); TIE(fl); if (tag_miss(fl, tag)) { MARK(SLOW); poll(hpo, ws, tag); } STAMP(11); H_LD(0, 0); H_LD(0, 1); }, { H_LD(1, 0); H_LD(1, 1); }, { H_LD(2, 0); H_LD(2, 1); }, {});
    ca[2] = mfma(za, y1_, ca[2]); ca[3] = mfma(za, y2_, ca[3]); ca[3] = mfma(zb, y1_, ca[3]);
    STAMP(9);
    f4 k0, k1;
    const u32 ew = (w < 6 ? LEX + w * 2048 : LEX2 + (w - 6) * 2048) + ho, er = ((w ^ 2) < 6 ? LEX + (w ^ 2) * 2048 : LEX2 + ((w ^ 2) - 6) * 2048) + ho;
    if (rh) { lds_st<f4>(ew, ca[0]); lds_st<f4>(ew + 1024, ca[1]); k0 = ca[2]; k1 = ca[3]; }
    else { lds_st<f4>(ew, ca[2]); lds_st<f4>(ew + 1024, ca[3]); k0 = ca[0]; k1 = ca[1]; }
    LGW0(); BAR();
    k0 += lds_ld<f4>(er); k1 += lds_ld<f4>(er + 1024);
    const int i0 = 4 * rh + th;
    const u32 o = WS_Y + (u32)dch * YS + i0 * 512 + 32 * l15 + 8 * g;
    { v2u v = {pk2(k0[0], k0[1]), pk2(k0[2], k0[3])}; GST("global_store_dwordx2", o, v, ws, 0, "sc1"); }
    { v2u v = {pk2(k1[0], k1[1]), pk2(k1[2], k1[3])}; GST("global_store_dwordx2", o, v, ws, 1024, "sc1"); }
    yfo = lane == 0 ? WS_YF + 4096 + (u32)((w & 3) * 512 + dch) * 128u : 0x40000000u;
    STAMP(10);
    H_LD(3, 0); H_LD(3, 1);
  }
  {
    f16v c2[2][2];
    v4u wn[2][2], xr, b2v, yf; v2u fl;
    const u32 ypo = WS_YF + (u32)((((mt >> 3) & 2) + ((mt >> 2) & 1)) * 512 + nt * 64 + (w & 1) * 32 + l31) * 128u;
    const u32 oo = (u32)(mt * 64 + (tid >> 3)) * 1024u + nt * 128 + (tid & 7) * 16;
#define W2OP(s, nl) ((s) < 6 ? wf2[(s) < 6 ? (s) >> 1 : 0][(s) & 1][nl] : wn[(s) & 1][nl])
#define G2_STEP(s, NV, X) do { VMW(NV); TIE(hf[s][0]); TIE(hf[s][1]); \
      c2[0][0] = mfma32(W2OP(s, 0), hf[s][0], (s) == 0 ? f16v{0} : c2[0][0]); SB(); if ((s) + 4 < 14) H_LD((s) + 4, 0); SB(); \
      c2[1][0] = mfma32(W2OP(s, 1), hf[s][0], (s) == 0 ? f16v{0} : c2[1][0]); \
      if ((s) >= 5 && (s) < 13) { wn[((s) + 1) & 1][0] = W2FRAG((((s) + 1) >> 1) & 3, ((s) + 1) & 1, 0); wn[((s) + 1) & 1][1] = W2FRAG((((s) + 1) >> 1) & 3, ((s) + 1) & 1, 1); } SG(0x8, 1); SB(); \
      c2[0][1] = mfma32(W2OP(s, 0), hf[s][1], (s) == 0 ? f16v{0} : c2[0][1]); SB(); if ((s) + 4 < 14) H_LD((s) + 4, 1); SB(); \
      c2[1][1] = mfma32(W2OP(s, 1), hf[s][1], (s) == 0 ? f16v{0} : c2[1][1]); SB(); X; SB(); } while (0)
    if (w >= 4) __builtin_amdgcn_s_setprio(1);
    G2_STEP(0, 8, {});
    G2_STEP(1, 8, {});
    G2_STEP(2, 8, { VMW(8); FLAG_ST(yfo); });
    __builtin_amdgcn_s_setprio(0);
    STAMP(29);
    G2_STEP(3, 7, {});
    G2_STEP(4, 7, {});
    G2_STEP(5, 7, {});
    G2_STEP(6, 7, {});
    STAMP(30);
    G2_STEP(7, 6, {});
    G2_STEP(8, 6, {});
    G2_STEP(9, 6, { GLD("global_load_dwordx4", xr, oo, a.x, 0, ""); const u32 o2 = nt * 128 + (tid & 7) * 16; GLD("global_load_dwordx4", b2v, o2, a.b2, 0, ""); GLD("global_load_dwordx2", fl, ypo, ws, 0, "sc1"); });
    STAMP(31);
    G2_STEP(10, 9, {});
    G2_STEP(11, 7, {});
    G2_STEP(12, 5, {});
    G2_STEP(13, 3, {});
    STAMP(32);
    VMW(0); TIE(fl); if (tag_miss(fl, tag)) { MARK(SLOW); poll(ypo, ws, tag); }
    { const u32 o = WS_Y + (u32)(nt * 64 + (w & 1) * 32 + l31) * YS + (u32)(mt * 64 + (w >> 1) * 16 + hh * 8) * 2u; GLD("global_load_dwordx4", yf, o, ws, 0, "sc1"); }
    STAMP(12);
    v4u id;
    { const int e = l31 - 16 * ((w >> 1) & 1) - 8 * hh;
#pragma unroll
      for (int i = 0; i < 4; i++) id[i] = (e >> 1) == i ? (0x3f80u << (16 * (e & 1))) : 0u; }
#define P_PUT(nl, ml) do { const int row_ = 32 * ((ml) ^ (w >> 2)) + l31; _Pragma("unroll") for (int qd = 0; qd < 4; qd++) \
      lds_st<f4>((row_ >> 4) * 32768 + w4k + (row_ & 15) * 256 + 16 * ((8 * ((nl) ^ (w & 1)) + 2 * qd + hh) ^ (row_ & 7)), \
                 f4{c2[nl][ml][4 * qd], c2[nl][ml][4 * qd + 1], c2[nl][ml][4 * qd + 2], c2[nl][ml][4 * qd + 3]}); } while (0)
    P_PUT(1, 0); P_PUT(0, 1); P_PUT(1, 1); SB();
    VMW(0); TIE(yf); TIE(xr); TIE(b2v);
    c2[0][0] = mfma32(yf, id, c2[0][0]);
    P_PUT(0, 0);
    STAMP(13);
    LGW0(); BAR();
    const int row = tid >> 3, c8 = tid & 7;
    const u32 ra = (row >> 4) * 32768 + (row & 15) * 256 + 16 * ((2 * c8) ^ (row & 7)), rb = ra ^ 16u;
    f4 s0 = lds_ld<f4>(ra), s1 = lds_ld<f4>(rb);
#pragma unroll
    for (int ww = 1; ww < 8; ww++) { s0 += lds_ld<f4>(ra + ww * 4096); s1 += lds_ld<f4>(rb + ww * 4096); }
    v4u o;
    o.x = pk2(s0[0] + bf2f(b2v.x) + bf2f(xr.x), s0[1] + bfhi(b2v.x) + bfhi(xr.x));
    o.y = pk2(s0[2] + bf2f(b2v.y) + bf2f(xr.y), s0[3] + bfhi(b2v.y) + bfhi(xr.y));
    o.z = pk2(s1[0] + bf2f(b2v.z) + bf2f(xr.z), s1[1] + bfhi(b2v.z) + bfhi(xr.z));
    o.w = pk2(s1[2] + bf2f(b2v.w) + bf2f(xr.w), s1[3] + bfhi(b2v.w) + bfhi(xr.w));
    GST("global_store_dwordx4", oo, o, a.out, 0, "");
  }
  STAMP(14);
  if (bid == 255) { poll(WS_UF + (u32)(tid & 255) * 128u, ws, tag); BAR(); if (tid == 0) { u32 z = 0; GST("global_store_dwordx2", z, tag, ws, 0, "sc1"); } }
  if (bid == 255 && w == 0) { u32 a0,a1,a2,a3; const u32 o_ = WS_ST + lane * 128u;
    GLD("global_load_dword", a0, o_, ws, 0, "sc1"); GLD("global_load_dword", a1, o_, ws + 8192, 0, "sc1"); GLD("global_load_dword", a2, o_, ws + 16384, 0, "sc1"); GLD("global_load_dword", a3, o_, ws + 24576, 0, "sc1");
    VMW(0); TIE(a0); TIE(a1); TIE(a2); TIE(a3);
    int mn = min(min((int)(a0-(u32)tst),(int)(a1-(u32)tst)),min((int)(a2-(u32)tst),(int)(a3-(u32)tst))), mx = max(max((int)(a0-(u32)tst),(int)(a1-(u32)tst)),max((int)(a2-(u32)tst),(int)(a3-(u32)tst)));
    for (int o = 32; o > 0; o >>= 1) { mn = min(mn, __shfl_xor(mn, o)); mx = max(mx, __shfl_xor(mx, o)); }
    mn = __builtin_amdgcn_readfirstlane(mn); mx = __builtin_amdgcn_readfirstlane(mx);
    u64 te; asm volatile("s_memrealtime %0\n\ts_waitcnt lgkmcnt(0)" : "=s"(te)::"memory");
    u32 ex = min((u32)(500), 20000u);
    for(int q_=0;q_<100000;q_++){ u64 tn; asm volatile("s_memrealtime %0\n\ts_waitcnt lgkmcnt(0)" : "=s"(tn)::"memory"); if((u32)(tn-te)>=ex) break; } }
  asm volatile(".Lfk_end:");
}

extern "C" void kernel_launch(void* const* d_in, const int* in_sizes, int n_in, void* d_out, int out_size, void* d_ws, size_t ws_size, hipStream_t stream) {
  Args a;
  a.x = (const u16*)d_in[0]; a.are = (const u16*)d_in[1]; a.aim = (const u16*)d_in[2]; a.bre = (const u16*)d_in[3]; a.bim = (const u16*)d_in[4];
  a.cre = (const u16*)d_in[5]; a.cim = (const u16*)d_in[6]; a.ldt = (const u16*)d_in[7]; a.dsk = (const u16*)d_in[8];
  a.w1 = (const u16*)d_in[10]; a.b1 = (const u16*)d_in[11]; a.w2 = (const u16*)d_in[12]; a.b2 = (const u16*)d_in[13];
  a.out = (u16*)d_out; a.ws = (char*)d_ws; a.pad = a.x;
  (void)hipFuncSetAttribute((const void*)fused_kernel, hipFuncAttributeMaxDynamicSharedMemorySize, LDS_BYTES);
  fused_kernel<<<256, 512, LDS_BYTES, stream>>>(a);
}
```

```cpp
#include <hip/hip_runtime.h>
#include <stdint.h>

typedef unsigned u32; typedef unsigned long long u64; typedef unsigned short u16;
typedef u32 v4u __attribute__((ext_vector_type(4)));
typedef u32 v2u __attribute__((ext_vector_type(2)));
typedef float f4 __attribute__((ext_vector_type(4)));
typedef float f2 __attribute__((ext_vector_type(2)));
typedef float f16v __attribute__((ext_vector_type(16)));
typedef short s4 __attribute__((ext_vector_type(4)));
typedef __bf16 bf8 __attribute__((ext_vector_type(8)));
typedef __bf16 bf2 __attribute__((ext_vector_type(2)));
#define LAS __attribute__((address_space(3)))
#define DI __device__ __forceinline__

constexpr u32 WS_CF = 4096, WS_C = 32768;
constexpr u32 WS_UF = 65536, WS_ST = 131072  , WS_HF = 262144, WS_YF = 524288;
constexpr u32 WS_U = 1u << 20, US = 8192 + 128, YS = 4096 + 128, WS_Y = WS_U + 256 * US, WS_W = WS_Y + 512 * YS, WS_H = WS_W + (2u << 20), WS_END = WS_H + 64 * 112 * 1024;
constexpr u32 SP = 131072, KA = 0, KB = 640, KC = 960, KQ = 1232, KD = 1616, TBS = 1664;
constexpr u32 LXN = 65536, LSS = 98304, LTS = SP + 2 * TBS + 1024, LT1 = LTS + 16384 + 1024, LWR = SP + 30720;
constexpr u32 LU = LTS - 992, LUS = LT1 - LTS, LLT = LTS + 4096, LEX = LLT, LEX2 = LT1 + 4096;
constexpr u32 LDS_BYTES = 163840;
static_assert(LT1 + 8192 <= LWR && LTS % 64 == 0 && LT1 % 64 == 0 && LEX + 6 * 2048 <= LT1 - 992 && LEX2 + 4096 <= LDS_BYTES, "LDS map");

constexpr int rne(int j, int sh) { int q = j >> sh, rem = j & ((1 << sh) - 1), h = 1 << (sh - 1); if (rem > h || (rem == h && (q & 1))) q++; return q << sh; }
constexpr int rnd_bf(int j) { return j <= 256 ? j : rne(j, j < 512 ? 1 : j < 1024 ? 2 : 3); }
constexpr int i1(int j) { return (rne(j, 1) - 256) / 2; }
constexpr int ic(int j) { return (rnd_bf(j) - 512) / 4; }
constexpr u32 sel1(int i) { return i < 0 ? 0x0c0cu : (u32)(2 * i) | (u32)(2 * i + 1) << 8; }
constexpr u32 sel(int lo, int hi) { return sel1(lo) | sel1(hi) << 16; }
constexpr int a3(int m) { return m ? KQ + 128 + 2 * (m - 1) : KC + 256; }
struct alignas(128) Tabs { u32 wr[512]; u32 lt[6 * 256]; };
constexpr Tabs make_tabs() {
  Tabs t{};
  for (int tl = 0; tl < 4; tl++) for (int l = 0; l < 64; l++) {
    const int sg = (l & 15) + 16 * (tl & 1), g = l >> 4; int ad[4] = {};
    for (int r = 0; r < 4; r++)
      ad[r] = !(tl >> 1) ? (g < 2 ? KA + 2 * (16 + 8 * sg + 4 * g + r) : g == 2 ? KB + 2 * (4 * sg + r) : KC + 2 * (2 * sg + (r & 1) + 64 * (r >> 1)))
                         : (g == 0 ? a3(sg + 32 * r) : (g == 1 && r == 0 && sg == 0) ? a3(128) : (int)(LT1 + 8192 - SP - TBS) + 4 * l);
    t.wr[(tl * 64 + l) * 2] = (u32)ad[0] | (u32)ad[1] << 16; t.wr[(tl * 64 + l) * 2 + 1] = (u32)ad[2] | (u32)ad[3] << 16;
  }
  for (int l = 0; l < 64; l++) {
    const int o = 16 + (l & 15) - 8 * (l >> 4);
    const int a0 = 16 + o - 7, j1 = 32 * 9 + o, j2 = 32 * 17 + o, jx = 32 * 32 + o;
    const int b1 = i1(j1 - 7) & ~1, b2 = ic(j2 - 7) & ~1, bx = jx - 7 <= 1028 ? ic(jx - 7) & ~1 : 0;
    t.lt[l * 4 + 0] = sel((a0 & 1) + 1, a0 & 1);
    t.lt[l * 4 + 1] = sel(i1(j1 - 4) - b1, i1(j1 - 5) - b1);
    t.lt[l * 4 + 2] = sel(i1(j1 - 6) - b1, i1(j1 - 7) - b1);
    t.lt[l * 4 + 3] = (u32)(KA + 2 * (a0 & ~1)) | (u32)(KB + 2 * b1 - 32) << 16;
    for (int i = 0; i < 4; i++) {
      t.lt[256 + l * 4 + i] = sel(ic(j2 - 2 * i) - b2, ic(j2 - 2 * i - 1) - b2);
      t.lt[512 + l * 4 + i] = sel(jx - 2 * i <= 1028 ? ic(jx - 2 * i) - bx : -1, jx - 2 * i - 1 <= 1028 ? ic(jx - 2 * i - 1) - bx : -1);
      const int p = o - 2 * i, q = o + 8 - 2 * i;
      t.lt[768 + l * 4 + i] = (p >= 5 && p <= 11 ? 0x3f80u : 0u) | (p - 1 >= 5 && p - 1 <= 11 ? 0x3f800000u : 0u);
      t.lt[1024 + l * 4 + i] = (q >= 12 && q <= 20 ? 0x3f80u : 0u) | (q - 1 >= 12 && q - 1 <= 20 ? 0x3f800000u : 0u);
    }
    t.lt[1280 + l * 4 + 0] = (u32)(KC + 2 * b2 - 16) | (u32)(KC + 2 * bx) << 16;
    t.lt[1280 + l * 4 + 1] = l < 32 ? (u32)(KB + 2 * i1(256 + l)) | (u32)(KA + 2 * (16 + 256 + l)) << 16
                           : l < 49 ? (u32)(KC + 2 * ic(512 + 2 * (l - 32))) | (u32)(KB + 2 * (128 + l - 32)) << 16 : (u32)KD | (u32)KD << 16;
  }
  return t;
}
__device__ const Tabs g_tabs = make_tabs();

struct Args { const u16 *are, *aim, *bre, *bim, *cre, *cim, *x, *w1, *w2; char* ws; const u16 *ldt, *dsk, *b1, *b2; u16* out; const u16* pad; };

DI float bf2f(u32 v) { return __uint_as_float(v << 16); }
DI float bfhi(u32 v) { return __uint_as_float(v & 0xffff0000u); }
DI u32 pk2(float a, float b) { bf2 c; c[0] = (__bf16)a; c[1] = (__bf16)b; return __builtin_bit_cast(u32, c); }
DI float rbf(float a) { return bf2f(pk2(a, a) & 0xffffu); }
DI f4 mfma(v4u a, v4u b, f4 c) { return __builtin_amdgcn_mfma_f32_16x16x32_bf16(__builtin_bit_cast(bf8, a), __builtin_bit_cast(bf8, b), c, 0, 0, 0); }
DI f16v mfma32(v4u a, v4u b, f16v c) { return __builtin_amdgcn_mfma_f32_32x32x16_bf16(__builtin_bit_cast(bf8, a), __builtin_bit_cast(bf8, b), c, 0, 0, 0); }
DI float dot2(u32 a, u32 b, float c) { return __builtin_amdgcn_fdot2_f32_bf16(__builtin_bit_cast(bf2, a), __builtin_bit_cast(bf2, b), c, false); }
template <class T> DI T lds_ld(u32 a) { return *(const LAS T*)(size_t)a; }
template <class T> DI void lds_st(u32 a, T v) { *(LAS T*)(size_t)a = v; }
DI v2u lds_tr(u32 a) { return __builtin_bit_cast(v2u, __builtin_amdgcn_ds_read_tr16_b64_v4i16((LAS s4*)(size_t)a)); }
DI v4u lds_tr2(u32 a, u32 b) { v2u x = lds_tr(a), y = lds_tr(b); return v4u{x.x, x.y, y.x, y.y}; }
DI v4u lds_2x64(u32 a) { v2u x = lds_ld<v2u>(a); asm volatile(""); v2u y = lds_ld<v2u>(a + 8); return v4u{x.x, x.y, y.x, y.y}; }
DI float xor32_add(float v) { auto r = __builtin_amdgcn_permlane32_swap(__float_as_uint(v), __float_as_uint(v), false, false); return __uint_as_float(r[0]) + __uint_as_float(r[1]); }
#define DPP_ADD(v, ctrl) v += __uint_as_float(__builtin_amdgcn_update_dpp(0u, __float_as_uint(v), ctrl, 0xf, 0xf, true))
DI float sum32(float v) {
  DPP_ADD(v, 0xB1); DPP_ADD(v, 0x4E); DPP_ADD(v, 0x141); DPP_ADD(v, 0x140);
  auto r = __builtin_amdgcn_permlane16_swap(__float_as_uint(v), __float_as_uint(v), false, false); return __uint_as_float(r[0]) + __uint_as_float(r[1]);
}
DI void cmul2(f2& r0, f2 a0, f2 w0, f2& r1, f2 a1, f2 w1) {
  asm("v_pk_mul_f32 %0, %2, %3 op_sel:[0,0] op_sel_hi:[1,0]\n\tv_pk_mul_f32 %1, %4, %5 op_sel:[0,0] op_sel_hi:[1,0]\n\t"
      "v_pk_fma_f32 %0, %2, %3, %0 op_sel:[1,1,0] op_sel_hi:[0,1,1] neg_lo:[1,0,0]\n\tv_pk_fma_f32 %1, %4, %5, %1 op_sel:[1,1,0] op_sel_hi:[0,1,1] neg_lo:[1,0,0]"
      : "=&v"(r0), "=&v"(r1) : "v"(a0), "v"(w0), "v"(a1), "v"(w1));
}
DI f2 cexp2(f2 x) {
  float e = __builtin_amdgcn_exp2f(x.x), c = __builtin_amdgcn_cosf(x.y), s = __builtin_amdgcn_sinf(x.y);
  return f2{c, s} * e;
}
DI void ln_stats(float s, float q, float& mb, float& sd) {
  const float mean = s * (1.f / 512.f);
  const float var = fmaxf(q * (1.f / 512.f) - mean * mean, 0.f);
  sd = rbf(__builtin_amdgcn_sqrtf(rbf(rbf(var) + 1e-5f))); mb = rbf(mean);
}
#define VMW(n) asm volatile("s_waitcnt vmcnt(%0)" ::"n"(n) : "memory")
#define LGW0() asm volatile("s_waitcnt lgkmcnt(0)" ::: "memory")
#define BAR() asm volatile("s_barrier" ::: "memory")
#define TIE(x) asm volatile("" : "+v"(x))
#define MARK(x) asm volatile("; PATH_" #x)
#define SB() __builtin_amdgcn_sched_barrier(0)
#define SG(m, n) __builtin_amdgcn_sched_group_barrier(m, n, 0)
#define GLD(op, dst, voff, sbase, imm, fl) asm volatile(op " %0, %1, %2 offset:%3 " fl : "=v"(dst) : "v"(voff), "s"(sbase), "n"(imm) : "memory")
#define GST(op, voff, src, sbase, imm, fl) asm volatile(op " %0, %1, %2 offset:%3 " fl "\n\ts_nop 1" ::"v"(voff), "v"(src), "s"(sbase), "n"(imm) : "memory")
#define M0SET(lbase, limm) asm volatile("s_add_u32 m0, %0, %1\n\ts_nop 0" ::"s"(lbase), "n"(limm) : "memory", "scc")
#define DMA(voff, srd, soff, off) asm volatile("buffer_load_dwordx4 %0, %1, %2 offen offset:%3 lds" ::"v"(voff), "s"(srd), "s"(soff), "n"(off) : "memory")
DI v4u make_srd(const void* p, u32 recs) { const u64 a_ = (u64)p - 4096; return v4u{(u32)a_, (u32)(a_ >> 32), recs, 0x20000u}; }
#define STIE(x) asm volatile("" : "+s"(x))
DI bool tag_miss(v2u f, v2u tag) { return __builtin_expect(__builtin_amdgcn_ballot_w64(f.x != tag.x || f.y != tag.y) != 0, 0); }
DI void poll(u32 voff, const char* ws, v2u tag) {
#pragma nounroll
  for (int it = 0; it < (1 << 16); it++) {
    v2u f;
    asm volatile("global_load_dwordx2 %0, %1, %2 sc1\n\ts_waitcnt vmcnt(0)" : "=v"(f) : "v"(voff), "s"(ws) : "memory");
    if (__builtin_amdgcn_ballot_w64(f.x != tag.x || f.y != tag.y) == 0) break;
    __builtin_amdgcn_s_sleep(6);
  }
}
#define STAMP(i)

struct CvA { u32 ua, a0, a1, a2; };
struct CvS { v4u s01, s2; };
struct ConvRaw { v4u A[8]; u32 e[5], f[3], q[2][2]; };
DI u32 prm(u32 hi, u32 lo, u32 s) { return __builtin_amdgcn_perm(hi, lo, s); }
DI v4u perm4(u32 hi, u32 lo, v4u s) { return v4u{prm(hi, lo, s.x), prm(hi, lo, s.y), prm(hi, lo, s.z), prm(hi, lo, s.w)}; }
DI v4u frag0(const u32 (&e)[5], u32 s) { return v4u{prm(e[4], e[3], s), prm(e[3], e[2], s), prm(e[2], e[1], s), prm(e[1], e[0], s)}; }
DI v4u frag1(const u32 (&f)[3], u32 sa, u32 sb) { return v4u{prm(f[2], f[1], sa), prm(f[2], f[1], sb), prm(f[1], f[0], sa), prm(f[1], f[0], sb)}; }
template <int RR, int P> DI void conv_load(ConvRaw& c, const CvA& a) {
  if (P == 0) {
#pragma unroll
    for (int i = 0; i < 5; i++) if (RR) c.e[i] = lds_ld<u32>(a.a0 + 64 * RR + 4 * i);
    c.A[7] = lds_ld<v4u>(a.ua + 64 * (3 - RR) + 512 * 7); c.A[5] = lds_ld<v4u>(a.ua + 64 * (3 - RR) + 512 * 5);
  }
  if (P == 1) {
    c.A[3] = lds_ld<v4u>(a.ua + 64 * (3 - RR) + 512 * 3); c.A[1] = lds_ld<v4u>(a.ua + 64 * (3 - RR) + 512);
    if (RR) {
#pragma unroll
      for (int i = 0; i < 3; i++) c.f[i] = lds_ld<u32>(a.a1 + 32 * RR + 4 * i);
    }
  }
  if (P == 2) {
#pragma unroll
    for (int i = 6; i >= 0; i -= 2) c.A[i] = lds_ld<v4u>(a.ua + 64 * (3 - RR) + 512 * i);
  }
  if (P == 3) {
#pragma unroll
    for (int m = 2; m < 4; m++) if (RR || m != 2) {
      const u32 p = a.a2 + 16 * RR + 128 * (m - 2);
      c.q[m - 2][0] = lds_ld<u32>(p); c.q[m - 2][1] = lds_ld<u32>(p + 4);
    }
  }
}
template <int RR, int M> DI v4u conv_b(const ConvRaw& c, const CvS& s, v4u x1, v4u x2) {
  return (RR == 0 && M == 1) ? x1 : (RR == 0 && M == 2) ? x2 : M == 1 ? frag1(c.f, s.s01.y, s.s01.z) : perm4(c.q[M < 2 ? 0 : M - 2][1], c.q[M < 2 ? 0 : M - 2][0], s.s2);
}
template <int M, bool Z> DI void conv_mm(f4 (&acc)[4], const ConvRaw& c, v4u B) {
#pragma unroll
  for (int t = 3; t >= 0; t--) if (2 * t + 1 >= M) acc[t] = mfma(B, c.A[2 * t + 1 - M], Z ? f4{0, 0, 0, 0} : acc[t]);
}
#define CV_SG(n) do { _Pragma("unroll") for (int i_ = 0; i_ < (n); i_++) { SG(0x8, 1); SG(0x100, 2); SG(0x2, 2); } SB(); } while (0)
#define CV_ROUND(RR, c, n, X0, X1, X2, X3) do { \
    v4u b1_ = conv_b<RR, 1>(c, cs_, x1, x2); if ((RR) < 3) conv_load<((RR) + 1) & 3, 0>(n, ca_); conv_mm<0, (RR) == 0>(ca, c, b0); CV_SG(4); TIE(b1_); X0; SB(); \
    v4u b2_ = conv_b<RR, 2>(c, cs_, x1, x2); if ((RR) < 3) conv_load<((RR) + 1) & 3, 1>(n, ca_); conv_mm<1, false>(ca, c, b1_); CV_SG(4); TIE(b2_); X1; SB(); \
    v4u b3_ = conv_b<RR, 3>(c, cs_, x1, x2); if ((RR) < 3) conv_load<((RR) + 1) & 3, 2>(n, ca_); conv_mm<2, false>(ca, c, b2_); CV_SG(3); TIE(b3_); X2; SB(); \
    if ((RR) < 3) { conv_load<((RR) + 1) & 3, 3>(n, ca_); b0 = frag0(n.e, cs_.s01.x); } \
    conv_mm<3, false>(ca, c, b3_); if ((RR) == 0) conv_mm<4, false>(ca, c, x4); CV_SG((RR) == 0 ? 5 : 3); TIE(b0); X3; SB(); } while (0)

typedef u32 v16u __attribute__((ext_vector_type(16)));
#define KP(v, i) ((const char*)(((u64)v[2 * (i) + 1] << 32) | v[2 * (i)]))
__global__ __launch_bounds__(512, 2) void fused_kernel(Args) {
  STAMP(0);
  v16u k0_, k1_;
  asm volatile("s_load_dwordx16 %0, %2, 0x0\n\ts_load_dwordx16 %1, %2, 0x40" : "=&s"(k0_), "=&s"(k1_) : "s"(__builtin_amdgcn_kernarg_segment_ptr()) : "memory");
  const int tid = threadIdx.x, lane = tid & 63, w = __builtin_amdgcn_readfirstlane(tid >> 6), l15 = lane & 15, g = lane >> 4, l31 = lane & 31, hh = lane >> 5;
  const int bid = blockIdx.x, xcd = bid & 7, idx = bid >> 3;
  const int oct = xcd * 32 + idx;
  const int ch = w >> 2, dch = 2 * oct + ch;
  const bool tw = ((w >> 1) ^ ch) & 1;
  const int lr = 4 * ch + 2 * (w & 1) + hh;
  const int md = (w & 1) * 64 + lane;
  u32 xo = (u32)(oct * 8 + lr) * 1024u + (u32)l31 * 16u, po = (u32)(dch * 128 + md) * 2u;
  TIE(xo); TIE(po);
  if (w < 2) { const u32 o = (u32)tid * 16u, d = LWR + w * 1024;
    asm volatile("s_mov_b32 m0, %2\n\ts_nop 0\n\tglobal_load_lds_dwordx4 %0, %1" ::"v"(o), "s"(g_tabs.wr), "s"(d) : "memory");
  } else if (w < 4) {
    u64 pc; u32 len; asm volatile("s_getpc_b64 %0\n.Lfk_beg:\n\ts_mov_b32 %1, .Lfk_end-.Lfk_beg" : "=s"(pc), "=s"(len));
    const u32 ln = (idx & 15) * 2 + w - 2, o = min((ln * 6 + lane) * 128u, len), o2 = min(ln * 2 + (lane & 1), 47u) * 128u;
    if (lane < 6) asm volatile("s_mov_b32 m0, %4\n\ts_nop 0\n\tglobal_load_lds_dword %0, %1\n\tglobal_load_lds_dword %2, %3" ::"v"(o), "s"(pc), "v"(o2), "s"(g_tabs.lt), "n"(SP + KD) : "memory");
  }
  const bool gw = __builtin_expect(idx < 28, 1);
  const int j4 = (idx * 37) >> 8, i7 = idx - 7 * j4;
  const int rt = (xcd >> 1) * 4 + j4, ft = (xcd & 1) * 7 + i7, rot = i7 ^ (2 * j4);
  const int mt = xcd * 4 + (idx >> 3), nt = idx & 7;
  const u32 w4k = (u32)w * 4096u;
  const int cg = (xcd & 1) * 14 + min((xcd >> 1) * 4 + (idx - 28), 13);
  u32 vx0, vx1, vw0, vw1, xs0, xs1, ws0, ws1;
  if (gw) {
    vx0 = (u32)(rt * 128 + w * 16 + (lane >> 3)) * 1024u + 16u * ((lane & 7) ^ (lane >> 4)) + 4096u; vx1 = (vx0 ^ 64u) + 8192u - 1024u;
    vw0 = (u32)(8 * w + g) * 3584u + 256u * ft + 16u * (l15 ^ (4 * g)) + 4096u - 2048u; vw1 = vw0 + 4 * 3584 - 1024;
  } else { vx0 = (u32)(64 * w + (lane >> 3)) * 3584u + 128u * cg + 16u * (lane & 7) + 4096u; vx1 = vx0 + 8 * 3584 - 1024; vw0 = vx0 + 16 * 3584 - 2048; vw1 = vx0 + 24 * 3584 - 3072; }
  xs0 = gw ? rot * 128 : 0; xs1 = gw ? (rot ^ 1) * 128 : 32 * 3584; ws0 = gw ? rot * (64 * 3584) : 0; ws1 = gw ? (rot ^ 1) * (64 * 3584) : 32 * 3584;
  const int fq = oct * 7 + w;
  u32 wo = (u32)((fq >> 4) * 16 + (lane >> 2)) * 1024u + (fq & 15) * 64u + (lane & 3) * 16u + (w < 7 ? 4096u - 2048u : 0x40000000u);
  TIE(vx0); TIE(vx1); TIE(vw0); TIE(vw1); TIE(wo);
  asm volatile("s_waitcnt lgkmcnt(0)" : "+s"(k0_), "+s"(k1_)::"memory");
  struct { const char *are, *aim, *bre, *bim, *cre, *cim, *x, *w1, *w2, *ws, *ldt, *dsk, *b1, *b2, *out; } a =
    {KP(k0_, 0), KP(k0_, 1), KP(k0_, 2), KP(k0_, 3), KP(k0_, 4), KP(k0_, 5), KP(k0_, 6), KP(k0_, 7), KP(k1_, 0), KP(k1_, 1), KP(k1_, 2), KP(k1_, 3), KP(k1_, 4), KP(k1_, 5), KP(k1_, 6)};
  const char* ws = a.ws;
  u32 pa, pb, pc_, pd, pe, pg; v4u xr0, xr1;
  GLD("global_load_short_d16_hi", pa, po, a.are, 0, ""); GLD("global_load_short_d16_hi", pb, po, a.aim, 0, "");
  u64 ep; u32 sl, sk_;
  { const u32 b4 = oct * 4;
    asm volatile("s_load_dwordx2 %0, %3, 0x0\n\ts_load_dword %1, %4, %6\n\ts_load_dword %2, %5, %6" : "=&s"(ep), "=&s"(sl), "=&s"(sk_) : "s"(ws), "s"(a.ldt), "s"(a.dsk), "s"(b4) : "memory"); }
  if (tw) { MARK(KT); GLD("global_load_dwordx4", xr0, xo, a.x, 0, ""); GLD("global_load_dwordx4", xr1, xo, a.x, 512, ""); }
  else { MARK(KS); GLD("global_load_short_d16_hi", pc_, po, a.bre, 0, ""); GLD("global_load_short_d16_hi", pd, po, a.bim, 0, ""); GLD("global_load_short_d16_hi", pe, po, a.cre, 0, ""); GLD("global_load_short_d16_hi", pg, po, a.cim, 0, ""); }
  SB();
  const v4u rw = make_srd(a.w1, 512 * 3584 + 4096), rx = make_srd(gw ? a.x : a.w1, gw ? 2048 * 1024 + 4096 : 512 * 3584 + 4096), r2 = make_srd(a.w2, 1792 * 1024 + 4096), rs = make_srd(ws, WS_END + 4096);
#define XS(c) (u32)(((c) ^ rot) * 128)
#define WS_(c) (u32)(((c) ^ rot) * (64 * 3584))
#define DMA_X(c) do { DMA(vx0, rx, XS(c), 0); DMA(vx1, rx, XS(c), 1024); } while (0)
#define DMA_W(c) do { DMA(vw0, rw, WS_(c), 2048); DMA(vw1, rw, WS_(c), 3072); } while (0)
  BAR();
  M0SET(w4k, 0); DMA(vx0, rx, xs0, 0); DMA(vx1, rx, xs0, 1024); DMA(vw0, rw, ws0, 2048); DMA(vw1, rw, ws0, 3072);
  STAMP(16);
  const int mw = w & 3, nh = w >> 2, q4 = l15 >> 2;
  u32 xa[2][4], wa[2][2], ksa, kta;
  { const int row = 32 * mw + l31;
#pragma unroll
    for (int r = 0; r < 4; r++) { xa[0][r] = (row >> 4) * 4096 + (row & 15) * 128 + 16 * ((2 * r + hh) ^ ((row >> 1) & 7)); TIE(xa[0][r]); }
#pragma unroll
    for (int nl = 0; nl < 2; nl++) { wa[0][nl] = hh * 4096 + 2048 + q4 * 256 + 64 * ((2 * nh + nl) ^ q4) + 32 * (g & 1) + 8 * (lane & 3); TIE(wa[0][nl]); }
    const u32 o = (2 * g + (q4 >> 1)) * 64 + 8 * (lane & 1), o1 = o + 16 * (((lane >> 1) & 3) ^ g);
    ksa = LSS + ch * 16384 + (w & 1) * 8192 + o1;
    kta = (w & 2) ? LT1 + o + 16 * ((2 * ch + (q4 & 1)) ^ g) : LTS + ch * 8192 + o1;
    TIE(ksa); TIE(kta); }
  u32 wra = 65536 + 2048 + w4k + (4 * hh + q4) * 64 + 32 * (g & 1) + 8 * (lane & 3); TIE(wra);
  u32 wso = WS_W + (u32)((((fq >> 1) & 7) * 112 + (fq >> 4)) * 2 + (fq & 1)) * 1024u + lane * 16u + (w < 7 ? 4096u : 0x40000000u), uso = WS_U + (u32)oct * US + (u32)tid * 16u;
  TIE(wso); TIE(uso);
  if (w < 2) lds_st<v4u>(lane < 62 ? LU + w * LUS + lane * 16 : SP + w * TBS + lane * 16 - 992, v4u{0, 0, 0, 0});
  else if (tid < 144) lds_st<v4u>(SP + (lane >> 3) * TBS + KQ + (lane & 7) * 16, v4u{0, 0, 0, 0});
  asm volatile("s_waitcnt lgkmcnt(0)" : "+s"(ep), "+s"(sl), "+s"(sk_)::"memory");
  v2u tag;
  { u64 z = (ep + 0x9E3779B97F4A7C15ull) * 0xBF58476D1CE4E5B9ull; z ^= z >> 29; STIE(z); tag.x = (u32)z; tag.y = (u32)(z >> 32); }
  float dt;
  { const float xx = __uint_as_float(ch ? sl & 0xffff0000u : sl << 16), t = xx * 1.4426950216293335f, r = __builtin_fmaf(xx, 1.9259629911266175e-8f, __builtin_fmaf(xx, 1.4426950216293335f, -t));
    const float e = __builtin_amdgcn_exp2f(t); dt = rbf(__builtin_fmaf(e * r, 0.6931471805599453f, e)); TIE(dt); }
  float k00 = __uint_as_float((tid & 255) == 0 ? (ch ? sk_ & 0xffff0000u : sk_ << 16) : 0u); TIE(k00);
  SB();
  M0SET(w4k, 65536); DMA(wo, r2, 0, 2048);
  M0SET(w4k, 32768); DMA(vw0, rw, ws1, 2048); DMA(vw1, rw, ws1, 3072);
  VMW(7); TIE(pa); TIE(pb);
  STAMP(1);
  v4u un; v2u sv;
#define SYNC_A() do { LGW0(); BAR(); const u32 ra = LXN + (tid & 1) * 16384 + (((tid >> 1) * 4) ^ ((tid & 1) * 64)); \
    un = v4u{lds_ld<u32>(ra), lds_ld<u32>(ra + 4096), lds_ld<u32>(ra + 8192), lds_ld<u32>(ra + 12288)}; sv = lds_ld<v2u>(LXN + 1024 + (lane & 7) * 8); SB(); } while (0)
#define ST_A() do { SB(); DMA(vx0, rx, xs1, 0); DMA(vx1, rx, xs1, 1024); GST("global_store_dwordx4", uso, un, ws, 0, "sc1"); \
    const u32 o_ = tid < 8 ? WS_U + 4096 + 8192 + (u32)oct * US + tid * 8u : 0x40000000u; asm volatile("buffer_store_dwordx2 %0, %1, %2, 0 offen sc1" ::"v"(sv), "v"(o_), "s"(rs) : "memory"); SB(); } while (0)

  const u32 tb = SP + (u32)ch * TBS;
  {
    const u32 dk = md * 64 + 16 * ((lane >> 1) & 3);
#define AF_MAKE() float arl = dt * bfhi(pa) * 1.4426950408889634f; arl = fminf(fmaxf(arl, -1e30f), 1e30f); float f = dt * bfhi(pb) * 0.15915494309189535f; f -= rintf(f); const f2 af = {arl, f}
    if (!tw) {
      MARK(KS);
      AF_MAKE();
      TIE(pc_); TIE(pd); TIE(pe); TIE(pg);
      const f2 z8 = cexp2(af * 8.f), bb = f2{bfhi(pc_), bfhi(pd)} * dt, cc = {bfhi(pe), bfhi(pg)};
      f2 z16, g0, g1, t_;
      cmul2(z16, z8, z8, g0, cc, bb); cmul2(g1, g0, z8, t_, g0, z8);
      const u32 d = LSS + ch * 16384 + dk;
#pragma unroll
      for (int c = 0; c < 4; c++) {
        v4u re, im;
#pragma unroll
        for (int i = 0; i < 4; i++) { f2 n0 = g0, n1 = g1; if (c < 3 || i < 3) cmul2(n0, g0, z16, n1, g1, z16); re[i] = pk2(g0.x, g1.x); im[i] = pk2(g0.y, g1.y); g0 = n0; g1 = n1; }
        lds_st<v4u>((d ^ (16 * (c & 1))) + (c >> 1) * 8192, re); lds_st<v4u>((d ^ (32 + 16 * (c & 1))) + (c >> 1) * 8192, im);
        if (c == 1) SYNC_A();
        if (c == 2) ST_A();
      }
    } else {
      MARK(KT);
      __builtin_amdgcn_s_setprio(2);
      TIE(xr0); TIE(xr1);
      float ss = 0.f, sq = 0.f, ss2 = 0.f, sq2 = 0.f;
#pragma unroll
      for (int e = 0; e < 4; e++) { const u32 v = xr0[e], v1 = xr1[e]; ss = dot2(v, 0x3f803f80u, ss); sq = dot2(v, v, sq); ss2 = dot2(v1, 0x3f803f80u, ss2); sq2 = dot2(v1, v1, sq2); }
      ss = sum32(ss + ss2); sq = sum32(sq + sq2);
      float mb, sd; ln_stats(ss, sq, mb, sd);
      lds_st<v2u>(LXN + 1024 + lr * 8, v2u{pk2(-mb, -mb), pk2(-mb, sd)});
      const float rstd = __builtin_amdgcn_rcpf(sd), nmr = -mb * rstd;
      v4u o0, o1;
#pragma unroll
      for (int e = 0; e < 4; e++) { const u32 v = xr0[e], v1 = xr1[e]; const f2 t = f2{bf2f(v), bfhi(v)} * rstd + nmr, t1 = f2{bf2f(v1), bfhi(v1)} * rstd + nmr; o0[e] = pk2(t.x, t.y); o1[e] = pk2(t1.x, t1.y); }
      lds_st<v4u>(LXN + (lr << 12) + ((l31 ^ (4 * ch)) << 4), o0); lds_st<v4u>(LXN + 512 + (lr << 12) + ((l31 ^ (4 * ch)) << 4), o1);
      SYNC_A();
      AF_MAKE();
      const f2 z1 = cexp2(af), y1 = cexp2(af * 256.f);
      f2 z2, y2, z3, z4, y3, y4, z5, z6, z7, a2, a4, a6, b4, c4, y5, y6, y7, y8;
      cmul2(z2, z1, z1, y2, y1, y1);
      ST_A();
      cmul2(z3, z2, z1, z4, z2, z2); cmul2(y3, y2, y1, y4, y2, y2); cmul2(z5, z4, z1, z6, z4, z2); cmul2(z7, z4, z3, a2, y1, z2);
      const u32 d = LTS + ch * 8192 + dk, d1 = (LT1 + dk) ^ (32 * ch);
      lds_st<v4u>(d, v4u{pk2(1.f, z1.x), pk2(z2.x, z3.x), pk2(z4.x, z5.x), pk2(z6.x, z7.x)});
      lds_st<v4u>(d ^ 32, v4u{pk2(0.f, -z1.y), pk2(-z2.y, -z3.y), pk2(-z4.y, -z5.y), pk2(-z6.y, -z7.y)});
      cmul2(a4, y1, z4, a6, y1, z6); cmul2(b4, y2, z4, c4, y3, z4); cmul2(y5, y4, y1, y6, y4, y2); cmul2(y7, y4, y3, y8, y4, y4);
      lds_st<v4u>(d ^ 16, v4u{pk2(y1.x, a2.x), pk2(a4.x, a6.x), pk2(y2.x, b4.x), pk2(y3.x, c4.x)});
      lds_st<v4u>(d ^ 48, v4u{pk2(-y1.y, -a2.y), pk2(-a4.y, -a6.y), pk2(-y2.y, -b4.y), pk2(-y3.y, -c4.y)});
      lds_st<v4u>(d1, v4u{pk2(y4.x, y5.x), pk2(y6.x, y7.x), pk2(y8.x, y8.x), 0u});
      lds_st<v4u>(d1 ^ 16, v4u{pk2(-y4.y, -y5.y), pk2(-y6.y, -y7.y), pk2(-y8.y, -y8.y), 0u});
      __builtin_amdgcn_s_setprio(0);
    }
  }
  STAMP(2);
#define LT_DMA() do { const u32 d_ = LLT + min(w, 6) * 1024; asm volatile("s_mov_b32 m0, %2\n\ts_nop 0\n\tglobal_load_lds_dwordx4 %0, %1" ::"v"(lto), "s"(g_tabs.lt), "s"(d_) : "memory"); } while (0)
  STAMP(40);
  VMW(6);
  STAMP(41);
  { const v4u wt = lds_tr2(wra, wra + 512); asm volatile("buffer_store_dwordx4 %0, %1, %2, 0 offen sc1\n\ts_nop 1" ::"v"(wt), "v"(wso), "s"(rs) : "memory"); }
  LGW0(); BAR();
  STAMP(18);
  u32 ta2, ta2h, afo, so, co, uo_, lto, ufs; v2u tagv = tag;
#define FLAG_ST(o) asm volatile("buffer_store_dwordx2 %0, %1, %2, 0 offen sc1" ::"v"(tagv), "v"(o), "s"(rs) : "memory")
    f16v acc[2];
    v4u xf[2][2], wf[2][2][2];
#define G1_RD(st, r_) do { constexpr int sl_ = ((st) >> 1) % 3, p_ = sl_ >> 1; constexpr u32 sb_ = (sl_ & 1) * 32768, wo_ = sb_ + ((st) & 1) * 16384 + (r_) * 8192; \
      xf[(st) & 1][r_] = lds_ld<v4u>(xa[p_][2 * ((st) & 1) + (r_)] + sb_); \
      wf[(st) & 1][0][r_] = lds_tr2(wa[p_][0] + wo_, wa[p_][0] + wo_ + 1024); wf[(st) & 1][1][r_] = lds_tr2(wa[p_][1] + wo_, wa[p_][1] + wo_ + 1024); } while (0)
#define G1_MM(st, n_, r_) acc[n_] = mfma32(wf[(st) & 1][n_][r_], xf[(st) & 1][r_], (st) + (r_) == 0 ? f16v{0} : acc[n_])
#define G1_STEP(st, X2, X3) do { \
      G1_MM(st, 0, 0); if ((st) < 15) G1_RD((st) + 1, 0); SG(0x8, 1); SG(0x100, 5); SB(); \
      G1_MM(st, 1, 0); if ((st) < 15) G1_RD((st) + 1, 1); SG(0x8, 1); SG(0x100, 5); SB(); \
      G1_MM(st, 0, 1); SB(); X2; SB(); \
      G1_MM(st, 1, 1); SB(); X3; SB(); } while (0)
#define G1_SYNC(NV) do { VMW(NV); LGW0(); BAR(); SB(); } while (0)
  v4u kA[2], kB[2]; f4 kk[2]; v2u wa_;
#define K_RD(k) do { kA[(k) & 1] = lds_tr2(kta + (k) * 1024, kta + (k) * 1024 + 512); kB[(k) & 1] = lds_tr2(ksa + (k) * 1024, ksa + (k) * 1024 + 512); } while (0)
#define K_MM(k) do { kk[(k) & 1] = mfma(kA[(k) & 1], kB[(k) & 1], (k) == 0 ? f4{k00, 0, 0, 0} : (k) == 1 ? f4{0, 0, 0, 0} : kk[(k) & 1]); TIE(kk[(k) & 1]); } while (0)
#define K_PUT() do { const f4 k_ = kk[0] + kk[1]; const u32 p0 = pk2(k_[0], k_[1]), p1 = pk2(k_[2], k_[3]);  { \
      lds_st<u16>(tb + (wa_.x & 0xffffu), (u16)p0); lds_st<u16>(tb + (wa_.x >> 16), (u16)(p0 >> 16)); \
      lds_st<u16>(tb + (wa_.y & 0xffffu), (u16)p1); lds_st<u16>(tb + (wa_.y >> 16), (u16)(p1 >> 16)); } } while (0)
#define SET_A() do { _Pragma("unroll") for (int r = 0; r < 4; r++) { xa[1][r] = xa[0][r] + 65536; TIE(xa[1][r]); } wa[1][0] = wa[0][0] + 65536; wa[1][1] = wa[0][1] + 65536; TIE(wa[1][0]); TIE(wa[1][1]); } while (0)
#define SET_B() do { ufs = tid == 448 ? WS_UF + 4096 + (u32)oct * 128u : 0x40000000u; TIE(ufs); TIE(tagv); } while (0)
#define SET_C() do { ta2 = w4k + lane * 16 + (w & 1) * 1024; ta2h = ta2 + 65536; TIE(ta2); TIE(ta2h); uo_ = WS_U + (u32)(tid >> 1) * US + (u32)oct * 32u + (tid & 1) * 16u; TIE(uo_); } while (0)
#define SET_D() do { afo = WS_UF + (u32)(hh ? rt * 16 + mw * 4 + (lane & 3) : 32 * w + lane) * 128u; const u32 t_ = WS_CF + (u32)(2 * ft + nh) * 128u; afo = lane >= 48 ? t_ : afo; TIE(afo); } while (0)
#define SET_F() do { lto = (u32)min(tid, 383) * 16u; TIE(lto); wa_ = lds_ld<v2u>(LWR + (tid & 255) * 8); } while (0)
#define SET_E() do { so = WS_U + 8192 + (u32)(rt * 16 + mw * 4 + (l31 >> 3)) * US + (lane & 7) * 8u; co = WS_C + (u32)(ft * 128 + nh * 64 + l31) * 8u; TIE(so); TIE(co); } while (0)
  const u32 seq = (u32)(0x65432106543210ull >> (4 * (nt == 7 ? 3 : nt)));
#define FR(f) ((seq >> (4 * (f))) & 7u)
  const u32 ho = (u32)lane * 16u, w2s = WS_W + 4096 + (u32)(nt * 112 + 14 * w) * 2048u;
#define DMA_2I(f, i) asm volatile("buffer_load_dwordx4 %0, %1, %2 offen offset:%3 sc1 lds" ::"v"(ho), "s"(rs), "s"(w2s + (FR(f) << 12)), "n"((i) * 1024) : "memory")
#define SL2(f) ((0x1023000 >> (4 * (f))) & 3)
#define M0_2(f) M0SET(w4k, SL2(f) * 32768)
#define DMA_2(f) do { M0_2(f); DMA_2I(f, 0); DMA_2I(f, 1); DMA_2I(f, 2); DMA_2I(f, 3); } while (0)
  v4u wf2[3][2][2];
#define W2FRAG(f, s2, nl) lds_ld<v4u>(((SL2(f) & 2 ? ta2h : ta2) ^ ((nl) * 1024)) + (SL2(f) & 1) * 32768 + (s2) * 2048)
  const u32 hx = ho ^ ((w & 1) * 1024);
#define W2_LD(f, s_, n_) asm volatile("buffer_load_dwordx4 %0, %1, %2, %3 offen offset:%4 sc1" : "=v"(wf2[f][s_][n_]) : "v"((n_) ? hx ^ 1024u : hx), "s"(rs), "s"(w2s + (FR(f) << 12)), "n"((s_) * 2048) : "memory")
#define W2_TIE2() do { _Pragma("unroll") for (int f_ = 0; f_ < 2; f_++) _Pragma("unroll") for (int s_ = 0; s_ < 2; s_++) _Pragma("unroll") for (int n_ = 0; n_ < 2; n_++) TIE(wf2[f_][s_][n_]); } while (0)
#define W2_TIE() do { _Pragma("unroll") for (int f_ = 0; f_ < 3; f_++) _Pragma("unroll") for (int s_ = 0; s_ < 2; s_++) _Pragma("unroll") for (int n_ = 0; n_ < 2; n_++) TIE(wf2[f_][s_][n_]); } while (0)
  const int th = w & 1, rh = (w >> 1) & 1;
  CvS cs_; v4u sx, t7, t9; u32 ad;
#define LT_READ() do { cs_.s01 = lds_ld<v4u>(LLT + ho); cs_.s2 = lds_ld<v4u>(LLT + 1024 + ho); sx = lds_ld<v4u>(LLT + 2048 + ho); \
    t7 = lds_ld<v4u>(LLT + 3072 + ho); t9 = lds_ld<v4u>(LLT + 4096 + ho); ad = lds_ld<u32>(LLT + 5120 + ho); } while (0)
  CvA ca_; ConvRaw c0, c1; v4u x1, x2, x4, b0, y1_, y2_; u32 pe_[5], pf_[3], pq_[2];
#define CV_ADDR() do { ca_.ua = LU + (u32)ch * LUS + 448 - 192 - 256 * rh + 512 * th + 32 * l15 + 16 * g; \
    ca_.a0 = tb + (cs_.s01.w & 0xffffu) + 256 * rh; ca_.a1 = tb + (cs_.s01.w >> 16) + 128 * rh; ca_.a2 = tb + (ad & 0xffffu) + 64 * rh; } while (0)
#define CV_PRE() do { const u32 p1_ = rh ? ca_.a2 : ca_.a0 + 512, p2_ = ca_.a1 + (rh ? 0 : 256), p3_ = tb + (ad >> 16), p4_ = tb + KQ + 128 - 12 + 64 * (th - rh) + 4 * l15 - 16 * g; \
    _Pragma("unroll") for (int i = 0; i < 5; i++) { pe_[i] = lds_ld<u32>(p1_ + 4 * i); c0.e[i] = lds_ld<u32>(ca_.a0 + 4 * i); } \
    _Pragma("unroll") for (int i = 0; i < 3; i++) pf_[i] = lds_ld<u32>(p2_ + 4 * i); pq_[0] = lds_ld<u32>(p3_); pq_[1] = lds_ld<u32>(p3_ + 4); \
    _Pragma("unroll") for (int i = 0; i < 4; i++) { y1_[i] = lds_ld<u32>(p4_ + 12 - 4 * i); y2_[i] = lds_ld<u32>(p4_ + 128 + 12 - 4 * i); } \
    conv_load<0, 3>(c0, ca_); \
    b0 = frag0(c0.e, cs_.s01.x); \
    if (rh) { x1 = frag1(pf_, cs_.s01.y, cs_.s01.z); x2 = perm4(pe_[1], pe_[0], cs_.s2); x4 = v4u{0, 0, 0, 0}; } \
    else { x1 = frag0(pe_, cs_.s01.x); x2 = frag1(pf_, cs_.s01.y, cs_.s01.z); x4 = perm4(pq_[1], pq_[0], sx); } TIE(b0); TIE(x1); TIE(x2); TIE(x4); TIE(y1_); TIE(y2_); TIE(t7); TIE(t9); } while (0)
#define U_PUT() do { lds_st<v2u>(LU + 992 + tid * 8, v2u{prm(uu.y, uu.x, 0x05040100u), prm(uu.w, uu.z, 0x05040100u)}); \
    lds_st<v2u>(LU + LUS + 992 + tid * 8, v2u{prm(uu.y, uu.x, 0x07060302u), prm(uu.w, uu.z, 0x07060302u)}); } while (0)
  v4u uu;

  if (gw) {
    MARK(GW);
    v2u fl;
    v2u st2, cq0, cq1; u32 fx, fv;
    G1_RD(0, 0); G1_RD(0, 1); SB();
    M0SET(w4k, 65536); DMA_X(2);
    G1_STEP(0, { DMA(vw0, rw, WS_(2), 2048); K_RD(0); }, DMA(vw1, rw, WS_(2), 3072));
    G1_SYNC(7); STAMP(20);
    G1_STEP(1, { M0SET(w4k, 0); DMA(vx0, rx, XS(3), 0); K_MM(0); K_RD(1); }, { DMA(vx1, rx, XS(3), 1024); SET_B(); });
    G1_STEP(2, { DMA(vw0, rw, WS_(3), 2048); K_MM(1); K_RD(2); }, { DMA(vw1, rw, WS_(3), 3072); SET_A(); });
    G1_SYNC(4); STAMP(21);
    G1_STEP(3, { FLAG_ST(ufs); M0SET(w4k, 32768); DMA(vx0, rx, XS(4), 0); K_MM(2); K_RD(3); }, DMA(vx1, rx, XS(4), 1024));
    G1_STEP(4, { DMA(vw0, rw, WS_(4), 2048); K_MM(3); K_RD(4); }, DMA(vw1, rw, WS_(4), 3072));
    G1_SYNC(5); STAMP(22);
    G1_STEP(5, { M0SET(w4k, 65536); DMA(vx0, rx, XS(5), 0); K_MM(4); K_RD(5); }, DMA(vx1, rx, XS(5), 1024));
    G1_STEP(6, { DMA(vw0, rw, WS_(5), 2048); K_MM(5); K_RD(6); }, { DMA(vw1, rw, WS_(5), 3072); SET_D(); });
    G1_SYNC(4); STAMP(23);
    G1_STEP(7, { M0SET(w4k, 0); DMA(vx0, rx, XS(6), 0); K_MM(6); K_RD(7); }, { DMA(vx1, rx, XS(6), 1024); SET_F(); });
    G1_STEP(8, { DMA(vw0, rw, WS_(6), 2048); K_MM(7); }, DMA(vw1, rw, WS_(6), 3072));
    G1_SYNC(4); STAMP(24);
    G1_STEP(9, { M0SET(w4k, 32768); DMA_X(7); SET_E(); }, DMA_W(7));
    G1_STEP(10, { GLD("global_load_dwordx2", fl, afo, ws, 0, "sc1"); LT_DMA(); }, SET_C());
    G1_SYNC(6); STAMP(25);
    G1_STEP(11, K_PUT(), { VMW(1); TIE(fl); if (tag_miss(fl, tag)) { MARK(SLOW); poll(afo, ws, tag); } M0_2(3); DMA_2I(3, 0); });
    G1_STEP(12, DMA_2I(3, 1), { GLD("global_load_dwordx2", st2, so, ws, 0, "sc1"); GLD("global_load_dwordx2", cq0, co, ws, 0, "sc1"); GLD("global_load_dwordx2", cq1, co, ws, 256, "sc1"); DMA_2I(3, 2); });
    G1_SYNC(6); STAMP(26);
    G1_STEP(13, { DMA_2I(3, 3); M0_2(4); fx = lds_ld<u32>(LLT + 5120 + lane * 16 + 4); }, DMA_2I(4, 0));
    G1_STEP(14, { DMA_2I(4, 1); fv = lds_ld<u16>(tb + (fx & 0xffffu)); }, { DMA_2I(4, 2); GLD("global_load_dwordx4", uu, uo_, ws, 0, "sc1"); });
    G1_STEP(15, { DMA_2I(4, 3); W2_LD(0, 0, 0); lds_st<u16>(tb + (fx >> 16), (u16)fv); }, { W2_LD(0, 0, 1); W2_LD(0, 1, 0); });
    W2_LD(0, 1, 1);
    VMW(11); TIE(st2); TIE(cq0); TIE(cq1);
    STAMP(4);
    __builtin_amdgcn_s_setprio(2);
    const float sd = bfhi(st2.y);
    { const u32 km = hh ? 0u : ~0u; const v4u sb = {st2.x & km, st2.y & km, 0u, 0u};
      acc[0] = mfma32(v4u{cq0.x, cq0.y, 0u, 0u}, sb, acc[0]); acc[1] = mfma32(v4u{cq1.x, cq1.y, 0u, 0u}, sb, acc[1]); }
    const float rstd = __builtin_amdgcn_rcpf(sd), k1 = -2.3022082f * rstd, k2 = -0.10294324f * rstd * rstd * rstd;
    const int M32 = rt * 4 + mw;
    const char* hb = ws + WS_H + (size_t)(M32 * 112 + ft * 8 + nh * 4) * 1024;
#pragma unroll
    for (int nl = 0; nl < 2; nl++) {
#pragma unroll
      for (int s = 0; s < 2; s++) {
        v4u hp;
#pragma unroll
        for (int e = 0; e < 4; e++) {
          const f2 v = {acc[nl][8 * s + 2 * e], acc[nl][8 * s + 2 * e + 1]};
          const f2 z = v * (v * v * k2 + k1);
          const f2 d = f2{__builtin_amdgcn_exp2f(z.x), __builtin_amdgcn_exp2f(z.y)} * sd + sd;
          const f2 o = v * f2{__builtin_amdgcn_rcpf(d.x), __builtin_amdgcn_rcpf(d.y)};
          hp[e] = pk2(o.x, o.y);
        }
        GST("global_store_dwordx4", ho, hp, hb, nl * 2048 + s * 1024, "sc1");
        if (nl == 0 && s == 0) { W2_LD(1, 0, 0); W2_LD(1, 0, 1); }
        if (nl == 0 && s == 1) { W2_LD(1, 1, 0); W2_LD(1, 1, 1); }
      }
    }
    __builtin_amdgcn_s_setprio(0);
    STAMP(5);
    VMW(13); TIE(uu); U_PUT();
    u32 hfs = lane == 0 ? WS_HF + 4096 + (u32)(M32 * 32 + ft * 2 + nh) * 128u : 0x40000000u; TIE(hfs);
    LT_READ(); CV_ADDR(); CV_PRE();
    VMW(0); W2_TIE2();
    FLAG_ST(hfs);
    STAMP(7);
    LGW0(); BAR();
  } else {
    MARK(LN);
    SET_B(); SET_C();
    u32 pb1; { const u32 o3 = (u32)(cg * 64 + lane) * 2u; GLD("global_load_ushort", pb1, o3, a.b1, 0, ""); }
    VMW(4);
    { v4u se; f4 ca = {0, 0, 0, 0};
#pragma unroll
      for (int i = 0; i < 4; i++) se[i] = (l15 >> 1) == i ? (0x3f80u << (16 * (l15 & 1))) : 0u;
#pragma unroll
      for (int i = 0; i < 8; i++) ca = mfma(se, lds_ld<v4u>(w4k + ho + (i >> 2) * 32768 + (i & 3) * 1024), ca);
      if (lane < 32) lds_st<f4>(w4k + (l15 >> 3) * 256 + (lane & 7) * 32 + g * 16, ca); }
    LGW0(); BAR();
    if (w == 0) {
      float c = 0.f;
#pragma unroll
      for (int i = 0; i < 16; i++) c += lds_ld<float>((i >> 1) * 4096 + (i & 1) * 256 + lane * 4);
      const float c0 = rbf(c), c1 = rbf(c - c0), c2 = c - c0 - c1;
      VMW(0); TIE(pb1);
      const u32 o = WS_C + (u32)(cg * 64 + lane) * 8u; const v2u v = {pk2(c0, c1), pk2(c2, bf2f(pb1))}; GST("global_store_dwordx2", o, v, ws, 0, "sc1");
    } else VMW(0);
    BAR();
    FLAG_ST(ufs);
    if (w == 0) { VMW(0); if (lane == 0) { const u32 o_ = WS_CF + (u32)cg * 128u; GST("global_store_dwordx2", o_, tag, ws, 0, "sc1"); } }
    STAMP(4);
    SET_F();
#pragma unroll
    for (int k = 0; k < 8; k++) { K_RD(k); K_MM(k); }
    BAR();
    K_PUT();
    LT_DMA();
    __builtin_amdgcn_s_sleep(24);
    poll(WS_UF + (u32)(32 * w + l31) * 128u, ws, tag);
    LGW0(); BAR();
    GLD("global_load_dwordx4", uu, uo_, ws, 0, "sc1");
    DMA_2(3); DMA_2(4);
#pragma unroll
    for (int i = 0; i < 8; i++) W2_LD(i >> 2, (i >> 1) & 1, i & 1);
    { const u32 fx = lds_ld<u32>(LLT + 5120 + lane * 16 + 4); lds_st<u16>(tb + (fx >> 16), lds_ld<u16>(tb + (fx & 0xffffu))); }
    LT_READ(); CV_ADDR();
    VMW(0); TIE(uu); W2_TIE2(); U_PUT();
    LGW0(); BAR();
    CV_PRE();
    STAMP(5);
  }
  v4u hf[14][2];
  const u32 hv0 = (u32)((2 * mt + (w >> 2)) * 112 + 14 * w) * 1024u + ho + 4096u + WS_H, hv1 = (u32)((2 * mt + 1 - (w >> 2)) * 112 + 14 * w) * 1024u + ho + 4096u + WS_H;
#define H_LD(s, ml) asm volatile("buffer_load_dwordx4 %0, %1, %2, %3 offen offset:%4 sc1" : "=v"(hf[s][ml]) : "v"((ml) ? hv1 : hv0), "s"(rs), "s"(FR((s) >> 1) << 11), "n"(((s) & 1) * 1024) : "memory")
  u32 yfo;
  {
    f4 ca[4];
    v4u za, zb;
    { const u32 p = LU + (u32)ch * LUS + 992 - 48 + 512 * rh + 32 * l15 + 16 * g;
      const v4u u0 = lds_ld<v4u>(p + 16), u1 = lds_ld<v4u>(p), u2 = lds_ld<v4u>(p + 1024 + 16), u3 = lds_ld<v4u>(p + 1024);
      conv_load<0, 0>(c0, ca_); conv_load<0, 1>(c0, ca_); conv_load<0, 2>(c0, ca_); SB();
      W2_LD(2, 0, 0); W2_LD(2, 0, 1);
      const f4 z0 = mfma(u0, t7, f4{0, 0, 0, 0}), z1 = mfma(u1, t9, f4{0, 0, 0, 0}), z2 = mfma(u2, t7, f4{0, 0, 0, 0}), z3 = mfma(u3, t9, f4{0, 0, 0, 0});
      SB();
      STAMP(8);
      CV_ROUND(0, c0, c1, { W2_LD(2, 1, 0); W2_LD(2, 1, 1); }, { M0_2(5); DMA_2I(5, 0); _Pragma("unroll") for (int i = 0; i < 4; i++) { za[i] = pk2(z0[i], z1[i]); zb[i] = pk2(z2[i], z3[i]); } TIE(za); TIE(zb); },
               DMA_2I(5, 1), DMA_2I(5, 2)); }
    STAMP(6);
    v2u fl; const u32 hpo = WS_HF + (u32)((2 * mt + ((lane >> 2) & 1)) * 32 + ((7 * w) >> 1) + (lane & 3)) * 128u;
    CV_ROUND(1, c1, c0, { DMA_2I(5, 3); M0_2(6); }, DMA_2I(6, 0), DMA_2I(6, 1), DMA_2I(6, 2));
    CV_ROUND(2, c0, c1, DMA_2I(6, 3), {}, GLD("global_load_dwordx2", fl, hpo, ws, 0, "sc1"), {});
    STAMP(28);
    CV_ROUND(3, c1, c0, {}, { VMW(0); W2_TIE(); TIE(fl); if (tag_miss(fl, tag)) { MARK(SLOW); poll(hpo, ws, tag); } STAMP(11); H_LD(0, 0); H_LD(0, 1); }, { H_LD(1, 0); H_LD(1, 1); }, { H_LD(2, 0); H_LD(2, 1); });
    ca[2] = mfma(za, y1_, ca[2]); ca[3] = mfma(za, y2_, ca[3]); ca[3] = mfma(zb, y1_, ca[3]);
    STAMP(9);
    f4 k0, k1;
    const u32 ew = (w < 6 ? LEX + w * 2048 : LEX2 + (w - 6) * 2048) + ho, er = ((w ^ 2) < 6 ? LEX + (w ^ 2) * 2048 : LEX2 + ((w ^ 2) - 6) * 2048) + ho;
    if (rh) { lds_st<f4>(ew, ca[0]); lds_st<f4>(ew + 1024, ca[1]); k0 = ca[2]; k1 = ca[3]; }
    else { lds_st<f4>(ew, ca[2]); lds_st<f4>(ew + 1024, ca[3]); k0 = ca[0]; k1 = ca[1]; }
    LGW0(); BAR();
    k0 += lds_ld<f4>(er); k1 += lds_ld<f4>(er + 1024);
    const int i0 = 4 * rh + th;
    const u32 o = WS_Y + (u32)dch * YS + i0 * 512 + 32 * l15 + 8 * g;
    { v2u v = {pk2(k0[0], k0[1]), pk2(k0[2], k0[3])}; GST("global_store_dwordx2", o, v, ws, 0, "sc1"); }
    { v2u v = {pk2(k1[0], k1[1]), pk2(k1[2], k1[3])}; GST("global_store_dwordx2", o, v, ws, 1024, "sc1"); }
    yfo = lane == 0 ? WS_YF + 4096 + (u32)((w & 3) * 512 + dch) * 128u : 0x40000000u;
    STAMP(10);
    H_LD(3, 0); H_LD(3, 1);
  }
  {
    f16v c2[2][2];
    v4u wn[2][2], xr, b2v, yf; v2u fl;
    const u32 ypo = WS_YF + (u32)((((mt >> 3) & 2) + ((mt >> 2) & 1)) * 512 + nt * 64 + (w & 1) * 32 + l31) * 128u;
    const u32 oo = (u32)(mt * 64 + (tid >> 3)) * 1024u + nt * 128 + (tid & 7) * 16;
#define W2OP(s, nl) ((s) < 6 ? wf2[(s) < 6 ? (s) >> 1 : 0][(s) & 1][nl] : wn[(s) & 1][nl])
#define G2_STEP(s, NV, X) do { VMW(NV); TIE(hf[s][0]); TIE(hf[s][1]); \
      c2[0][0] = mfma32(W2OP(s, 0), hf[s][0], (s) == 0 ? f16v{0} : c2[0][0]); SB(); if ((s) + 4 < 14) H_LD((s) + 4, 0); SB(); \
      c2[1][0] = mfma32(W2OP(s, 1), hf[s][0], (s) == 0 ? f16v{0} : c2[1][0]); \
      if ((s) >= 5 && (s) < 13) { wn[((s) + 1) & 1][0] = W2FRAG(((s) + 1) >> 1, ((s) + 1) & 1, 0); wn[((s) + 1) & 1][1] = W2FRAG(((s) + 1) >> 1, ((s) + 1) & 1, 1); } SG(0x8, 1); SB(); \
      c2[0][1] = mfma32(W2OP(s, 0), hf[s][1], (s) == 0 ? f16v{0} : c2[0][1]); SB(); if ((s) + 4 < 14) H_LD((s) + 4, 1); SB(); \
      c2[1][1] = mfma32(W2OP(s, 1), hf[s][1], (s) == 0 ? f16v{0} : c2[1][1]); SB(); X; SB(); } while (0)
    if (w >= 4) __builtin_amdgcn_s_setprio(1);
    G2_STEP(0, 8, {});
    G2_STEP(1, 8, {});
    G2_STEP(2, 8, { VMW(8); FLAG_ST(yfo); });
    __builtin_amdgcn_s_setprio(0);
    STAMP(29);
    G2_STEP(3, 7, {});
    G2_STEP(4, 7, {});
    G2_STEP(5, 7, {});
    G2_STEP(6, 7, {});
    STAMP(30);
    G2_STEP(7, 6, {});
    G2_STEP(8, 6, {});
    G2_STEP(9, 6, { GLD("global_load_dwordx4", xr, oo, a.x, 0, ""); const u32 o2 = nt * 128 + (tid & 7) * 16; GLD("global_load_dwordx4", b2v, o2, a.b2, 0, ""); GLD("global_load_dwordx2", fl, ypo, ws, 0, "sc1"); });
    STAMP(31);
    G2_STEP(10, 9, {});
    G2_STEP(11, 7, {});
    G2_STEP(12, 5, {});
    G2_STEP(13, 3, {});
    STAMP(32);
    VMW(0); TIE(fl); if (tag_miss(fl, tag)) { MARK(SLOW); poll(ypo, ws, tag); }
    { const u32 o = WS_Y + (u32)(nt * 64 + (w & 1) * 32 + l31) * YS + (u32)(mt * 64 + (w >> 1) * 16 + hh * 8) * 2u; GLD("global_load_dwordx4", yf, o, ws, 0, "sc1"); }
    STAMP(12);
    v4u id;
    { const int e = l31 - 16 * ((w >> 1) & 1) - 8 * hh;
#pragma unroll
      for (int i = 0; i < 4; i++) id[i] = (e >> 1) == i ? (0x3f80u << (16 * (e & 1))) : 0u; }
#define P_PUT(nl, ml) do { const int row_ = 32 * ((ml) ^ (w >> 2)) + l31; _Pragma("unroll") for (int qd = 0; qd < 4; qd++) \
      lds_st<f4>((row_ >> 4) * 32768 + w4k + (row_ & 15) * 256 + 16 * ((8 * ((nl) ^ (w & 1)) + 2 * qd + hh) ^ (row_ & 7)), \
                 f4{c2[nl][ml][4 * qd], c2[nl][ml][4 * qd + 1], c2[nl][ml][4 * qd + 2], c2[nl][ml][4 * qd + 3]}); } while (0)
    P_PUT(1, 0); P_PUT(0, 1); P_PUT(1, 1); SB();
    VMW(0); TIE(yf); TIE(xr); TIE(b2v);
    c2[0][0] = mfma32(yf, id, c2[0][0]);
    P_PUT(0, 0);
    STAMP(13);
    LGW0(); BAR();
    const int row = tid >> 3, c8 = tid & 7;
    const u32 ra = (row >> 4) * 32768 + (row & 15) * 256 + 16 * ((2 * c8) ^ (row & 7)), rb = ra ^ 16u;
    f4 s0 = lds_ld<f4>(ra), s1 = lds_ld<f4>(rb);
#pragma unroll
    for (int ww = 1; ww < 8; ww++) { s0 += lds_ld<f4>(ra + ww * 4096); s1 += lds_ld<f4>(rb + ww * 4096); }
    v4u o;
    o.x = pk2(s0[0] + bf2f(b2v.x) + bf2f(xr.x), s0[1] + bfhi(b2v.x) + bfhi(xr.x));
    o.y = pk2(s0[2] + bf2f(b2v.y) + bf2f(xr.y), s0[3] + bfhi(b2v.y) + bfhi(xr.y));
    o.z = pk2(s1[0] + bf2f(b2v.z) + bf2f(xr.z), s1[1] + bfhi(b2v.z) + bfhi(xr.z));
    o.w = pk2(s1[2] + bf2f(b2v.w) + bf2f(xr.w), s1[3] + bfhi(b2v.w) + bfhi(xr.w));
    GST("global_store_dwordx4", oo, o, a.out, 0, "");
  }
  STAMP(14);
  if (bid == 255) { poll(WS_UF + (u32)(tid & 255) * 128u, ws, tag); BAR(); if (tid == 0) { u32 z = 0; GST("global_store_dwordx2", z, tag, ws, 0, "sc1"); } }
  asm volatile(".Lfk_end:");
}

extern "C" void kernel_launch(void* const* d_in, const int* in_sizes, int n_in, void* d_out, int out_size, void* d_ws, size_t ws_size, hipStream_t stream) {
  Args a;
  a.x = (const u16*)d_in[0]; a.are = (const u16*)d_in[1]; a.aim = (const u16*)d_in[2]; a.bre = (const u16*)d_in[3]; a.bim = (const u16*)d_in[4];
  a.cre = (const u16*)d_in[5]; a.cim = (const u16*)d_in[6]; a.ldt = (const u16*)d_in[7]; a.dsk = (const u16*)d_in[8];
  a.w1 = (const u16*)d_in[10]; a.b1 = (const u16*)d_in[11]; a.w2 = (const u16*)d_in[12]; a.b2 = (const u16*)d_in[13];
  a.out = (u16*)d_out; a.ws = (char*)d_ws; a.pad = a.x;
  (void)hipFuncSetAttribute((const void*)fused_kernel, hipFuncAttributeMaxDynamicSharedMemorySize, LDS_BYTES);
  fused_kernel<<<256, 512, LDS_BYTES, stream>>>(a);
}
```

```cpp
#include <hip/hip_runtime.h>
#include <stdint.h>

typedef unsigned u32; typedef unsigned long long u64; typedef unsigned short u16;
typedef u32 v4u __attribute__((ext_vector_type(4)));
typedef u32 v2u __attribute__((ext_vector_type(2)));
typedef float f4 __attribute__((ext_vector_type(4)));
typedef float f2 __attribute__((ext_vector_type(2)));
typedef float f16v __attribute__((ext_vector_type(16)));
typedef short s4 __attribute__((ext_vector_type(4)));
typedef __bf16 bf8 __attribute__((ext_vector_type(8)));
typedef __bf16 bf2 __attribute__((ext_vector_type(2)));
#define LAS __attribute__((address_space(3)))
#define DI __device__ __forceinline__

constexpr u32 WS_CF = 4096, WS_C = 32768;
constexpr u32 WS_UF = 65536, WS_HF = 262144, WS_YF = 524288;
constexpr u32 WS_U = 1u << 20, US = 8192 + 128, YS = 4096 + 128, WS_Y = WS_U + 256 * US, WS_W = WS_Y + 512 * YS, WS_H = WS_W + (2u << 20), WS_END = WS_H + 64 * 112 * 1024;
constexpr u32 SP = 131072, KA = 0, KB = 640, KC = 960, KQ = 1232, KD = 1616, TBS = 1664;
constexpr u32 LXN = 65536, LSS = 98304, LTS = SP + 2 * TBS + 1024, LT1 = LTS + 16384 + 1024, LWR = SP + 30720;
constexpr u32 LU = LTS - 992, LUS = LT1 - LTS, LLT = LTS + 4096, LEX = LLT, LEX2 = LT1 + 4096;
constexpr u32 LDS_BYTES = 163840;
constexpr int HWIN = 5;
static_assert(LT1 + 8192 <= LWR && LTS % 64 == 0 && LT1 % 64 == 0 && LEX + 6 * 2048 <= LT1 - 992 && LEX2 + 4096 <= LDS_BYTES, "LDS map");

constexpr int rne(int j, int sh) { int q = j >> sh, rem = j & ((1 << sh) - 1), h = 1 << (sh - 1); if (rem > h || (rem == h && (q & 1))) q++; return q << sh; }
constexpr int rnd_bf(int j) { return j <= 256 ? j : rne(j, j < 512 ? 1 : j < 1024 ? 2 : 3); }
constexpr int i1(int j) { return (rne(j, 1) - 256) / 2; }
constexpr int ic(int j) { return (rnd_bf(j) - 512) / 4; }
constexpr u32 sel1(int i) { return i < 0 ? 0x0c0cu : (u32)(2 * i) | (u32)(2 * i + 1) << 8; }
constexpr u32 sel(int lo, int hi) { return sel1(lo) | sel1(hi) << 16; }
constexpr int a3(int m) { return m ? KQ + 128 + 2 * (m - 1) : KC + 256; }
struct alignas(128) Tabs { u32 wr[512]; u32 lt[6 * 256]; };
constexpr Tabs make_tabs() {
  Tabs t{};
  for (int tl = 0; tl < 4; tl++) for (int l = 0; l < 64; l++) {
    const int sg = (l & 15) + 16 * (tl & 1), g = l >> 4; int ad[4] = {};
    for (int r = 0; r < 4; r++)
      ad[r] = !(tl >> 1) ? (g < 2 ? KA + 2 * (16 + 8 * sg + 4 * g + r) : g == 2 ? KB + 2 * (4 * sg + r) : KC + 2 * (2 * sg + (r & 1) + 64 * (r >> 1)))
                         : (g == 0 ? a3(sg + 32 * r) : (g == 1 && r == 0 && sg == 0) ? a3(128) : (int)(LT1 + 8192 - SP - TBS) + 4 * l);
    t.wr[(tl * 64 + l) * 2] = (u32)ad[0] | (u32)ad[1] << 16; t.wr[(tl * 64 + l) * 2 + 1] = (u32)ad[2] | (u32)ad[3] << 16;
  }
  for (int l = 0; l < 64; l++) {
    const int o = 16 + (l & 15) - 8 * (l >> 4);
    const int a0 = 16 + o - 7, j1 = 32 * 9 + o, j2 = 32 * 17 + o, jx = 32 * 32 + o;
    const int b1 = i1(j1 - 7) & ~1, b2 = ic(j2 - 7) & ~1, bx = jx - 7 <= 1028 ? ic(jx - 7) & ~1 : 0;
    t.lt[l * 4 + 0] = sel((a0 & 1) + 1, a0 & 1);
    t.lt[l * 4 + 1] = sel(i1(j1 - 4) - b1, i1(j1 - 5) - b1);
    t.lt[l * 4 + 2] = sel(i1(j1 - 6) - b1, i1(j1 - 7) - b1);
    t.lt[l * 4 + 3] = (u32)(KA + 2 * (a0 & ~1)) | (u32)(KB + 2 * b1 - 32) << 16;
    for (int i = 0; i < 4; i++) {
      t.lt[256 + l * 4 + i] = sel(ic(j2 - 2 * i) - b2, ic(j2 - 2 * i - 1) - b2);
      t.lt[512 + l * 4 + i] = sel(jx - 2 * i <= 1028 ? ic(jx - 2 * i) - bx : -1, jx - 2 * i - 1 <= 1028 ? ic(jx - 2 * i - 1) - bx : -1);
      const int p = o - 2 * i, q = o + 8 - 2 * i;
      t.lt[768 + l * 4 + i] = (p >= 5 && p <= 11 ? 0x3f80u : 0u) | (p - 1 >= 5 && p - 1 <= 11 ? 0x3f800000u : 0u);
      t.lt[1024 + l * 4 + i] = (q >= 12 && q <= 20 ? 0x3f80u : 0u) | (q - 1 >= 12 && q - 1 <= 20 ? 0x3f800000u : 0u);
    }
    t.lt[1280 + l * 4 + 0] = (u32)(KC + 2 * b2 - 16) | (u32)(KC + 2 * bx) << 16;
    t.lt[1280 + l * 4 + 1] = l < 32 ? (u32)(KB + 2 * i1(256 + l)) | (u32)(KA + 2 * (16 + 256 + l)) << 16
                           : l < 49 ? (u32)(KC + 2 * ic(512 + 2 * (l - 32))) | (u32)(KB + 2 * (128 + l - 32)) << 16 : (u32)KD | (u32)KD << 16;
  }
  return t;
}
__device__ const Tabs g_tabs = make_tabs();

struct Args { const u16 *are, *aim, *bre, *bim, *cre, *cim, *x, *w1, *w2; char* ws; const u16 *ldt, *dsk, *b1, *b2; u16* out; const u16* pad; };

DI float bf2f(u32 v) { return __uint_as_float(v << 16); }
DI float bfhi(u32 v) { return __uint_as_float(v & 0xffff0000u); }
DI u32 pk2(float a, float b) { bf2 c; c[0] = (__bf16)a; c[1] = (__bf16)b; return __builtin_bit_cast(u32, c); }
DI float rbf(float a) { return bf2f(pk2(a, a) & 0xffffu); }
DI f4 mfma(v4u a, v4u b, f4 c) { return __builtin_amdgcn_mfma_f32_16x16x32_bf16(__builtin_bit_cast(bf8, a), __builtin_bit_cast(bf8, b), c, 0, 0, 0); }
DI f16v mfma32(v4u a, v4u b, f16v c) { return __builtin_amdgcn_mfma_f32_32x32x16_bf16(__builtin_bit_cast(bf8, a), __builtin_bit_cast(bf8, b), c, 0, 0, 0); }
DI float dot2(u32 a, u32 b, float c) { return __builtin_amdgcn_fdot2_f32_bf16(__builtin_bit_cast(bf2, a), __builtin_bit_cast(bf2, b), c, false); }
template <class T> DI T lds_ld(u32 a) { return *(const LAS T*)(size_t)a; }
template <class T> DI void lds_st(u32 a, T v) { *(LAS T*)(size_t)a = v; }
DI v2u lds_tr(u32 a) { return __builtin_bit_cast(v2u, __builtin_amdgcn_ds_read_tr16_b64_v4i16((LAS s4*)(size_t)a)); }
DI v4u lds_tr2(u32 a, u32 b) { v2u x = lds_tr(a), y = lds_tr(b); return v4u{x.x, x.y, y.x, y.y}; }
#define DPP_ADD(v, ctrl) v += __uint_as_float(__builtin_amdgcn_update_dpp(0u, __float_as_uint(v), ctrl, 0xf, 0xf, true))
DI float sum32(float v) {
  DPP_ADD(v, 0xB1); DPP_ADD(v, 0x4E); DPP_ADD(v, 0x141); DPP_ADD(v, 0x140);
  auto r = __builtin_amdgcn_permlane16_swap(__float_as_uint(v), __float_as_uint(v), false, false); return __uint_as_float(r[0]) + __uint_as_float(r[1]);
}
DI void cmul2(f2& r0, f2 a0, f2 w0, f2& r1, f2 a1, f2 w1) {
  asm("v_pk_mul_f32 %0, %2, %3 op_sel:[0,0] op_sel_hi:[1,0]\n\tv_pk_mul_f32 %1, %4, %5 op_sel:[0,0] op_sel_hi:[1,0]\n\t"
      "v_pk_fma_f32 %0, %2, %3, %0 op_sel:[1,1,0] op_sel_hi:[0,1,1] neg_lo:[1,0,0]\n\tv_pk_fma_f32 %1, %4, %5, %1 op_sel:[1,1,0] op_sel_hi:[0,1,1] neg_lo:[1,0,0]"
      : "=&v"(r0), "=&v"(r1) : "v"(a0), "v"(w0), "v"(a1), "v"(w1));
}
DI f2 cexp2(f2 x) {
  float e = __builtin_amdgcn_exp2f(x.x), c = __builtin_amdgcn_cosf(x.y), s = __builtin_amdgcn_sinf(x.y);
  return f2{c, s} * e;
}
DI void ln_stats(float s, float q, float& mb, float& sd) {
  const float mean = s * (1.f / 512.f);
  const float var = fmaxf(q * (1.f / 512.f) - mean * mean, 0.f);
  sd = rbf(__builtin_amdgcn_sqrtf(rbf(rbf(var) + 1e-5f))); mb = rbf(mean);
}
#define VMW(n) asm volatile("s_waitcnt vmcnt(%0)" ::"n"(n) : "memory")
#define LGW0() asm volatile("s_waitcnt lgkmcnt(0)" ::: "memory")
#define BAR() asm volatile("s_barrier" ::: "memory")
#define TIE(x) asm volatile("" : "+v"(x))
#define MARK(x) asm volatile("; PATH_" #x)
#define SB() __builtin_amdgcn_sched_barrier(0)
#define SG(m, n) __builtin_amdgcn_sched_group_barrier(m, n, 0)
#define GLD(op, dst, voff, sbase, imm, fl) asm volatile(op " %0, %1, %2 offset:%3 " fl : "=v"(dst) : "v"(voff), "s"(sbase), "n"(imm) : "memory")
#define GST(op, voff, src, sbase, imm, fl) asm volatile(op " %0, %1, %2 offset:%3 " fl "\n\ts_nop 1" ::"v"(voff), "v"(src), "s"(sbase), "n"(imm) : "memory")
#define M0SET(lbase, limm) asm volatile("s_add_u32 m0, %0, %1\n\ts_nop 0" ::"s"(lbase), "n"(limm) : "memory", "scc")
#define DMA(voff, srd, soff, off) asm volatile("buffer_load_dwordx4 %0, %1, %2 offen offset:%3 lds" ::"v"(voff), "s"(srd), "s"(soff), "n"(off) : "memory")
DI v4u make_srd(const void* p, u32 recs) { const u64 a_ = (u64)p - 4096; return v4u{(u32)a_, (u32)(a_ >> 32), recs, 0x20000u}; }
#define STIE(x) asm volatile("" : "+s"(x))
DI bool tag_miss(v2u f, v2u tag) { return __builtin_expect(__builtin_amdgcn_ballot_w64(f.x != tag.x || f.y != tag.y) != 0, 0); }
#define ALD(f, voff, srd) do { f = v2u{0, 0}; asm volatile("buffer_atomic_or_x2 %0, %1, %2, 0 offen sc0 sc1" : "+v"(f) : "v"(voff), "s"(srd) : "memory"); } while (0)
DI bool tag_miss_m(v2u f, v2u tag, u64 m) { return __builtin_expect((__builtin_amdgcn_ballot_w64(f.x != tag.x || f.y != tag.y) & m) != 0, 0); }
DI void apoll(u32 voff, v4u srd, v2u tag, u64 m) {
#pragma nounroll
  for (int it = 0; it < (1 << 16); it++) {
    v2u f; ALD(f, voff, srd); VMW(0); TIE(f);
    if (!tag_miss_m(f, tag, m)) break;
    __builtin_amdgcn_s_sleep(2);
  }
}
DI void poll(u32 voff, const char* ws, v2u tag) {
#pragma nounroll
  for (int it = 0; it < (1 << 16); it++) {
    v2u f;
    asm volatile("global_load_dwordx2 %0, %1, %2 sc1\n\ts_waitcnt vmcnt(0)" : "=v"(f) : "v"(voff), "s"(ws) : "memory");
    if (__builtin_amdgcn_ballot_w64(f.x != tag.x || f.y != tag.y) == 0) break;
    __builtin_amdgcn_s_sleep(6);
  }
}
struct CvA { u32 ua, a0, a1, a2; };
struct CvS { v4u s01, s2; };
struct ConvRaw { v4u A[8]; u32 e[5], f[3], q[2][2]; };
DI u32 prm(u32 hi, u32 lo, u32 s) { return __builtin_amdgcn_perm(hi, lo, s); }
DI v4u perm4(u32 hi, u32 lo, v4u s) { return v4u{prm(hi, lo, s.x), prm(hi, lo, s.y), prm(hi, lo, s.z), prm(hi, lo, s.w)}; }
DI v4u frag0(const u32 (&e)[5], u32 s) { return v4u{prm(e[4], e[3], s), prm(e[3], e[2], s), prm(e[2], e[1], s), prm(e[1], e[0], s)}; }
DI v4u frag1(const u32 (&f)[3], u32 sa, u32 sb) { return v4u{prm(f[2], f[1], sa), prm(f[2], f[1], sb), prm(f[1], f[0], sa), prm(f[1], f[0], sb)}; }
template <int RR, int P> DI void conv_load(ConvRaw& c, const CvA& a) {
  if (P == 0) {
#pragma unroll
    for (int i = 0; i < 5; i++) if (RR) c.e[i] = lds_ld<u32>(a.a0 + 64 * RR + 4 * i);
    c.A[7] = lds_ld<v4u>(a.ua + 64 * (3 - RR) + 512 * 7); c.A[5] = lds_ld<v4u>(a.ua + 64 * (3 - RR) + 512 * 5);
  }
  if (P == 1) {
    c.A[3] = lds_ld<v4u>(a.ua + 64 * (3 - RR) + 512 * 3); c.A[1] = lds_ld<v4u>(a.ua + 64 * (3 - RR) + 512);
    if (RR) {
#pragma unroll
      for (int i = 0; i < 3; i++) c.f[i] = lds_ld<u32>(a.a1 + 32 * RR + 4 * i);
    }
  }
  if (P == 2) {
#pragma unroll
    for (int i = 6; i >= 0; i -= 2) c.A[i] = lds_ld<v4u>(a.ua + 64 * (3 - RR) + 512 * i);
  }
  if (P == 3) {
#pragma unroll
    for (int m = 2; m < 4; m++) if (RR || m != 2) {
      const u32 p = a.a2 + 16 * RR + 128 * (m - 2);
      c.q[m - 2][0] = lds_ld<u32>(p); c.q[m - 2][1] = lds_ld<u32>(p + 4);
    }
  }
}
template <int RR, int M> DI v4u conv_b(const ConvRaw& c, const CvS& s, v4u x1, v4u x2) {
  return (RR == 0 && M == 1) ? x1 : (RR == 0 && M == 2) ? x2 : M == 1 ? frag1(c.f, s.s01.y, s.s01.z) : perm4(c.q[M < 2 ? 0 : M - 2][1], c.q[M < 2 ? 0 : M - 2][0], s.s2);
}
template <int M, bool Z> DI void conv_mm(f4 (&acc)[4], const ConvRaw& c, v4u B) {
#pragma unroll
  for (int t = 3; t >= 0; t--) if (2 * t + 1 >= M) acc[t] = mfma(B, c.A[2 * t + 1 - M], Z ? f4{0, 0, 0, 0} : acc[t]);
}
#define CV_SG(n) do { _Pragma("unroll") for (int i_ = 0; i_ < (n); i_++) { SG(0x8, 1); SG(0x100, 2); SG(0x2, 2); } SB(); } while (0)
#define CV_ROUND(RR, c, n, X0, X1, X2, X3) do { \
    v4u b1_ = conv_b<RR, 1>(c, cs_, x1, x2); if ((RR) < 3) conv_load<((RR) + 1) & 3, 0>(n, ca_); conv_mm<0, (RR) == 0>(ca, c, b0); CV_SG(4); TIE(b1_); X0; SB(); \
    v4u b2_ = conv_b<RR, 2>(c, cs_, x1, x2); if ((RR) < 3) conv_load<((RR) + 1) & 3, 1>(n, ca_); conv_mm<1, false>(ca, c, b1_); CV_SG(4); TIE(b2_); X1; SB(); \
    v4u b3_ = conv_b<RR, 3>(c, cs_, x1, x2); if ((RR) < 3) conv_load<((RR) + 1) & 3, 2>(n, ca_); conv_mm<2, false>(ca, c, b2_); CV_SG(3); TIE(b3_); X2; SB(); \
    if ((RR) < 3) { conv_load<((RR) + 1) & 3, 3>(n, ca_); b0 = frag0(n.e, cs_.s01.x); } \
    conv_mm<3, false>(ca, c, b3_); if ((RR) == 0) conv_mm<4, false>(ca, c, x4); CV_SG((RR) == 0 ? 5 : 3); TIE(b0); X3; SB(); } while (0)

typedef u32 v16u __attribute__((ext_vector_type(16)));
#define KP(v, i) ((const char*)(((u64)v[2 * (i) + 1] << 32) | v[2 * (i)]))
__global__ __launch_bounds__(512, 2) void fused_kernel(Args) {
  v16u k0_, k1_;
  asm volatile("s_load_dwordx16 %0, %2, 0x0\n\ts_load_dwordx16 %1, %2, 0x40" : "=&s"(k0_), "=&s"(k1_) : "s"(__builtin_amdgcn_kernarg_segment_ptr()) : "memory");
  const int tid = threadIdx.x, lane = tid & 63, w = __builtin_amdgcn_readfirstlane(tid >> 6), l15 = lane & 15, g = lane >> 4, l31 = lane & 31, hh = lane >> 5;
  const int bid = blockIdx.x, xcd = bid & 7, idx = bid >> 3;
  const int oct = xcd * 32 + idx;
  const int ch = w >> 2, dch = 2 * oct + ch;
  const bool tw = ((w >> 1) ^ ch) & 1;
  const int lr = 4 * ch + 2 * (w & 1) + hh;
  const int md = (w & 1) * 64 + lane;
  u32 xo = (u32)(oct * 8 + lr) * 1024u + (u32)l31 * 16u, po = (u32)(dch * 128 + md) * 2u;
  TIE(xo); TIE(po);
  if (w < 2) { const u32 o = (u32)tid * 16u, d = LWR + w * 1024;
    asm volatile("s_mov_b32 m0, %2\n\ts_nop 0\n\tglobal_load_lds_dwordx4 %0, %1" ::"v"(o), "s"(g_tabs.wr), "s"(d) : "memory");
  } else if (w < 4) {
    u64 pc; u32 len; asm volatile("s_getpc_b64 %0\n.Lfk_beg:\n\ts_mov_b32 %1, .Lfk_end-.Lfk_beg" : "=s"(pc), "=s"(len));
    const u32 ln = (idx & 15) * 2 + w - 2, o = min((ln * 6 + lane) * 128u, len), o2 = min(ln * 2 + (lane & 1), 47u) * 128u;
    if (lane < 6) asm volatile("s_mov_b32 m0, %4\n\ts_nop 0\n\tglobal_load_lds_dword %0, %1\n\tglobal_load_lds_dword %2, %3" ::"v"(o), "s"(pc), "v"(o2), "s"(g_tabs.lt), "n"(SP + KD) : "memory");
  }
  const bool gw = __builtin_expect(idx < 28, 1);
  const int j4 = (idx * 37) >> 8, i7 = idx - 7 * j4;
  const int rt = (xcd >> 1) * 4 + j4, ft = (xcd & 1) * 7 + i7, rot = ((i7 ^ j4) & 1) * 4;
  const int mt = xcd * 4 + (idx >> 3), nt = idx & 7;
  const u32 w4k = (u32)w * 4096u;
  const int cg = (xcd & 1) * 14 + min((xcd >> 1) * 4 + (idx - 28), 13);
  u32 vx0, vx1, vw0, vw1, xs0, xs1, ws0, ws1;
  if (gw) {
    vx0 = (u32)(rt * 128 + w * 16 + (lane >> 3)) * 1024u + 16u * ((lane & 7) ^ (lane >> 4)) + 4096u; vx1 = (vx0 ^ 64u) + 8192u - 1024u;
    vw0 = (u32)(8 * w + g) * 3584u + 256u * ft + 16u * (l15 ^ (4 * g)) + 4096u - 2048u; vw1 = vw0 + 4 * 3584 - 1024;
  } else { vx0 = (u32)(64 * w + (lane >> 3)) * 3584u + 128u * cg + 16u * (lane & 7) + 4096u; vx1 = vx0 + 8 * 3584 - 1024; vw0 = vx0 + 16 * 3584 - 2048; vw1 = vx0 + 24 * 3584 - 3072; }
  xs0 = gw ? rot * 128 : 0; xs1 = gw ? (rot ^ 1) * 128 : 32 * 3584; ws0 = gw ? rot * (64 * 3584) : 0; ws1 = gw ? (rot ^ 1) * (64 * 3584) : 32 * 3584;
  const int fq = oct * 7 + w;
  u32 wo = (u32)((fq >> 4) * 16 + (lane >> 2)) * 1024u + (fq & 15) * 64u + (lane & 3) * 16u + (w < 7 ? 4096u - 2048u : 0x40000000u);
  TIE(vx0); TIE(vx1); TIE(vw0); TIE(vw1); TIE(wo);
  asm volatile("s_waitcnt lgkmcnt(0)" : "+s"(k0_), "+s"(k1_)::"memory");
  struct { const char *are, *aim, *bre, *bim, *cre, *cim, *x, *w1, *w2, *ws, *ldt, *dsk, *b1, *b2, *out; } a =
    {KP(k0_, 0), KP(k0_, 1), KP(k0_, 2), KP(k0_, 3), KP(k0_, 4), KP(k0_, 5), KP(k0_, 6), KP(k0_, 7), KP(k1_, 0), KP(k1_, 1), KP(k1_, 2), KP(k1_, 3), KP(k1_, 4), KP(k1_, 5), KP(k1_, 6)};
  const char* ws = a.ws;
  u32 pa, pb, pc_, pd, pe, pg; v4u xr0, xr1;
  GLD("global_load_short_d16_hi", pa, po, a.are, 0, ""); GLD("global_load_short_d16_hi", pb, po, a.aim, 0, "");
  u64 ep; u32 sl, sk_;
  { const u32 b4 = oct * 4;
    asm volatile("s_load_dwordx2 %0, %3, 0x0\n\ts_load_dword %1, %4, %6\n\ts_load_dword %2, %5, %6" : "=&s"(ep), "=&s"(sl), "=&s"(sk_) : "s"(ws), "s"(a.ldt), "s"(a.dsk), "s"(b4) : "memory"); }
  if (tw) { MARK(KT); GLD("global_load_dwordx4", xr0, xo, a.x, 0, ""); GLD("global_load_dwordx4", xr1, xo, a.x, 512, ""); }
  else { MARK(KS); GLD("global_load_short_d16_hi", pc_, po, a.bre, 0, ""); GLD("global_load_short_d16_hi", pd, po, a.bim, 0, ""); GLD("global_load_short_d16_hi", pe, po, a.cre, 0, ""); GLD("global_load_short_d16_hi", pg, po, a.cim, 0, ""); }
  SB();
  const v4u rw = make_srd(a.w1, 512 * 3584 + 4096), rx = make_srd(gw ? a.x : a.w1, gw ? 2048 * 1024 + 4096 : 512 * 3584 + 4096), r2 = make_srd(a.w2, 1792 * 1024 + 4096), rs = make_srd(ws, WS_END + 4096);
#define XS(c) (u32)(((c) ^ rot) * 128)
#define WS_(c) (u32)(((c) ^ rot) * (64 * 3584))
#define DMA_X(c) do { DMA(vx0, rx, XS(c), 0); DMA(vx1, rx, XS(c), 1024); } while (0)
#define DX(c, i) DMA((i) ? vx1 : vx0, rx, XS(c), (i) * 1024)
#define DW(c, i) DMA((i) ? vw1 : vw0, rw, WS_(c), 2048 + (i) * 1024)
#define DMA_W(c) do { DMA(vw0, rw, WS_(c), 2048); DMA(vw1, rw, WS_(c), 3072); } while (0)
  BAR();
  M0SET(w4k, 0); DMA(vx0, rx, xs0, 0); DMA(vx1, rx, xs0, 1024); DMA(vw0, rw, ws0, 2048); DMA(vw1, rw, ws0, 3072);
  const int mw = w & 3, nh = w >> 2, q4 = l15 >> 2;
  u32 xa[2][4], wa[2][2], ksa, kta;
  { const int row = 32 * mw + l31;
#pragma unroll
    for (int r = 0; r < 4; r++) { xa[0][r] = (row >> 4) * 4096 + (row & 15) * 128 + 16 * ((2 * r + hh) ^ ((row >> 1) & 7)); TIE(xa[0][r]); }
#pragma unroll
    for (int nl = 0; nl < 2; nl++) { wa[0][nl] = hh * 4096 + 2048 + q4 * 256 + 64 * ((2 * nh + nl) ^ q4) + 32 * (g & 1) + 8 * (lane & 3); TIE(wa[0][nl]); }
    const u32 o = (2 * g + (q4 >> 1)) * 64 + 8 * (lane & 1), o1 = o + 16 * (((lane >> 1) & 3) ^ g);
    ksa = LSS + ch * 16384 + (w & 1) * 8192 + o1;
    kta = (w & 2) ? LT1 + o + 16 * ((2 * ch + (q4 & 1)) ^ g) : LTS + ch * 8192 + o1;
    TIE(ksa); TIE(kta); }
  u32 wra = 65536 + 2048 + w4k + (4 * hh + q4) * 64 + 32 * (g & 1) + 8 * (lane & 3); TIE(wra);
  u32 wso = WS_W + (u32)((((fq >> 1) & 7) * 112 + (fq >> 4)) * 2 + (fq & 1)) * 1024u + lane * 16u + (w < 7 ? 4096u : 0x40000000u), uso = WS_U + (u32)oct * US + (u32)tid * 16u;
  TIE(wso); TIE(uso);
  if (w < 2) lds_st<v4u>(lane < 62 ? LU + w * LUS + lane * 16 : SP + w * TBS + lane * 16 - 992, v4u{0, 0, 0, 0});
  else if (tid < 144) lds_st<v4u>(SP + (lane >> 3) * TBS + KQ + (lane & 7) * 16, v4u{0, 0, 0, 0});
  asm volatile("s_waitcnt lgkmcnt(0)" : "+s"(ep), "+s"(sl), "+s"(sk_)::"memory");
  v2u tag;
  { u64 z = (ep + 0x9E3779B97F4A7C15ull) * 0xBF58476D1CE4E5B9ull; z ^= z >> 29; STIE(z); tag.x = (u32)z; tag.y = (u32)(z >> 32); }
  float dt;
  { const float xx = __uint_as_float(ch ? sl & 0xffff0000u : sl << 16), t = xx * 1.4426950216293335f, r = __builtin_fmaf(xx, 1.9259629911266175e-8f, __builtin_fmaf(xx, 1.4426950216293335f, -t));
    const float e = __builtin_amdgcn_exp2f(t); dt = rbf(__builtin_fmaf(e * r, 0.6931471805599453f, e)); TIE(dt); }
  float k00 = __uint_as_float((tid & 255) == 0 ? (ch ? sk_ & 0xffff0000u : sk_ << 16) : 0u); TIE(k00);
  SB();
  M0SET(w4k, 65536); DMA(wo, r2, 0, 2048);
  M0SET(w4k, 32768); DMA(vw0, rw, ws1, 2048); DMA(vw1, rw, ws1, 3072);
  VMW(7); TIE(pa); TIE(pb);
  v4u un; v2u sv;
#define SYNC_A() do { LGW0(); BAR(); const u32 ra = LXN + (tid & 1) * 16384 + (((tid >> 1) * 4) ^ ((tid & 1) * 64)); \
    un = v4u{lds_ld<u32>(ra), lds_ld<u32>(ra + 4096), lds_ld<u32>(ra + 8192), lds_ld<u32>(ra + 12288)}; sv = lds_ld<v2u>(LXN + 1024 + (lane & 7) * 8); SB(); } while (0)
#define ST_A() do { SB(); DMA(vx0, rx, xs1, 0); DMA(vx1, rx, xs1, 1024); GST("global_store_dwordx4", uso, un, ws, 0, "sc1"); \
    const u32 o_ = tid < 8 ? WS_U + 4096 + 8192 + (u32)oct * US + tid * 8u : 0x40000000u; asm volatile("buffer_store_dwordx2 %0, %1, %2, 0 offen sc1" ::"v"(sv), "v"(o_), "s"(rs) : "memory"); SB(); } while (0)

  const u32 tb = SP + (u32)ch * TBS;
  {
    const u32 dk = md * 64 + 16 * ((lane >> 1) & 3);
#define AF_MAKE() float arl = dt * bfhi(pa) * 1.4426950408889634f; arl = fminf(fmaxf(arl, -1e30f), 1e30f); float f = dt * bfhi(pb) * 0.15915494309189535f; f -= rintf(f); const f2 af = {arl, f}
    if (!tw) {
      MARK(KS);
      AF_MAKE();
      TIE(pc_); TIE(pd); TIE(pe); TIE(pg);
      const f2 z8 = cexp2(af * 8.f), bb = f2{bfhi(pc_), bfhi(pd)} * dt, cc = {bfhi(pe), bfhi(pg)};
      f2 z16, g0, g1, t_;
      cmul2(z16, z8, z8, g0, cc, bb); cmul2(g1, g0, z8, t_, g0, z8);
      const u32 d = LSS + ch * 16384 + dk;
#pragma unroll
      for (int c = 0; c < 4; c++) {
        v4u re, im;
#pragma unroll
        for (int i = 0; i < 4; i++) { f2 n0 = g0, n1 = g1; if (c < 3 || i < 3) cmul2(n0, g0, z16, n1, g1, z16); re[i] = pk2(g0.x, g1.x); im[i] = pk2(g0.y, g1.y); g0 = n0; g1 = n1; }
        lds_st<v4u>((d ^ (16 * (c & 1))) + (c >> 1) * 8192, re); lds_st<v4u>((d ^ (32 + 16 * (c & 1))) + (c >> 1) * 8192, im);
        if (c == 1) SYNC_A();
        if (c == 2) ST_A();
      }
    } else {
      MARK(KT);
      __builtin_amdgcn_s_setprio(2);
      TIE(xr0); TIE(xr1);
      float ss = 0.f, sq = 0.f, ss2 = 0.f, sq2 = 0.f;
#pragma unroll
      for (int e = 0; e < 4; e++) { const u32 v = xr0[e], v1 = xr1[e]; ss = dot2(v, 0x3f803f80u, ss); sq = dot2(v, v, sq); ss2 = dot2(v1, 0x3f803f80u, ss2); sq2 = dot2(v1, v1, sq2); }
      ss = sum32(ss + ss2); sq = sum32(sq + sq2);
      float mb, sd; ln_stats(ss, sq, mb, sd);
      lds_st<v2u>(LXN + 1024 + lr * 8, v2u{pk2(-mb, -mb), pk2(-mb, sd)});
      const float rstd = __builtin_amdgcn_rcpf(sd), nmr = -mb * rstd;
      v4u o0, o1;
#pragma unroll
      for (int e = 0; e < 4; e++) { const u32 v = xr0[e], v1 = xr1[e]; const f2 t = f2{bf2f(v), bfhi(v)} * rstd + nmr, t1 = f2{bf2f(v1), bfhi(v1)} * rstd + nmr; o0[e] = pk2(t.x, t.y); o1[e] = pk2(t1.x, t1.y); }
      lds_st<v4u>(LXN + (lr << 12) + ((l31 ^ (4 * ch)) << 4), o0); lds_st<v4u>(LXN + 512 + (lr << 12) + ((l31 ^ (4 * ch)) << 4), o1);
      SYNC_A();
      AF_MAKE();
      const f2 z1 = cexp2(af), y1 = cexp2(af * 256.f);
      f2 z2, y2, z3, z4, y3, y4, z5, z6, z7, a2, a4, a6, b4, c4, y5, y6, y7, y8;
      cmul2(z2, z1, z1, y2, y1, y1);
      ST_A();
      cmul2(z3, z2, z1, z4, z2, z2); cmul2(y3, y2, y1, y4, y2, y2); cmul2(z5, z4, z1, z6, z4, z2); cmul2(z7, z4, z3, a2, y1, z2);
      const u32 d = LTS + ch * 8192 + dk, d1 = (LT1 + dk) ^ (32 * ch);
      lds_st<v4u>(d, v4u{pk2(1.f, z1.x), pk2(z2.x, z3.x), pk2(z4.x, z5.x), pk2(z6.x, z7.x)});
      lds_st<v4u>(d ^ 32, v4u{pk2(0.f, -z1.y), pk2(-z2.y, -z3.y), pk2(-z4.y, -z5.y), pk2(-z6.y, -z7.y)});
      cmul2(a4, y1, z4, a6, y1, z6); cmul2(b4, y2, z4, c4, y3, z4); cmul2(y5, y4, y1, y6, y4, y2); cmul2(y7, y4, y3, y8, y4, y4);
      lds_st<v4u>(d ^ 16, v4u{pk2(y1.x, a2.x), pk2(a4.x, a6.x), pk2(y2.x, b4.x), pk2(y3.x, c4.x)});
      lds_st<v4u>(d ^ 48, v4u{pk2(-y1.y, -a2.y), pk2(-a4.y, -a6.y), pk2(-y2.y, -b4.y), pk2(-y3.y, -c4.y)});
      lds_st<v4u>(d1, v4u{pk2(y4.x, y5.x), pk2(y6.x, y7.x), pk2(y8.x, y8.x), 0u});
      lds_st<v4u>(d1 ^ 16, v4u{pk2(-y4.y, -y5.y), pk2(-y6.y, -y7.y), pk2(-y8.y, -y8.y), 0u});
      __builtin_amdgcn_s_setprio(0);
    }
  }
#define LT_DMA() do { const u32 d_ = LLT + min(w, 6) * 1024; asm volatile("s_mov_b32 m0, %2\n\ts_nop 0\n\tglobal_load_lds_dwordx4 %0, %1" ::"v"(lto), "s"(g_tabs.lt), "s"(d_) : "memory"); } while (0)
  VMW(6);
  { const v4u wt = lds_tr2(wra, wra + 512); asm volatile("buffer_store_dwordx4 %0, %1, %2, 0 offen sc1\n\ts_nop 1" ::"v"(wt), "v"(wso), "s"(rs) : "memory"); }
  LGW0(); BAR();
  u32 ta2, ta2h, afo, so, co, uo_, lto, ufs; v2u tagv = tag;
#define FLAG_ST(o) asm volatile("buffer_store_dwordx2 %0, %1, %2, 0 offen sc1" ::"v"(tagv), "v"(o), "s"(rs) : "memory")
    f16v acc[2];
    v4u xf[2][2], wf[2][2][2];
#define G1_RD(st, r_) do { constexpr int sl_ = ((st) >> 1) % 3, p_ = sl_ >> 1; constexpr u32 sb_ = (sl_ & 1) * 32768, wo_ = sb_ + ((st) & 1) * 16384 + (r_) * 8192; \
      xf[(st) & 1][r_] = lds_ld<v4u>(xa[p_][2 * ((st) & 1) + (r_)] + sb_); \
      wf[(st) & 1][0][r_] = lds_tr2(wa[p_][0] + wo_, wa[p_][0] + wo_ + 1024); wf[(st) & 1][1][r_] = lds_tr2(wa[p_][1] + wo_, wa[p_][1] + wo_ + 1024); } while (0)
#define G1_MM(st, n_, r_) acc[n_] = mfma32(wf[(st) & 1][n_][r_], xf[(st) & 1][r_], (st) + (r_) == 0 ? f16v{0} : acc[n_])
#define G1_STEP(st, X2, X3) do { \
      G1_MM(st, 0, 0); if ((st) < 15) G1_RD((st) + 1, 0); SG(0x8, 1); SG(0x100, 5); SB(); \
      G1_MM(st, 1, 0); if ((st) < 15) G1_RD((st) + 1, 1); SG(0x8, 1); SG(0x100, 5); SB(); \
      G1_MM(st, 0, 1); SB(); X2; SB(); \
      G1_MM(st, 1, 1); SB(); X3; SB(); } while (0)
#define G1_SYNC(NV) do { VMW(NV); LGW0(); BAR(); SB(); } while (0)
  v4u kA[2], kB[2]; f4 kk[2]; v2u wa_;
#define K_RD(k) do { kA[(k) & 1] = lds_tr2(kta + (k) * 1024, kta + (k) * 1024 + 512); kB[(k) & 1] = lds_tr2(ksa + (k) * 1024, ksa + (k) * 1024 + 512); } while (0)
#define K_MM(k) do { kk[(k) & 1] = mfma(kA[(k) & 1], kB[(k) & 1], (k) == 0 ? f4{k00, 0, 0, 0} : (k) == 1 ? f4{0, 0, 0, 0} : kk[(k) & 1]); TIE(kk[(k) & 1]); } while (0)
#define K_PUT() do { const f4 k_ = kk[0] + kk[1]; const u32 p0 = pk2(k_[0], k_[1]), p1 = pk2(k_[2], k_[3]);  { \
      lds_st<u16>(tb + (wa_.x & 0xffffu), (u16)p0); lds_st<u16>(tb + (wa_.x >> 16), (u16)(p0 >> 16)); \
      lds_st<u16>(tb + (wa_.y & 0xffffu), (u16)p1); lds_st<u16>(tb + (wa_.y >> 16), (u16)(p1 >> 16)); } } while (0)
#define SET_A() do { _Pragma("unroll") for (int r = 0; r < 4; r++) { xa[1][r] = xa[0][r] + 65536; TIE(xa[1][r]); } wa[1][0] = wa[0][0] + 65536; wa[1][1] = wa[0][1] + 65536; TIE(wa[1][0]); TIE(wa[1][1]); } while (0)
#define SET_B() do { ufs = tid == 448 ? WS_UF + 4096 + (u32)oct * 128u : 0x40000000u; TIE(ufs); TIE(tagv); } while (0)
#define SET_C() do { ta2 = w4k + lane * 16; ta2h = ta2 + 65536; TIE(ta2); TIE(ta2h); uo_ = WS_U + (u32)(tid >> 1) * US + (u32)oct * 32u + (tid & 1) * 16u; TIE(uo_); } while (0)
#define SET_D() do { afo = WS_UF + (u32)(hh ? rt * 16 + mw * 4 + (lane & 3) : 32 * w + lane) * 128u; const u32 t_ = WS_CF + (u32)(2 * ft + nh) * 128u; afo = lane >= 48 ? t_ : afo; TIE(afo); } while (0)
#define SET_F() do { lto = (u32)min(tid, 383) * 16u; TIE(lto); wa_ = lds_ld<v2u>(LWR + (tid & 255) * 8); } while (0)
#define SET_E() do { so = WS_U + 8192 + (u32)(rt * 16 + mw * 4 + (l31 >> 3)) * US + (lane & 7) * 8u; co = WS_C + (u32)(ft * 128 + nh * 64 + l31) * 8u; TIE(so); TIE(co); } while (0)
  const u32 seq = (u32)(0x65432106543210ull >> (4 * ((nt & 1) * 3)));
#define FR(f) ((seq >> (4 * (f))) & 7u)
  const u32 ho = (u32)lane * 16u, w2s = WS_W + 4096 + (u32)(nt * 112 + 14 * w) * 2048u;
#define DMA_2I(f, i) asm volatile("buffer_load_dwordx4 %0, %1, %2 offen offset:%3 sc1 lds" ::"v"(ho), "s"(rs), "s"(w2s + (FR(f) << 12)), "n"((i) * 1024) : "memory")
#define SL2(f) ((0x1023000 >> (4 * (f))) & 3)
#define M0_2(f) M0SET(w4k, SL2(f) * 32768)
#define DMA_2(f) do { M0_2(f); DMA_2I(f, 0); DMA_2I(f, 1); DMA_2I(f, 2); DMA_2I(f, 3); } while (0)
  v4u wf2[3][2][2];
#define W2FRAG(f, s2, nl) lds_ld<v4u>((SL2(f) & 2 ? ta2h : ta2) + (nl) * 1024 + (SL2(f) & 1) * 32768 + (s2) * 2048)
  const u32 hx = ho;
#define W2_LD(f, s_, n_) asm volatile("buffer_load_dwordx4 %0, %1, %2, %3 offen offset:%4 sc1" : "=v"(wf2[f][s_][n_]) : "v"(hx), "s"(rs), "s"(w2s + (FR(f) << 12)), "n"((s_) * 2048 + (n_) * 1024) : "memory")
#define W2_TIE2() do { _Pragma("unroll") for (int f_ = 0; f_ < 2; f_++) _Pragma("unroll") for (int s_ = 0; s_ < 2; s_++) _Pragma("unroll") for (int n_ = 0; n_ < 2; n_++) TIE(wf2[f_][s_][n_]); } while (0)
#define W2_TIE() do { _Pragma("unroll") for (int f_ = 0; f_ < 3; f_++) _Pragma("unroll") for (int s_ = 0; s_ < 2; s_++) _Pragma("unroll") for (int n_ = 0; n_ < 2; n_++) TIE(wf2[f_][s_][n_]); } while (0)
  const int th = w & 1, rh = (w >> 1) & 1;
  CvS cs_; v4u sx, t7, t9; u32 ad;
#define LT_READ() do { cs_.s01 = lds_ld<v4u>(LLT + ho); cs_.s2 = lds_ld<v4u>(LLT + 1024 + ho); sx = lds_ld<v4u>(LLT + 2048 + ho); \
    t7 = lds_ld<v4u>(LLT + 3072 + ho); t9 = lds_ld<v4u>(LLT + 4096 + ho); ad = lds_ld<u32>(LLT + 5120 + ho); } while (0)
  CvA ca_; ConvRaw c0, c1; v4u x1, x2, x4, b0, y1_, y2_; u32 pe_[5], pf_[3], pq_[2];
#define CV_ADDR() do { ca_.ua = LU + (u32)ch * LUS + 448 - 192 - 256 * rh + 512 * th + 32 * l15 + 16 * g; \
    ca_.a0 = tb + (cs_.s01.w & 0xffffu) + 256 * rh; ca_.a1 = tb + (cs_.s01.w >> 16) + 128 * rh; ca_.a2 = tb + (ad & 0xffffu) + 64 * rh; } while (0)
#define CV_PRE() do { const u32 p1_ = rh ? ca_.a2 : ca_.a0 + 512, p2_ = ca_.a1 + (rh ? 0 : 256), p3_ = tb + (ad >> 16), p4_ = tb + KQ + 128 - 12 + 64 * (th - rh) + 4 * l15 - 16 * g; \
    _Pragma("unroll") for (int i = 0; i < 5; i++) { pe_[i] = lds_ld<u32>(p1_ + 4 * i); c0.e[i] = lds_ld<u32>(ca_.a0 + 4 * i); } \
    _Pragma("unroll") for (int i = 0; i < 3; i++) pf_[i] = lds_ld<u32>(p2_ + 4 * i); pq_[0] = lds_ld<u32>(p3_); pq_[1] = lds_ld<u32>(p3_ + 4); \
    _Pragma("unroll") for (int i = 0; i < 4; i++) { y1_[i] = lds_ld<u32>(p4_ + 12 - 4 * i); y2_[i] = lds_ld<u32>(p4_ + 128 + 12 - 4 * i); } \
    conv_load<0, 3>(c0, ca_); \
    b0 = frag0(c0.e, cs_.s01.x); \
    if (rh) { x1 = frag1(pf_, cs_.s01.y, cs_.s01.z); x2 = perm4(pe_[1], pe_[0], cs_.s2); x4 = v4u{0, 0, 0, 0}; } \
    else { x1 = frag0(pe_, cs_.s01.x); x2 = frag1(pf_, cs_.s01.y, cs_.s01.z); x4 = perm4(pq_[1], pq_[0], sx); } TIE(b0); TIE(x1); TIE(x2); TIE(x4); TIE(y1_); TIE(y2_); TIE(t7); TIE(t9); } while (0)
#define U_PUT() do { lds_st<v2u>(LU + 992 + tid * 8, v2u{prm(uu.y, uu.x, 0x05040100u), prm(uu.w, uu.z, 0x05040100u)}); \
    lds_st<v2u>(LU + LUS + 992 + tid * 8, v2u{prm(uu.y, uu.x, 0x07060302u), prm(uu.w, uu.z, 0x07060302u)}); } while (0)
  v4u uu;

  if (gw) {
    MARK(GW);
    v2u fl;
    v2u st2, cq0, cq1; u32 fx, fv;
#define LD_AF() GLD("global_load_dwordx2", fl, afo, ws, 0, "sc1")
#define LD_U() GLD("global_load_dwordx4", uu, uo_, ws, 0, "sc1")
#define LD_ST() GLD("global_load_dwordx2", st2, so, ws, 0, "sc1")
#define LD_C0() GLD("global_load_dwordx2", cq0, co, ws, 0, "sc1")
#define LD_C1() GLD("global_load_dwordx2", cq1, co, ws, 256, "sc1")
    G1_RD(0, 0); G1_RD(0, 1); SB();
    M0SET(w4k, 65536); DMA_X(2);
    G1_STEP(0, { DW(2, 0); K_RD(0); }, DW(2, 1));
    G1_SYNC(7  );
    G1_STEP(1, { M0SET(w4k, 0); DX(3, 0); K_MM(0); K_RD(1); }, { DX(3, 1); SET_B(); });
    G1_STEP(2, { DW(3, 0); K_MM(1); K_RD(2); }, { DW(3, 1); SET_A(); });
    G1_SYNC(4  );
    G1_STEP(3, { FLAG_ST(ufs); M0SET(w4k, 32768); DX(4, 0); K_MM(2); K_RD(3); }, DX(4, 1));
    G1_STEP(4, { DW(4, 0); K_MM(3); K_RD(4); }, DW(4, 1));
    G1_SYNC(5  );
    G1_STEP(5, { M0SET(w4k, 65536); DX(5, 0); K_MM(4); K_RD(5); }, DX(5, 1));
    G1_STEP(6, { DW(5, 0); K_MM(5); K_RD(6); }, { DW(5, 1); SET_D(); });
    G1_SYNC(4  );
    G1_STEP(7, { M0SET(w4k, 0); DX(6, 0); K_MM(6); K_RD(7); }, { DX(6, 1); SET_F(); });
    G1_STEP(8, { DW(6, 0); K_MM(7); }, DW(6, 1));
    G1_SYNC(4  );
    G1_STEP(9, { M0SET(w4k, 32768); DMA_X(7); SET_E(); }, DMA_W(7));
    G1_STEP(10, { LD_AF(); LT_DMA(); }, SET_C());
    G1_SYNC(6  );
    G1_STEP(11, K_PUT(), { VMW(1  ); TIE(fl); if (tag_miss(fl, tag)) { MARK(SLOW); poll(afo, ws, tag); } M0_2(3); DMA_2I(3, 0); });
    G1_STEP(12, { LD_U(); DMA_2I(3, 1); }, { LD_ST(); LD_C0(); LD_C1(); DMA_2I(3, 2); });
    G1_SYNC(7  );
    G1_STEP(13, { DMA_2I(3, 3); M0_2(4); fx = lds_ld<u32>(LLT + 5120 + lane * 16 + 4); }, DMA_2I(4, 0));
    G1_STEP(14, { DMA_2I(4, 1); fv = lds_ld<u16>(tb + (fx & 0xffffu)); }, DMA_2I(4, 2));
    G1_STEP(15, { DMA_2I(4, 3); W2_LD(0, 0, 0); lds_st<u16>(tb + (fx >> 16), (u16)fv); }, { W2_LD(0, 0, 1); W2_LD(0, 1, 0); });
    W2_LD(0, 1, 1);
    VMW(10  ); TIE(st2); TIE(cq0); TIE(cq1); TIE(uu);
    __builtin_amdgcn_s_setprio(2);
    const float sd = bfhi(st2.y);
    { const u32 km = hh ? 0u : ~0u; const v4u sb = {st2.x & km, st2.y & km, 0u, 0u};
      acc[0] = mfma32(v4u{cq0.x, cq0.y, 0u, 0u}, sb, acc[0]); acc[1] = mfma32(v4u{cq1.x, cq1.y, 0u, 0u}, sb, acc[1]); }
    const float rstd = __builtin_amdgcn_rcpf(sd), k1 = -2.3022082f * rstd, k2 = -0.10294324f * rstd * rstd * rstd;
    const int M32 = rt * 4 + mw;
    const char* hb = ws + WS_H + (size_t)(M32 * 112 + ft * 8 + nh * 4) * 1024;
#define GELU_Q(nl, s) do { v4u hp; _Pragma("unroll") for (int e = 0; e < 4; e++) { \
      const f2 v = {acc[nl][8 * (s) + 2 * e], acc[nl][8 * (s) + 2 * e + 1]};        \
      const f2 z = v * (v * v * k2 + k1); \
      const f2 d = f2{__builtin_amdgcn_exp2f(z.x), __builtin_amdgcn_exp2f(z.y)} * sd + sd; \
      const f2 o = v * f2{__builtin_amdgcn_rcpf(d.x), __builtin_amdgcn_rcpf(d.y)}; \
      hp[e] = pk2(o.x, o.y); } \
    GST("global_store_dwordx4", ho, hp, hb, (nl) * 2048 + (s) * 1024, "sc1"); } while (0)
    GELU_Q(0, 0); W2_LD(1, 0, 0); W2_LD(1, 0, 1);
    GELU_Q(0, 1); W2_LD(1, 1, 0); W2_LD(1, 1, 1);
    GELU_Q(1, 0);
    GELU_Q(1, 1);
    __builtin_amdgcn_s_setprio(0);
    U_PUT();
    u32 hfs = lane == 0 ? WS_HF + 4096 + (u32)(M32 * 32 + ft * 2 + nh) * 128u : 0x40000000u; TIE(hfs);
    LT_READ(); CV_ADDR(); CV_PRE();
    VMW(0  ); W2_TIE2();
    FLAG_ST(hfs);
    LGW0(); BAR();
  } else {
    MARK(LN);
    SET_B(); SET_C();
    u32 pb1; { const u32 o3 = (u32)(cg * 64 + lane) * 2u; GLD("global_load_ushort", pb1, o3, a.b1, 0, ""); }
    VMW(4);
    { v4u se; f4 ca = {0, 0, 0, 0};
#pragma unroll
      for (int i = 0; i < 4; i++) se[i] = (l15 >> 1) == i ? (0x3f80u << (16 * (l15 & 1))) : 0u;
#pragma unroll
      for (int i = 0; i < 8; i++) ca = mfma(se, lds_ld<v4u>(w4k + ho + (i >> 2) * 32768 + (i & 3) * 1024), ca);
      if (lane < 32) lds_st<f4>(w4k + (l15 >> 3) * 256 + (lane & 7) * 32 + g * 16, ca); }
    LGW0(); BAR();
    if (w == 0) {
      float c = 0.f;
#pragma unroll
      for (int i = 0; i < 16; i++) c += lds_ld<float>((i >> 1) * 4096 + (i & 1) * 256 + lane * 4);
      const float c0 = rbf(c), c1 = rbf(c - c0), c2 = c - c0 - c1;
      VMW(0); TIE(pb1);
      const u32 o = WS_C + (u32)(cg * 64 + lane) * 8u; const v2u v = {pk2(c0, c1), pk2(c2, bf2f(pb1))}; GST("global_store_dwordx2", o, v, ws, 0, "sc1");
    } else VMW(0);
    BAR();
    FLAG_ST(ufs);
    if (w == 0) { VMW(0); if (lane == 0) { const u32 o_ = WS_CF + (u32)cg * 128u; GST("global_store_dwordx2", o_, tag, ws, 0, "sc1"); } }
    SET_F();
#pragma unroll
    for (int k = 0; k < 8; k++) { K_RD(k); K_MM(k); }
    BAR();
    K_PUT();
    LT_DMA();
    __builtin_amdgcn_s_sleep(24);
    poll(WS_UF + (u32)(32 * w + l31) * 128u, ws, tag);
    LGW0(); BAR();
    { const u32 o_ = (bid == 255 && tid == 0) ? 4096u : 0x40000000u; FLAG_ST(o_); }
    GLD("global_load_dwordx4", uu, uo_, ws, 0, "sc1");
    DMA_2(3); DMA_2(4);
#pragma unroll
    for (int i = 0; i < 8; i++) W2_LD(i >> 2, (i >> 1) & 1, i & 1);
    { const u32 fx = lds_ld<u32>(LLT + 5120 + lane * 16 + 4); lds_st<u16>(tb + (fx >> 16), lds_ld<u16>(tb + (fx & 0xffffu))); }
    LT_READ(); CV_ADDR();
    VMW(0); TIE(uu); W2_TIE2(); U_PUT();
    LGW0(); BAR();
    CV_PRE();
  }
  v4u hf[14][2];
  const u32 hv0 = (u32)((2 * mt) * 112 + 14 * w) * 1024u + ho + 4096u + WS_H, hv1 = (u32)((2 * mt + 1) * 112 + 14 * w) * 1024u + ho + 4096u + WS_H;
#define H_LD(s, ml) asm volatile("buffer_load_dwordx4 %0, %1, %2, %3 offen offset:%4 sc1" : "=v"(hf[s][ml]) : "v"((ml) ? hv1 : hv0), "s"(rs), "s"(FR((s) >> 1) << 11), "n"(((s) & 1) * 1024) : "memory")
  u32 yfo;
  {
    f4 ca[4];
    v4u za, zb;
    { const u32 p = LU + (u32)ch * LUS + 992 - 48 + 512 * rh + 32 * l15 + 16 * g;
      const v4u u0 = lds_ld<v4u>(p + 16), u1 = lds_ld<v4u>(p), u2 = lds_ld<v4u>(p + 1024 + 16), u3 = lds_ld<v4u>(p + 1024);
      conv_load<0, 0>(c0, ca_); conv_load<0, 1>(c0, ca_); conv_load<0, 2>(c0, ca_); SB();
      W2_LD(2, 0, 0); W2_LD(2, 0, 1);
      const f4 z0 = mfma(u0, t7, f4{0, 0, 0, 0}), z1 = mfma(u1, t9, f4{0, 0, 0, 0}), z2 = mfma(u2, t7, f4{0, 0, 0, 0}), z3 = mfma(u3, t9, f4{0, 0, 0, 0});
      SB();
      CV_ROUND(0, c0, c1, { W2_LD(2, 1, 0); W2_LD(2, 1, 1); }, { M0_2(5); DMA_2I(5, 0); _Pragma("unroll") for (int i = 0; i < 4; i++) { za[i] = pk2(z0[i], z1[i]); zb[i] = pk2(z2[i], z3[i]); } TIE(za); TIE(zb); },
               { DMA_2I(5, 1); }, { DMA_2I(5, 2); }); }
    v2u fl; const u32 hpo = lane < 8 ? WS_HF + 4096 + (u32)((2 * mt + ((lane >> 2) & 1)) * 32 + ((7 * w) >> 1) + (lane & 3)) * 128u : 0x40000000u;
    CV_ROUND(1, c1, c0, { DMA_2I(5, 3); M0_2(6); }, {  }, { ALD(fl, hpo, rs); }, { DMA_2I(6, 0); });
    CV_ROUND(2, c0, c1, { DMA_2I(6, 1); }, { DMA_2I(6, 2); }, { DMA_2I(6, 3); }, { VMW(4  ); W2_TIE(); TIE(fl); if (tag_miss_m(fl, tag, 0xff)) { MARK(SLOW); apoll(hpo, rs, tag, 0xff); } H_LD(0, 0); H_LD(0, 1); });
    CV_ROUND(3, c1, c0, { H_LD(1, 0); H_LD(1, 1); }, { H_LD(2, 0); H_LD(2, 1); }, { H_LD(3, 0); H_LD(3, 1); }, { H_LD(4, 0); H_LD(4, 1); });
    ca[2] = mfma(za, y1_, ca[2]); ca[3] = mfma(za, y2_, ca[3]); ca[3] = mfma(zb, y1_, ca[3]);
    f4 k0, k1;
    const u32 ew = (w < 6 ? LEX + w * 2048 : LEX2 + (w - 6) * 2048) + ho, er = ((w ^ 2) < 6 ? LEX + (w ^ 2) * 2048 : LEX2 + ((w ^ 2) - 6) * 2048) + ho;
    if (rh) { lds_st<f4>(ew, ca[0]); lds_st<f4>(ew + 1024, ca[1]); k0 = ca[2]; k1 = ca[3]; }
    else { lds_st<f4>(ew, ca[2]); lds_st<f4>(ew + 1024, ca[3]); k0 = ca[0]; k1 = ca[1]; }
    LGW0(); BAR();
    k0 += lds_ld<f4>(er); k1 += lds_ld<f4>(er + 1024);
    const auto r0 = __builtin_amdgcn_permlane16_swap(pk2(k0[0], k0[1]), pk2(k1[0], k1[1]), false, false), r1 = __builtin_amdgcn_permlane16_swap(pk2(k0[2], k0[3]), pk2(k1[2], k1[3]), false, false);
    const u32 o = WS_Y + (u32)dch * YS + (4 * rh + th + 2 * (g & 1)) * 512 + (l15 >> 2) * 128 + hh * 64 + (lane & 3) * 16;
    { const v4u v = {r0[0], r1[0], r0[1], r1[1]}; GST("global_store_dwordx4", o, v, ws, 0, "sc1"); }
    yfo = lane == 0 ? WS_YF + 4096 + (u32)((w & 3) * 512 + dch) * 128u : 0x40000000u;
  }
  {
    f16v c2[2][2];
    v4u wn[2][2], xr, b2v; v2u fl; u32 pa_[2][4]; f4 e0, e1;
    const u32 ypo = lane >= 8 ? 0x40000000u : WS_YF + 4096 + (u32)((((mt >> 3) & 2) + ((mt >> 2) & 1)) * 512 + nt * 64 + w * 8 + (lane & 7)) * 128u;
    const u32 oo = (u32)(mt * 64 + (tid >> 3)) * 1024u + nt * 128 + (tid & 7) * 16;
#define W2OP(s, nl) ((s) < 6 ? wf2[(s) < 6 ? (s) >> 1 : 0][(s) & 1][nl] : wn[(s) & 1][nl])
#define G2_STEP(s, NV, ...) do { VMW(NV); TIE(hf[s][0]); TIE(hf[s][1]); \
      c2[0][0] = mfma32(W2OP(s, 0), hf[s][0], (s) == 0 ? f16v{0} : c2[0][0]); TIE(c2[0][0]); SB(); if ((s) + HWIN < 14) H_LD((s) + HWIN, 0); SB(); \
      c2[1][0] = mfma32(W2OP(s, 1), hf[s][0], (s) == 0 ? f16v{0} : c2[1][0]); TIE(c2[1][0]); \
      if ((s) >= 5 && (s) < 13) { wn[((s) + 1) & 1][0] = W2FRAG(((s) + 1) >> 1, ((s) + 1) & 1, 0); wn[((s) + 1) & 1][1] = W2FRAG(((s) + 1) >> 1, ((s) + 1) & 1, 1); } SG(0x8, 1); SB(); \
      c2[0][1] = mfma32(W2OP(s, 0), hf[s][1], (s) == 0 ? f16v{0} : c2[0][1]); TIE(c2[0][1]); SB(); if ((s) + HWIN < 14) H_LD((s) + HWIN, 1); SB(); \
      c2[1][1] = mfma32(W2OP(s, 1), hf[s][1], (s) == 0 ? f16v{0} : c2[1][1]); TIE(c2[1][1]); SB(); __VA_ARGS__; SB(); } while (0)
    if (w >= 4) __builtin_amdgcn_s_setprio(1);
    G2_STEP(0, 9  , {});
    G2_STEP(1, 9  , {});
    G2_STEP(2, 9  , {});
    G2_STEP(3, 9  , { VMW(8  ); FLAG_ST(yfo); __builtin_amdgcn_s_setprio(0); });
    G2_STEP(4, 10  , {});
    G2_STEP(5, 9  , { GLD("global_load_dwordx4", xr, oo, a.x, 0, ""); const u32 o2 = nt * 128 + (tid & 7) * 16; GLD("global_load_dwordx4", b2v, o2, a.b2, 0, ""); });
    G2_STEP(6, 11  , {});
    G2_STEP(7, 11  , {});
    G2_STEP(8, 11  , { ALD(fl, ypo, rs); });
    G2_STEP(9, 11  , {});
    G2_STEP(10, 7  , { TIE(xr); TIE(b2v); e0 = f4{bf2f(b2v.x) + bf2f(xr.x), bfhi(b2v.x) + bfhi(xr.x), bf2f(b2v.y) + bf2f(xr.y), bfhi(b2v.y) + bfhi(xr.y)}; TIE(e0); });
    G2_STEP(11, 5  , { e1 = f4{bf2f(b2v.z) + bf2f(xr.z), bfhi(b2v.z) + bfhi(xr.z), bf2f(b2v.w) + bf2f(xr.w), bfhi(b2v.w) + bfhi(xr.w)}; TIE(e1); });
    G2_STEP(12, 3  , { _Pragma("unroll") for (int qd = 0; qd < 4; qd++) { pa_[0][qd] = (l31 >> 4) * 32768 + w4k + l15 * 256 + 16 * ((2 * qd + hh) ^ (lane & 7)); pa_[1][qd] = pa_[0][qd] + 65536; TIE(pa_[0][qd]); TIE(pa_[1][qd]); }
      VMW(0  ); TIE(fl); if (tag_miss_m(fl, tag, 0xff)) { MARK(SLOW); apoll(ypo, rs, tag, 0xff); }
      const u32 o = WS_Y + 4096 + (u32)(nt * 64 + w * 8 + (lane >> 3)) * YS + (u32)mt * 128u + (lane & 7) * 16u, d = SP + w * 1040;
      asm volatile("s_mov_b32 m0, %2\n\ts_nop 0\n\tbuffer_load_dwordx4 %0, %1, 0 offen sc1 lds" ::"v"(o), "s"(rs), "s"(d) : "memory"); });
    G2_STEP(13, 2  , {});
#define P_PUT(nl, ml) do { _Pragma("unroll") for (int qd = 0; qd < 4; qd++) \
      lds_st<f4>(pa_[ml][qd] + (nl) * 128, f4{c2[nl][ml][4 * qd], c2[nl][ml][4 * qd + 1], c2[nl][ml][4 * qd + 2], c2[nl][ml][4 * qd + 3]}); } while (0)
    P_PUT(0, 0); P_PUT(1, 0); P_PUT(0, 1); P_PUT(1, 1);
    const int row = tid >> 3, c8 = tid & 7;
    u32 ra = (row >> 4) * 32768 + (row & 15) * 256 + 16 * ((2 * c8) ^ (row & 7)), rb = ra ^ 16u, ya = SP + c8 * 1040 + ((row >> 3) & 1) * 64 + (row >> 4) * 16 + (row & 7) * 2;
    TIE(ra); TIE(rb); TIE(ya);
    VMW(0); LGW0(); BAR();
    f4 p0[8], p1[8]; u32 yv[8];
#pragma unroll
    for (int ww = 0; ww < 8; ww++) { p0[ww] = lds_ld<f4>(ra + ww * 4096); p1[ww] = lds_ld<f4>(rb + ww * 4096); }
#pragma unroll
    for (int i = 0; i < 8; i++) yv[i] = lds_ld<u16>(ya + i * 128);
    SB();
    e0 += ((p0[0] + p0[1]) + (p0[2] + p0[3])) + ((p0[4] + p0[5]) + (p0[6] + p0[7])); e1 += ((p1[0] + p1[1]) + (p1[2] + p1[3])) + ((p1[4] + p1[5]) + (p1[6] + p1[7]));
    e0 += f4{bf2f(yv[0]), bf2f(yv[1]), bf2f(yv[2]), bf2f(yv[3])}; e1 += f4{bf2f(yv[4]), bf2f(yv[5]), bf2f(yv[6]), bf2f(yv[7])};
    const v4u o = {pk2(e0[0], e0[1]), pk2(e0[2], e0[3]), pk2(e1[0], e1[1]), pk2(e1[2], e1[3])};
    GST("global_store_dwordx4", oo, o, a.out, 0, "");
  }
  asm volatile(".Lfk_end:");
}

extern "C" void kernel_launch(void* const* d_in, const int* in_sizes, int n_in, void* d_out, int out_size, void* d_ws, size_t ws_size, hipStream_t stream) {
  Args a;
  a.x = (const u16*)d_in[0]; a.are = (const u16*)d_in[1]; a.aim = (const u16*)d_in[2]; a.bre = (const u16*)d_in[3]; a.bim = (const u16*)d_in[4];
  a.cre = (const u16*)d_in[5]; a.cim = (const u16*)d_in[6]; a.ldt = (const u16*)d_in[7]; a.dsk = (const u16*)d_in[8];
  a.w1 = (const u16*)d_in[10]; a.b1 = (const u16*)d_in[11]; a.w2 = (const u16*)d_in[12]; a.b2 = (const u16*)d_in[13];
  a.out = (u16*)d_out; a.ws = (char*)d_ws; a.pad = a.x;
  (void)hipFuncSetAttribute((const void*)fused_kernel, hipFuncAttributeMaxDynamicSharedMemorySize, LDS_BYTES);
  fused_kernel<<<256, 512, LDS_BYTES, stream>>>(a);
}
```
